```python
import torch

def _softmax(x, axis=-1):
    return torch.softmax(x, dim=axis)

def _gelu(x):
    return torch.nn.functional.gelu(x, approximate='tanh')

def _silu(x):
    return torch.nn.functional.silu(x)

def _sigmoid(x):
    return torch.sigmoid(x)

def _layernorm(x, axis=-1, eps=1e-05):
    m = x.mean(dim=axis, keepdim=True)
    v = x.var(dim=axis, keepdim=True, unbiased=False)
    return (x - m) / torch.sqrt(v + eps)

def _scatter_add(tgt, idx, src, axis=0):
    return tgt.clone().index_add_(axis, idx, src)

def _segment_sum(src, seg, n):
    out = torch.zeros((n,) + src.shape[1:], dtype=src.dtype)
    return out.index_add_(0, seg, src)

def _pad(x, pw):
    flat = tuple((v for pair in reversed(pw) for v in pair))
    return torch.nn.functional.pad(x, flat)
kernel_name = 'recurrent_scan__memory_aug_recurrent_mem_tr__dense_activation_squared_relu__prompt'

def setup_inputs():

    _w = lambda a: (lambda t: t.to(torch.bfloat16) if t.is_floating_point() else t)(torch.as_tensor(a))

    def _A_setup():
        B, S, Dm = (1, 2048, 640)
        H, Dh = (32, 192)
        M, seg = (8, 32)
        seg = min(seg, S)
        S = S // seg * seg
        return {'x': _w((lambda *s: torch.randn(*s, dtype=torch.bfloat16) * 0.05)(B, S, Dm)), 'mem0': _w((lambda *s: torch.randn(*s, dtype=torch.bfloat16) * 0.05)(B, M, Dm)), 'Wq': _w((lambda *s: torch.randn(*s, dtype=torch.bfloat16) * 1.6)(Dm, H, Dh) * Dm ** (-0.5)), 'Wk': _w((lambda *s: torch.randn(*s, dtype=torch.bfloat16) * 1.6)(Dm, H, Dh) * Dm ** (-0.5)), 'Wv': _w((lambda *s: torch.randn(*s, dtype=torch.bfloat16) * 0.05)(Dm, H, Dh) * Dm ** (-0.5)), 'Wo': _w((lambda *s: torch.randn(*s, dtype=torch.bfloat16) * 0.05)(H, Dh, Dm) * (H * Dh) ** (-0.5)), 'seg_len': seg}
    _A_in = _A_setup()

    def _B_setup():
        B, S, D, F = (1, 2048, 640, 1536)
        return {'x': _w((lambda *s: torch.randn(*s, dtype=torch.bfloat16) * 0.05)(B, S, D)), 'w1': _w((lambda *s: torch.randn(*s, dtype=torch.bfloat16) * 0.05)(D, F)), 'b1': _w((lambda *s: torch.randn(*s, dtype=torch.bfloat16) * 0.4)(F)), 'w2': _w((lambda *s: torch.randn(*s, dtype=torch.bfloat16) * 0.05)(F, D)), 'b2': _w((lambda *s: torch.randn(*s, dtype=torch.bfloat16) * 1.6)(D))}
    _B_in = _B_setup()
    return {'A_x': _A_in['x'], 'A_mem0': _A_in['mem0'], 'A_Wq': _A_in['Wq'], 'A_Wk': _A_in['Wk'], 'A_Wv': _A_in['Wv'], 'A_Wo': _A_in['Wo'], 'A_seg_len': _A_in['seg_len'], 'B_x': _B_in['x'], 'B_w1': _B_in['w1'], 'B_b1': _B_in['b1'], 'B_w2': _B_in['w2'], 'B_b2': _B_in['b2']}

def reference(A_x, A_mem0, A_Wq, A_Wk, A_Wv, A_Wo, A_seg_len, B_x, B_w1, B_b1, B_w2, B_b2):

    def _A_ref(x, mem0, Wq, Wk, Wv, Wo, seg_len):
        B, S, Dm = x.shape
        H, Dh = (Wq.shape[1], Wq.shape[2])
        M = mem0.shape[1]
        scale = Dh ** (-0.5)
        n_seg = S // seg_len
        L = M + seg_len
        idx = torch.arange(L)
        mask = (idx[:, None] < M) | (idx[:, None] >= idx[None, :])
        mem = mem0
        outs = []
        for s in range(n_seg):
            xs = x[:, s * seg_len:(s + 1) * seg_len, :].clone()
            h = torch.concatenate([mem, xs], axis=1)
            q = torch.einsum('btd,dhe->bhte', h, Wq)
            k = torch.einsum('btd,dhe->bhte', h, Wk)
            v = torch.einsum('btd,dhe->bhte', h, Wv)
            scores = torch.einsum('bhqe,bhke->bhqk', q, k) * scale
            scores = torch.where(mask[None, None], scores, -1000000000.0)
            p = _softmax(scores, axis=-1)
            o = torch.einsum('bhqk,bhke->bhqe', p, v)
            o = torch.einsum('bhte,hed->btd', o, Wo)
            mem = o[:, :M, :].clone()
            outs.append(o[:, M:, :])
        return torch.concatenate(outs, axis=1)

    def _B_ref(x, w1, b1, w2, b2):
        h = torch.einsum('bsd,df->bsf', x, w1) + b1
        zero = torch.zeros_like(h)
        r = torch.where(h > zero, h, zero)
        h = r * r
        out = torch.einsum('bsf,fd->bsd', h, w2) + b2
        return out
    x = A_x
    xn = _layernorm(x, axis=-1)
    s = _A_ref(xn, A_mem0, A_Wq, A_Wk, A_Wv, A_Wo, A_seg_len)
    g = _silu(_B_ref(xn, B_w1, B_b1, B_w2, B_b2))
    return x + s * g
```

```cpp
#include <hip/hip_runtime.h>
#include <stdint.h>

#define DI __device__ __forceinline__
#define LAS __attribute__((address_space(3)))
#define GAS __attribute__((address_space(1)))
#define RLX_AGENT __ATOMIC_RELAXED, __HIP_MEMORY_SCOPE_AGENT
typedef __bf16 bf16x2 __attribute__((ext_vector_type(2)));
typedef __bf16 bf16x4 __attribute__((ext_vector_type(4)));
typedef __bf16 bf16x8 __attribute__((ext_vector_type(8)));
typedef __bf16 bf16x16 __attribute__((ext_vector_type(16)));
typedef short s16x4 __attribute__((ext_vector_type(4)));
typedef float f32x2 __attribute__((ext_vector_type(2)));
typedef float f32x4 __attribute__((ext_vector_type(4)));
typedef unsigned u32x2 __attribute__((ext_vector_type(2)));
typedef unsigned u32x4 __attribute__((ext_vector_type(4)));
typedef unsigned u32x8 __attribute__((ext_vector_type(8)));
typedef __amdgpu_buffer_rsrc_t rsrc_t;
typedef GAS unsigned gu32;

constexpr int SEQ = 2048, DM = 640, NH = 32, DH = 192, NM = 8, SEG = 32, NS = 64, FF = 1536;
constexpr int HD = NH * DH;
constexpr unsigned SENT = 0xFFFFFFFFu;
constexpr unsigned OOB = 0x7FFFFF00u;
constexpr int SC1 = 16;
constexpr int SPIN_CAP = 1 << 20;
#define DEFER_TICKS 400
#define PACE 60
#define HROW 0
#define HWO 0
#define PROBE_N 2
#define PAUSE_K 100
#define PAUSE_H 60

constexpr size_t MiB = 1u << 20;
constexpr size_t SZ_SLOT = 65536;
constexpr size_t OFF_SLOT = 0, SZ_SLOTS = 8 * MiB;
constexpr size_t OFF_YM = 8 * MiB;
constexpr size_t OFF_U = 8 * MiB;
constexpr size_t OFF_KX = 72 * MiB;
constexpr size_t OFF_VX = 192 * MiB;
constexpr size_t OFF_KM = 136 * MiB;
constexpr size_t OFF_PT = 225 * MiB;
constexpr size_t OFF_H1 = 230 * MiB;
constexpr size_t OFF_G = 236 * MiB;
constexpr size_t OFF_XF = 239 * MiB;
constexpr size_t OFF_CTL = 242 * MiB;
constexpr size_t OFF_DB = OFF_SLOT + 6 * MiB;
constexpr size_t OFF_PROBE = OFF_SLOT + 7 * MiB;
constexpr int FS = 64;
constexpr int C_TICKET = 0;
constexpr int C_PARITY = 8 * FS;
constexpr int C_TMO = 9 * FS;
constexpr int C_QHEAD = 10 * FS;
constexpr int C_KP = 16 * FS;
constexpr int C_VP = 48 * FS;
constexpr int C_UP = 80 * FS;
constexpr int C_F1P = 112 * FS;
constexpr int C_GP = 120 * FS;
constexpr int C_QP = 136 * FS;
constexpr int C_WORDS = 168 * FS;

struct Params {
  const uint16_t *x, *mem0, *wq, *wk, *wv, *wo, *w1, *b1, *w2, *b2;
  uint16_t* out;
  char* ws;
};

DI unsigned cvtpk(float lo, float hi) { f32x2 v = {lo, hi}; bf16x2 b = __builtin_convertvector(v, bf16x2); return __builtin_bit_cast(unsigned, b); }
DI unsigned nosent(unsigned x) { return x < 0xFFFFFFFEu ? x : 0xFFFFFFFEu; }
DI unsigned umax3(unsigned a, unsigned b, unsigned c) { unsigned m = a > b ? a : b; return m > c ? m : c; }
DI unsigned umax4(unsigned m, u32x4 v) { return umax3(umax3(m, v.x, v.y), v.z, v.w); }
DI rsrc_t mkrsrc(const void* p, size_t bytes) { return __builtin_amdgcn_make_buffer_rsrc((void*)p, 0, (int)bytes, 0x00020000); }
DI u32x4 ld128(rsrc_t r, unsigned v, unsigned s) { return __builtin_amdgcn_raw_buffer_load_b128(r, v, s, SC1); }
DI u32x2 ld64(rsrc_t r, unsigned v, unsigned s) { return __builtin_amdgcn_raw_buffer_load_b64(r, v, s, SC1); }
DI void st128(u32x4 d, rsrc_t r, unsigned v, unsigned s) { __builtin_amdgcn_raw_buffer_store_b128(d, r, v, s, SC1); }
DI void st64(u32x2 d, rsrc_t r, unsigned v, unsigned s) { __builtin_amdgcn_raw_buffer_store_b64(d, r, v, s, SC1); }
DI void st32(unsigned d, rsrc_t r, unsigned v, unsigned s) { __builtin_amdgcn_raw_buffer_store_b32(d, r, v, s, SC1); }
DI int lane_id() { int l; asm volatile("v_mbcnt_lo_u32_b32 %0, -1, 0\n\tv_mbcnt_hi_u32_b32 %0, -1, %0" : "=v"(l)); return l; }
DI unsigned xcc_id() { return (unsigned)__builtin_amdgcn_s_getreg(20 | (3 << 11)) & 0xfu; }
DI bf16x4 trread(LAS char* p) { return __builtin_amdgcn_ds_read_tr16_b64_v4bf16((LAS bf16x4*)p); }
DI bf16x8 cat8(bf16x4 a, bf16x4 b) { return __builtin_shufflevector(a, b, 0, 1, 2, 3, 4, 5, 6, 7); }
DI bf16x16 cat16(bf16x8 a, bf16x8 b) { return __builtin_shufflevector(a, b, 0, 1, 2, 3, 4, 5, 6, 7, 8, 9, 10, 11, 12, 13, 14, 15); }
DI f32x4 smf(bf16x8 a, bf16x16 b, f32x4 c, int idx) { return __builtin_amdgcn_smfmac_f32_16x16x64_bf16(a, b, c, idx, 0, 0); }
DI f32x4 mf32(bf16x8 a, bf16x8 b, f32x4 c) { return __builtin_amdgcn_mfma_f32_16x16x32_bf16(a, b, c, 0, 0, 0); }
DI f32x4 mf16(u32x2 a, u32x2 b, f32x4 c) { return __builtin_amdgcn_mfma_f32_16x16x16bf16_1k(__builtin_bit_cast(s16x4, a), __builtin_bit_cast(s16x4, b), c, 0, 0, 0); }
DI float swapadd(float a, float b) {
  auto r = __builtin_amdgcn_permlane32_swap(__builtin_bit_cast(unsigned, a), __builtin_bit_cast(unsigned, b), false, false);
  return __builtin_bit_cast(float, (unsigned)r[0]) + __builtin_bit_cast(float, (unsigned)r[1]);
}
#define BAR() asm volatile("s_barrier" ::: "memory")
#define WAIT_L0() asm volatile("s_waitcnt lgkmcnt(0)" ::: "memory")
#define WAIT_V0() asm volatile("s_waitcnt vmcnt(0)" ::: "memory")
#define SCHED() __builtin_amdgcn_sched_barrier(0)
template <int N> DI void wait_vm() { asm volatile("s_waitcnt vmcnt(%0)" ::"i"(N) : "memory"); }
DI unsigned nearblk(unsigned a, unsigned par) { return a + (((((a >> 21) & 1u) ^ par)) << 13); }
template <int CTRL> DI float dppf(float v) { return __builtin_bit_cast(float, __builtin_amdgcn_update_dpp(0, __builtin_bit_cast(int, v), CTRL, 0xf, 0xf, false)); }

constexpr size_t OFF_STAMP = 243 * MiB;
constexpr size_t WS_SPAN = 246 * MiB;
constexpr int L_MEM = 0, L_SX = 10240, SXROW = 160, L_RES = 81920, LDS_BYTES = 160 * 1024;
#define LDS_DROP 0x28000u
#define ASM_QK \
    "ds_read_b128 v[2:5], %[vres]\n" \
    "ds_read_b128 v[6:9], %[vres] offset:1024\n" \
    "ds_read_b128 v[10:13], %[vres] offset:2048\n" \
    "ds_read_b64 v[14:15], %[vres] offset:3072\n" \
    "ds_read_b64 v[252:253], %[vres] offset:3080\n" \
    "s_xor_b32 s99, %[par], 0x2000\n" \
    "s_add_u32 s99, s99, 6291456\n" \
    "ds_read_b128 v[16:19], %[vres] offset:4096\n" \
    "ds_read_b128 v[20:23], %[vres] offset:5120\n" \
    "ds_read_b128 v[24:27], %[vres] offset:6144\n" \
    "s_waitcnt lgkmcnt(0)\n" \
    "s_barrier\n" \
    "s_mul_i32 s70, %[wave], 3072\n" \
    "v_add_u32 v40, 61440, v24\n" \
    "v_add_u32 v43, 122880, v24\n" \
    "v_add_u32 v41, 61440, v25\n" \
    "v_add_u32 v44, 122880, v25\n" \
    "v_add_u32 v42, 61440, v26\n" \
    "v_add_u32 v45, 122880, v26\n" \
    "s_mov_b32 s67, 0\n" \
    "s_add_u32 m0, s70, 49152\n" \
    "s_nop 0\n" \
    "buffer_load_dwordx4 v16, %[w0], s67 offen lds\n" \
    "s_add_u32 m0, s70, 50176\n" \
    "s_nop 0\n" \
    "buffer_load_dwordx4 v17, %[w0], s67 offen lds\n" \
    "s_add_u32 m0, s70, 51200\n" \
    "s_nop 0\n" \
    "buffer_load_dwordx4 v18, %[w0], s67 offen lds\n" \
    "s_add_u32 m0, s70, 61440\n" \
    "s_nop 0\n" \
    "buffer_load_dwordx4 v20, %[w1], s67 offen lds\n" \
    "s_add_u32 m0, s70, 62464\n" \
    "s_nop 0\n" \
    "buffer_load_dwordx4 v21, %[w1], s67 offen lds\n" \
    "s_add_u32 m0, s70, 63488\n" \
    "s_nop 0\n" \
    "buffer_load_dwordx4 v22, %[w1], s67 offen lds\n" \
    "s_mov_b32 s67, 393216\n" \
    "s_add_u32 m0, s70, 73728\n" \
    "s_nop 0\n" \
    "buffer_load_dwordx4 v16, %[w0], s67 offen lds\n" \
    "s_add_u32 m0, s70, 74752\n" \
    "s_nop 0\n" \
    "buffer_load_dwordx4 v17, %[w0], s67 offen lds\n" \
    "s_add_u32 m0, s70, 75776\n" \
    "s_nop 0\n" \
    "buffer_load_dwordx4 v18, %[w0], s67 offen lds\n" \
    "s_add_u32 m0, s70, 86016\n" \
    "s_nop 0\n" \
    "buffer_load_dwordx4 v20, %[w1], s67 offen lds\n" \
    "s_add_u32 m0, s70, 87040\n" \
    "s_nop 0\n" \
    "buffer_load_dwordx4 v21, %[w1], s67 offen lds\n" \
    "s_add_u32 m0, s70, 88064\n" \
    "s_nop 0\n" \
    "buffer_load_dwordx4 v22, %[w1], s67 offen lds\n" \
    "s_mov_b32 s67, 1179648\n" \
    "s_add_u32 m0, s70, 98304\n" \
    "s_nop 0\n" \
    "buffer_load_dwordx4 v16, %[w0], s67 offen lds\n" \
    "s_add_u32 m0, s70, 99328\n" \
    "s_nop 0\n" \
    "buffer_load_dwordx4 v17, %[w0], s67 offen lds\n" \
    "s_add_u32 m0, s70, 100352\n" \
    "s_nop 0\n" \
    "buffer_load_dwordx4 v18, %[w0], s67 offen lds\n" \
    "s_add_u32 m0, s70, 110592\n" \
    "s_nop 0\n" \
    "buffer_load_dwordx4 v20, %[w1], s67 offen lds\n" \
    "s_add_u32 m0, s70, 111616\n" \
    "s_nop 0\n" \
    "buffer_load_dwordx4 v21, %[w1], s67 offen lds\n" \
    "s_add_u32 m0, s70, 112640\n" \
    "s_nop 0\n" \
    "buffer_load_dwordx4 v22, %[w1], s67 offen lds\n" \
    "s_mov_b32 s67, 1572864\n" \
    "s_add_u32 m0, s70, 122880\n" \
    "s_nop 0\n" \
    "buffer_load_dwordx4 v16, %[w0], s67 offen lds\n" \
    "s_add_u32 m0, s70, 123904\n" \
    "s_nop 0\n" \
    "buffer_load_dwordx4 v17, %[w0], s67 offen lds\n" \
    "s_add_u32 m0, s70, 124928\n" \
    "s_nop 0\n" \
    "buffer_load_dwordx4 v18, %[w0], s67 offen lds\n" \
    "s_add_u32 m0, s70, 135168\n" \
    "s_nop 0\n" \
    "buffer_load_dwordx4 v20, %[w1], s67 offen lds\n" \
    "s_add_u32 m0, s70, 136192\n" \
    "s_nop 0\n" \
    "buffer_load_dwordx4 v21, %[w1], s67 offen lds\n" \
    "s_add_u32 m0, s70, 137216\n" \
    "s_nop 0\n" \
    "buffer_load_dwordx4 v22, %[w1], s67 offen lds\n" \
    "s_mov_b32 s67, 1966080\n" \
    "s_add_u32 m0, s70, 0\n" \
    "s_nop 0\n" \
    "buffer_load_dwordx4 v16, %[w0], s67 offen lds\n" \
    "s_add_u32 m0, s70, 1024\n" \
    "s_nop 0\n" \
    "buffer_load_dwordx4 v17, %[w0], s67 offen lds\n" \
    "s_add_u32 m0, s70, 2048\n" \
    "s_nop 0\n" \
    "buffer_load_dwordx4 v18, %[w0], s67 offen lds\n" \
    "s_add_u32 m0, s70, 12288\n" \
    "s_nop 0\n" \
    "buffer_load_dwordx4 v20, %[w1], s67 offen lds\n" \
    "s_add_u32 m0, s70, 13312\n" \
    "s_nop 0\n" \
    "buffer_load_dwordx4 v21, %[w1], s67 offen lds\n" \
    "s_add_u32 m0, s70, 14336\n" \
    "s_nop 0\n" \
    "buffer_load_dwordx4 v22, %[w1], s67 offen lds\n" \
    "s_mov_b32 s67, 2359296\n" \
    "s_add_u32 m0, s70, 24576\n" \
    "s_nop 0\n" \
    "buffer_load_dwordx4 v16, %[w0], s67 offen lds\n" \
    "s_add_u32 m0, s70, 25600\n" \
    "s_nop 0\n" \
    "buffer_load_dwordx4 v17, %[w0], s67 offen lds\n" \
    "s_add_u32 m0, s70, 26624\n" \
    "s_nop 0\n" \
    "buffer_load_dwordx4 v18, %[w0], s67 offen lds\n" \
    "s_add_u32 m0, s70, 36864\n" \
    "s_nop 0\n" \
    "buffer_load_dwordx4 v20, %[w1], s67 offen lds\n" \
    "s_add_u32 m0, s70, 37888\n" \
    "s_nop 0\n" \
    "buffer_load_dwordx4 v21, %[w1], s67 offen lds\n" \
    "s_add_u32 m0, s70, 38912\n" \
    "s_nop 0\n" \
    "buffer_load_dwordx4 v22, %[w1], s67 offen lds\n" \
    "s_waitcnt vmcnt(30)\n" \
    "s_barrier\n" \
    "ds_read_b64_tr_b16 a[0:1], v24 offset:49152\n" \
    "ds_read_b64_tr_b16 a[2:3], v24 offset:49920\n" \
    "ds_read_b64_tr_b16 a[4:5], v24 offset:55296\n" \
    "ds_read_b64_tr_b16 a[6:7], v24 offset:56064\n" \
    "ds_read_b64_tr_b16 a[8:9], v25 offset:49152\n" \
    "ds_read_b64_tr_b16 a[10:11], v25 offset:49920\n" \
    "ds_read_b64_tr_b16 a[12:13], v25 offset:55296\n" \
    "ds_read_b64_tr_b16 a[14:15], v25 offset:56064\n" \
    "ds_read_b64_tr_b16 a[16:17], v26 offset:49152\n" \
    "ds_read_b64_tr_b16 a[18:19], v26 offset:49920\n" \
    "ds_read_b64_tr_b16 a[20:21], v26 offset:55296\n" \
    "ds_read_b64_tr_b16 a[22:23], v26 offset:56064\n" \
    "s_waitcnt lgkmcnt(0)\n" \
    "s_waitcnt vmcnt(24)\n" \
    "s_barrier\n" \
    "s_mov_b32 s67, 3145728\n" \
    "s_add_u32 m0, s70, 49152\n" \
    "s_nop 0\n" \
    "buffer_load_dwordx4 v16, %[w0], s67 offen lds\n" \
    "s_add_u32 m0, s70, 50176\n" \
    "s_nop 0\n" \
    "buffer_load_dwordx4 v17, %[w0], s67 offen lds\n" \
    "s_add_u32 m0, s70, 51200\n" \
    "s_nop 0\n" \
    "buffer_load_dwordx4 v18, %[w0], s67 offen lds\n" \
    "s_add_u32 m0, s70, 61440\n" \
    "s_nop 0\n" \
    "buffer_load_dwordx4 v20, %[w1], s67 offen lds\n" \
    "s_add_u32 m0, s70, 62464\n" \
    "s_nop 0\n" \
    "buffer_load_dwordx4 v21, %[w1], s67 offen lds\n" \
    "s_add_u32 m0, s70, 63488\n" \
    "s_nop 0\n" \
    "buffer_load_dwordx4 v22, %[w1], s67 offen lds\n" \
    "ds_read_b64_tr_b16 a[24:25], v40 offset:12288\n" \
    "ds_read_b64_tr_b16 a[26:27], v40 offset:13056\n" \
    "ds_read_b64_tr_b16 a[28:29], v40 offset:18432\n" \
    "ds_read_b64_tr_b16 a[30:31], v40 offset:19200\n" \
    "ds_read_b64_tr_b16 a[32:33], v41 offset:12288\n" \
    "ds_read_b64_tr_b16 a[34:35], v41 offset:13056\n" \
    "ds_read_b64_tr_b16 a[36:37], v41 offset:18432\n" \
    "ds_read_b64_tr_b16 a[38:39], v41 offset:19200\n" \
    "ds_read_b64_tr_b16 a[40:41], v42 offset:12288\n" \
    "ds_read_b64_tr_b16 a[42:43], v42 offset:13056\n" \
    "ds_read_b64_tr_b16 a[44:45], v42 offset:18432\n" \
    "ds_read_b64_tr_b16 a[46:47], v42 offset:19200\n" \
    "s_waitcnt lgkmcnt(0)\n" \
    "s_waitcnt vmcnt(24)\n" \
    "s_barrier\n" \
    "s_mov_b32 s67, 3538944\n" \
    "s_add_u32 m0, s70, 73728\n" \
    "s_nop 0\n" \
    "buffer_load_dwordx4 v16, %[w0], s67 offen lds\n" \
    "s_add_u32 m0, s70, 74752\n" \
    "s_nop 0\n" \
    "buffer_load_dwordx4 v17, %[w0], s67 offen lds\n" \
    "s_add_u32 m0, s70, 75776\n" \
    "s_nop 0\n" \
    "buffer_load_dwordx4 v18, %[w0], s67 offen lds\n" \
    "s_add_u32 m0, s70, 86016\n" \
    "s_nop 0\n" \
    "buffer_load_dwordx4 v20, %[w1], s67 offen lds\n" \
    "s_add_u32 m0, s70, 87040\n" \
    "s_nop 0\n" \
    "buffer_load_dwordx4 v21, %[w1], s67 offen lds\n" \
    "s_add_u32 m0, s70, 88064\n" \
    "s_nop 0\n" \
    "buffer_load_dwordx4 v22, %[w1], s67 offen lds\n" \
    "ds_read_b64_tr_b16 a[48:49], v40 offset:36864\n" \
    "ds_read_b64_tr_b16 a[50:51], v40 offset:37632\n" \
    "ds_read_b64_tr_b16 a[52:53], v40 offset:43008\n" \
    "ds_read_b64_tr_b16 a[54:55], v40 offset:43776\n" \
    "ds_read_b64_tr_b16 a[56:57], v41 offset:36864\n" \
    "ds_read_b64_tr_b16 a[58:59], v41 offset:37632\n" \
    "ds_read_b64_tr_b16 a[60:61], v41 offset:43008\n" \
    "ds_read_b64_tr_b16 a[62:63], v41 offset:43776\n" \
    "ds_read_b64_tr_b16 a[64:65], v42 offset:36864\n" \
    "ds_read_b64_tr_b16 a[66:67], v42 offset:37632\n" \
    "ds_read_b64_tr_b16 a[68:69], v42 offset:43008\n" \
    "ds_read_b64_tr_b16 a[70:71], v42 offset:43776\n" \
    "s_waitcnt lgkmcnt(0)\n" \
    "s_waitcnt vmcnt(24)\n" \
    "s_barrier\n" \
    "s_mov_b32 s67, 3932160\n" \
    "s_add_u32 m0, s70, 98304\n" \
    "s_nop 0\n" \
    "buffer_load_dwordx4 v16, %[w0], s67 offen lds\n" \
    "s_add_u32 m0, s70, 99328\n" \
    "s_nop 0\n" \
    "buffer_load_dwordx4 v17, %[w0], s67 offen lds\n" \
    "s_add_u32 m0, s70, 100352\n" \
    "s_nop 0\n" \
    "buffer_load_dwordx4 v18, %[w0], s67 offen lds\n" \
    "s_add_u32 m0, s70, 110592\n" \
    "s_nop 0\n" \
    "buffer_load_dwordx4 v20, %[w1], s67 offen lds\n" \
    "s_add_u32 m0, s70, 111616\n" \
    "s_nop 0\n" \
    "buffer_load_dwordx4 v21, %[w1], s67 offen lds\n" \
    "s_add_u32 m0, s70, 112640\n" \
    "s_nop 0\n" \
    "buffer_load_dwordx4 v22, %[w1], s67 offen lds\n" \
    "ds_read_b64_tr_b16 a[72:73], v43\n" \
    "ds_read_b64_tr_b16 a[74:75], v43 offset:768\n" \
    "ds_read_b64_tr_b16 a[76:77], v43 offset:6144\n" \
    "ds_read_b64_tr_b16 a[78:79], v43 offset:6912\n" \
    "ds_read_b64_tr_b16 a[80:81], v44\n" \
    "ds_read_b64_tr_b16 a[82:83], v44 offset:768\n" \
    "ds_read_b64_tr_b16 a[84:85], v44 offset:6144\n" \
    "ds_read_b64_tr_b16 a[86:87], v44 offset:6912\n" \
    "ds_read_b64_tr_b16 a[88:89], v45\n" \
    "ds_read_b64_tr_b16 a[90:91], v45 offset:768\n" \
    "ds_read_b64_tr_b16 a[92:93], v45 offset:6144\n" \
    "ds_read_b64_tr_b16 a[94:95], v45 offset:6912\n" \
    "s_waitcnt lgkmcnt(0)\n" \
    "s_waitcnt vmcnt(24)\n" \
    "s_barrier\n" \
    "s_mov_b32 s67, 4325376\n" \
    "s_add_u32 m0, s70, 122880\n" \
    "s_nop 0\n" \
    "buffer_load_dwordx4 v16, %[w0], s67 offen lds\n" \
    "s_add_u32 m0, s70, 123904\n" \
    "s_nop 0\n" \
    "buffer_load_dwordx4 v17, %[w0], s67 offen lds\n" \
    "s_add_u32 m0, s70, 124928\n" \
    "s_nop 0\n" \
    "buffer_load_dwordx4 v18, %[w0], s67 offen lds\n" \
    "s_add_u32 m0, s70, 135168\n" \
    "s_nop 0\n" \
    "buffer_load_dwordx4 v20, %[w1], s67 offen lds\n" \
    "s_add_u32 m0, s70, 136192\n" \
    "s_nop 0\n" \
    "buffer_load_dwordx4 v21, %[w1], s67 offen lds\n" \
    "s_add_u32 m0, s70, 137216\n" \
    "s_nop 0\n" \
    "buffer_load_dwordx4 v22, %[w1], s67 offen lds\n" \
    "ds_read_b64_tr_b16 a[96:97], v24\n" \
    "ds_read_b64_tr_b16 a[98:99], v24 offset:768\n" \
    "ds_read_b64_tr_b16 a[100:101], v24 offset:6144\n" \
    "ds_read_b64_tr_b16 a[102:103], v24 offset:6912\n" \
    "ds_read_b64_tr_b16 a[104:105], v25\n" \
    "ds_read_b64_tr_b16 a[106:107], v25 offset:768\n" \
    "ds_read_b64_tr_b16 a[108:109], v25 offset:6144\n" \
    "ds_read_b64_tr_b16 a[110:111], v25 offset:6912\n" \
    "ds_read_b64_tr_b16 a[112:113], v26\n" \
    "ds_read_b64_tr_b16 a[114:115], v26 offset:768\n" \
    "ds_read_b64_tr_b16 a[116:117], v26 offset:6144\n" \
    "ds_read_b64_tr_b16 a[118:119], v26 offset:6912\n" \
    "s_waitcnt lgkmcnt(0)\n" \
    "s_waitcnt vmcnt(24)\n" \
    "s_barrier\n" \
    "s_mov_b32 s67, 5111808\n" \
    "s_add_u32 m0, s70, 0\n" \
    "s_nop 0\n" \
    "buffer_load_dwordx4 v16, %[w0], s67 offen lds\n" \
    "s_add_u32 m0, s70, 1024\n" \
    "s_nop 0\n" \
    "buffer_load_dwordx4 v17, %[w0], s67 offen lds\n" \
    "s_add_u32 m0, s70, 2048\n" \
    "s_nop 0\n" \
    "buffer_load_dwordx4 v18, %[w0], s67 offen lds\n" \
    "s_add_u32 m0, s70, 12288\n" \
    "s_nop 0\n" \
    "buffer_load_dwordx4 v20, %[w1], s67 offen lds\n" \
    "s_add_u32 m0, s70, 13312\n" \
    "s_nop 0\n" \
    "buffer_load_dwordx4 v21, %[w1], s67 offen lds\n" \
    "s_add_u32 m0, s70, 14336\n" \
    "s_nop 0\n" \
    "buffer_load_dwordx4 v22, %[w1], s67 offen lds\n" \
    "ds_read_b64_tr_b16 a[120:121], v24 offset:24576\n" \
    "ds_read_b64_tr_b16 a[122:123], v24 offset:25344\n" \
    "ds_read_b64_tr_b16 a[124:125], v24 offset:30720\n" \
    "ds_read_b64_tr_b16 a[126:127], v24 offset:31488\n" \
    "ds_read_b64_tr_b16 a[128:129], v25 offset:24576\n" \
    "ds_read_b64_tr_b16 a[130:131], v25 offset:25344\n" \
    "ds_read_b64_tr_b16 a[132:133], v25 offset:30720\n" \
    "ds_read_b64_tr_b16 a[134:135], v25 offset:31488\n" \
    "ds_read_b64_tr_b16 a[136:137], v26 offset:24576\n" \
    "ds_read_b64_tr_b16 a[138:139], v26 offset:25344\n" \
    "ds_read_b64_tr_b16 a[140:141], v26 offset:30720\n" \
    "ds_read_b64_tr_b16 a[142:143], v26 offset:31488\n" \
    "s_waitcnt lgkmcnt(0)\n" \
    "s_waitcnt vmcnt(24)\n" \
    "s_barrier\n" \
    "s_mov_b32 s67, 5505024\n" \
    "s_add_u32 m0, s70, 24576\n" \
    "s_nop 0\n" \
    "buffer_load_dwordx4 v16, %[w0], s67 offen lds\n" \
    "s_add_u32 m0, s70, 25600\n" \
    "s_nop 0\n" \
    "buffer_load_dwordx4 v17, %[w0], s67 offen lds\n" \
    "s_add_u32 m0, s70, 26624\n" \
    "s_nop 0\n" \
    "buffer_load_dwordx4 v18, %[w0], s67 offen lds\n" \
    "s_add_u32 m0, s70, 36864\n" \
    "s_nop 0\n" \
    "buffer_load_dwordx4 v20, %[w1], s67 offen lds\n" \
    "s_add_u32 m0, s70, 37888\n" \
    "s_nop 0\n" \
    "buffer_load_dwordx4 v21, %[w1], s67 offen lds\n" \
    "s_add_u32 m0, s70, 38912\n" \
    "s_nop 0\n" \
    "buffer_load_dwordx4 v22, %[w1], s67 offen lds\n" \
    "ds_read_b64_tr_b16 a[144:145], v24 offset:49152\n" \
    "ds_read_b64_tr_b16 a[146:147], v24 offset:49920\n" \
    "ds_read_b64_tr_b16 a[148:149], v24 offset:55296\n" \
    "ds_read_b64_tr_b16 a[150:151], v24 offset:56064\n" \
    "ds_read_b64_tr_b16 a[152:153], v25 offset:49152\n" \
    "ds_read_b64_tr_b16 a[154:155], v25 offset:49920\n" \
    "ds_read_b64_tr_b16 a[156:157], v25 offset:55296\n" \
    "ds_read_b64_tr_b16 a[158:159], v25 offset:56064\n" \
    "ds_read_b64_tr_b16 a[160:161], v26 offset:49152\n" \
    "ds_read_b64_tr_b16 a[162:163], v26 offset:49920\n" \
    "ds_read_b64_tr_b16 a[164:165], v26 offset:55296\n" \
    "ds_read_b64_tr_b16 a[166:167], v26 offset:56064\n" \
    "s_waitcnt lgkmcnt(0)\n" \
    "s_waitcnt vmcnt(24)\n" \
    "s_barrier\n" \
    "s_mov_b32 s67, 5898240\n" \
    "s_add_u32 m0, s70, 49152\n" \
    "s_nop 0\n" \
    "buffer_load_dwordx4 v16, %[w0], s67 offen lds\n" \
    "s_add_u32 m0, s70, 50176\n" \
    "s_nop 0\n" \
    "buffer_load_dwordx4 v17, %[w0], s67 offen lds\n" \
    "s_add_u32 m0, s70, 51200\n" \
    "s_nop 0\n" \
    "buffer_load_dwordx4 v18, %[w0], s67 offen lds\n" \
    "s_add_u32 m0, s70, 61440\n" \
    "s_nop 0\n" \
    "buffer_load_dwordx4 v20, %[w1], s67 offen lds\n" \
    "s_add_u32 m0, s70, 62464\n" \
    "s_nop 0\n" \
    "buffer_load_dwordx4 v21, %[w1], s67 offen lds\n" \
    "s_add_u32 m0, s70, 63488\n" \
    "s_nop 0\n" \
    "buffer_load_dwordx4 v22, %[w1], s67 offen lds\n" \
    "ds_read_b64_tr_b16 a[168:169], v40 offset:12288\n" \
    "ds_read_b64_tr_b16 a[170:171], v40 offset:13056\n" \
    "ds_read_b64_tr_b16 a[172:173], v40 offset:18432\n" \
    "ds_read_b64_tr_b16 a[174:175], v40 offset:19200\n" \
    "ds_read_b64_tr_b16 a[176:177], v41 offset:12288\n" \
    "ds_read_b64_tr_b16 a[178:179], v41 offset:13056\n" \
    "ds_read_b64_tr_b16 a[180:181], v41 offset:18432\n" \
    "ds_read_b64_tr_b16 a[182:183], v41 offset:19200\n" \
    "ds_read_b64_tr_b16 a[184:185], v42 offset:12288\n" \
    "ds_read_b64_tr_b16 a[186:187], v42 offset:13056\n" \
    "ds_read_b64_tr_b16 a[188:189], v42 offset:18432\n" \
    "ds_read_b64_tr_b16 a[190:191], v42 offset:19200\n" \
    "s_waitcnt lgkmcnt(0)\n" \
    "s_waitcnt vmcnt(24)\n" \
    "s_barrier\n" \
    "s_mov_b32 s67, 6291456\n" \
    "s_add_u32 m0, s70, 73728\n" \
    "s_nop 0\n" \
    "buffer_load_dwordx4 v16, %[w0], s67 offen lds\n" \
    "s_add_u32 m0, s70, 74752\n" \
    "s_nop 0\n" \
    "buffer_load_dwordx4 v17, %[w0], s67 offen lds\n" \
    "s_add_u32 m0, s70, 75776\n" \
    "s_nop 0\n" \
    "buffer_load_dwordx4 v18, %[w0], s67 offen lds\n" \
    "s_add_u32 m0, s70, 86016\n" \
    "s_nop 0\n" \
    "buffer_load_dwordx4 v20, %[w1], s67 offen lds\n" \
    "s_add_u32 m0, s70, 87040\n" \
    "s_nop 0\n" \
    "buffer_load_dwordx4 v21, %[w1], s67 offen lds\n" \
    "s_add_u32 m0, s70, 88064\n" \
    "s_nop 0\n" \
    "buffer_load_dwordx4 v22, %[w1], s67 offen lds\n" \
    "ds_read_b64_tr_b16 a[192:193], v40 offset:36864\n" \
    "ds_read_b64_tr_b16 a[194:195], v40 offset:37632\n" \
    "ds_read_b64_tr_b16 a[196:197], v40 offset:43008\n" \
    "ds_read_b64_tr_b16 a[198:199], v40 offset:43776\n" \
    "ds_read_b64_tr_b16 a[200:201], v41 offset:36864\n" \
    "ds_read_b64_tr_b16 a[202:203], v41 offset:37632\n" \
    "ds_read_b64_tr_b16 a[204:205], v41 offset:43008\n" \
    "ds_read_b64_tr_b16 a[206:207], v41 offset:43776\n" \
    "ds_read_b64_tr_b16 a[208:209], v42 offset:36864\n" \
    "ds_read_b64_tr_b16 a[210:211], v42 offset:37632\n" \
    "ds_read_b64_tr_b16 a[212:213], v42 offset:43008\n" \
    "ds_read_b64_tr_b16 a[214:215], v42 offset:43776\n" \
    "s_waitcnt lgkmcnt(0)\n" \
    "s_waitcnt vmcnt(24)\n" \
    "s_barrier\n" \
    "s_mov_b32 s67, 6684672\n" \
    "s_add_u32 m0, s70, 98304\n" \
    "s_nop 0\n" \
    "buffer_load_dwordx4 v16, %[w0], s67 offen lds\n" \
    "s_add_u32 m0, s70, 99328\n" \
    "s_nop 0\n" \
    "buffer_load_dwordx4 v17, %[w0], s67 offen lds\n" \
    "s_add_u32 m0, s70, 100352\n" \
    "s_nop 0\n" \
    "buffer_load_dwordx4 v18, %[w0], s67 offen lds\n" \
    "s_add_u32 m0, s70, 110592\n" \
    "s_nop 0\n" \
    "buffer_load_dwordx4 v20, %[w1], s67 offen lds\n" \
    "s_add_u32 m0, s70, 111616\n" \
    "s_nop 0\n" \
    "buffer_load_dwordx4 v21, %[w1], s67 offen lds\n" \
    "s_add_u32 m0, s70, 112640\n" \
    "s_nop 0\n" \
    "buffer_load_dwordx4 v22, %[w1], s67 offen lds\n" \
    "ds_read_b64_tr_b16 a[216:217], v43\n" \
    "ds_read_b64_tr_b16 a[218:219], v43 offset:768\n" \
    "ds_read_b64_tr_b16 a[220:221], v43 offset:6144\n" \
    "ds_read_b64_tr_b16 a[222:223], v43 offset:6912\n" \
    "ds_read_b64_tr_b16 a[224:225], v44\n" \
    "ds_read_b64_tr_b16 a[226:227], v44 offset:768\n" \
    "ds_read_b64_tr_b16 a[228:229], v44 offset:6144\n" \
    "ds_read_b64_tr_b16 a[230:231], v44 offset:6912\n" \
    "ds_read_b64_tr_b16 a[232:233], v45\n" \
    "ds_read_b64_tr_b16 a[234:235], v45 offset:768\n" \
    "ds_read_b64_tr_b16 a[236:237], v45 offset:6144\n" \
    "ds_read_b64_tr_b16 a[238:239], v45 offset:6912\n" \
    "s_waitcnt lgkmcnt(0)\n" \
    "s_waitcnt vmcnt(24)\n" \
    "s_barrier\n" \
    "s_mov_b32 s67, 7471104\n" \
    "s_add_u32 m0, s70, 122880\n" \
    "s_nop 0\n" \
    "buffer_load_dwordx4 v16, %[w0], s67 offen lds\n" \
    "s_add_u32 m0, s70, 123904\n" \
    "s_nop 0\n" \
    "buffer_load_dwordx4 v17, %[w0], s67 offen lds\n" \
    "s_add_u32 m0, s70, 124928\n" \
    "s_nop 0\n" \
    "buffer_load_dwordx4 v18, %[w0], s67 offen lds\n" \
    "s_add_u32 m0, s70, 135168\n" \
    "s_nop 0\n" \
    "buffer_load_dwordx4 v20, %[w1], s67 offen lds\n" \
    "s_add_u32 m0, s70, 136192\n" \
    "s_nop 0\n" \
    "buffer_load_dwordx4 v21, %[w1], s67 offen lds\n" \
    "s_add_u32 m0, s70, 137216\n" \
    "s_nop 0\n" \
    "buffer_load_dwordx4 v22, %[w1], s67 offen lds\n" \
    "ds_read_b64_tr_b16 a[240:241], v24\n" \
    "ds_read_b64_tr_b16 a[242:243], v24 offset:768\n" \
    "ds_read_b64_tr_b16 a[244:245], v24 offset:6144\n" \
    "ds_read_b64_tr_b16 a[246:247], v24 offset:6912\n" \
    "ds_read_b64_tr_b16 a[248:249], v25\n" \
    "ds_read_b64_tr_b16 a[250:251], v25 offset:768\n" \
    "ds_read_b64_tr_b16 a[252:253], v25 offset:6144\n" \
    "ds_read_b64_tr_b16 a[254:255], v25 offset:6912\n" \
    "ds_read_b64_tr_b16 v[108:109], v26\n" \
    "ds_read_b64_tr_b16 v[110:111], v26 offset:768\n" \
    "ds_read_b64_tr_b16 v[112:113], v26 offset:6144\n" \
    "ds_read_b64_tr_b16 v[114:115], v26 offset:6912\n" \
    "s_waitcnt lgkmcnt(0)\n" \
    "s_waitcnt vmcnt(24)\n" \
    "s_barrier\n" \
    "s_mov_b32 s67, 786432\n" \
    "s_add_u32 m0, s70, 0\n" \
    "s_nop 0\n" \
    "buffer_load_dwordx4 v16, %[w0], s67 offen lds\n" \
    "s_add_u32 m0, s70, 1024\n" \
    "s_nop 0\n" \
    "buffer_load_dwordx4 v17, %[w0], s67 offen lds\n" \
    "s_add_u32 m0, s70, 2048\n" \
    "s_nop 0\n" \
    "buffer_load_dwordx4 v18, %[w0], s67 offen lds\n" \
    "s_add_u32 m0, s70, 12288\n" \
    "s_nop 0\n" \
    "buffer_load_dwordx4 v20, %[w1], s67 offen lds\n" \
    "s_add_u32 m0, s70, 13312\n" \
    "s_nop 0\n" \
    "buffer_load_dwordx4 v21, %[w1], s67 offen lds\n" \
    "s_add_u32 m0, s70, 14336\n" \
    "s_nop 0\n" \
    "buffer_load_dwordx4 v22, %[w1], s67 offen lds\n" \
    "ds_read_b64_tr_b16 v[116:117], v24 offset:24576\n" \
    "ds_read_b64_tr_b16 v[118:119], v24 offset:25344\n" \
    "ds_read_b64_tr_b16 v[120:121], v24 offset:30720\n" \
    "ds_read_b64_tr_b16 v[122:123], v24 offset:31488\n" \
    "ds_read_b64_tr_b16 v[164:165], v25 offset:24576\n" \
    "ds_read_b64_tr_b16 v[166:167], v25 offset:25344\n" \
    "ds_read_b64_tr_b16 v[168:169], v25 offset:30720\n" \
    "ds_read_b64_tr_b16 v[170:171], v25 offset:31488\n" \
    "ds_read_b64_tr_b16 v[204:205], v26 offset:24576\n" \
    "ds_read_b64_tr_b16 v[206:207], v26 offset:25344\n" \
    "ds_read_b64_tr_b16 v[208:209], v26 offset:30720\n" \
    "ds_read_b64_tr_b16 v[210:211], v26 offset:31488\n" \
    "s_waitcnt lgkmcnt(0)\n" \
    "s_waitcnt vmcnt(24)\n" \
    "s_barrier\n" \
    "s_mov_b32 s67, 2752512\n" \
    "s_add_u32 m0, s70, 24576\n" \
    "s_nop 0\n" \
    "buffer_load_dwordx4 v16, %[w0], s67 offen lds\n" \
    "s_add_u32 m0, s70, 25600\n" \
    "s_nop 0\n" \
    "buffer_load_dwordx4 v17, %[w0], s67 offen lds\n" \
    "s_add_u32 m0, s70, 26624\n" \
    "s_nop 0\n" \
    "buffer_load_dwordx4 v18, %[w0], s67 offen lds\n" \
    "s_add_u32 m0, s70, 36864\n" \
    "s_nop 0\n" \
    "buffer_load_dwordx4 v20, %[w1], s67 offen lds\n" \
    "s_add_u32 m0, s70, 37888\n" \
    "s_nop 0\n" \
    "buffer_load_dwordx4 v21, %[w1], s67 offen lds\n" \
    "s_add_u32 m0, s70, 38912\n" \
    "s_nop 0\n" \
    "buffer_load_dwordx4 v22, %[w1], s67 offen lds\n" \
    "ds_read_b64_tr_b16 v[124:125], v24 offset:49152\n" \
    "ds_read_b64_tr_b16 v[126:127], v24 offset:49920\n" \
    "ds_read_b64_tr_b16 v[128:129], v24 offset:55296\n" \
    "ds_read_b64_tr_b16 v[130:131], v24 offset:56064\n" \
    "ds_read_b64_tr_b16 v[172:173], v25 offset:49152\n" \
    "ds_read_b64_tr_b16 v[174:175], v25 offset:49920\n" \
    "ds_read_b64_tr_b16 v[176:177], v25 offset:55296\n" \
    "ds_read_b64_tr_b16 v[178:179], v25 offset:56064\n" \
    "ds_read_b64_tr_b16 v[212:213], v26 offset:49152\n" \
    "ds_read_b64_tr_b16 v[214:215], v26 offset:49920\n" \
    "ds_read_b64_tr_b16 v[216:217], v26 offset:55296\n" \
    "ds_read_b64_tr_b16 v[218:219], v26 offset:56064\n" \
    "s_waitcnt lgkmcnt(0)\n" \
    "s_waitcnt vmcnt(24)\n" \
    "s_barrier\n" \
    "s_mov_b32 s67, 4718592\n" \
    "s_add_u32 m0, s70, 49152\n" \
    "s_nop 0\n" \
    "buffer_load_dwordx4 v16, %[w0], s67 offen lds\n" \
    "s_add_u32 m0, s70, 50176\n" \
    "s_nop 0\n" \
    "buffer_load_dwordx4 v17, %[w0], s67 offen lds\n" \
    "s_add_u32 m0, s70, 51200\n" \
    "s_nop 0\n" \
    "buffer_load_dwordx4 v18, %[w0], s67 offen lds\n" \
    "s_add_u32 m0, s70, 61440\n" \
    "s_nop 0\n" \
    "buffer_load_dwordx4 v20, %[w1], s67 offen lds\n" \
    "s_add_u32 m0, s70, 62464\n" \
    "s_nop 0\n" \
    "buffer_load_dwordx4 v21, %[w1], s67 offen lds\n" \
    "s_add_u32 m0, s70, 63488\n" \
    "s_nop 0\n" \
    "buffer_load_dwordx4 v22, %[w1], s67 offen lds\n" \
    "ds_read_b64_tr_b16 v[132:133], v40 offset:12288\n" \
    "ds_read_b64_tr_b16 v[134:135], v40 offset:13056\n" \
    "ds_read_b64_tr_b16 v[136:137], v40 offset:18432\n" \
    "ds_read_b64_tr_b16 v[138:139], v40 offset:19200\n" \
    "ds_read_b64_tr_b16 v[180:181], v41 offset:12288\n" \
    "ds_read_b64_tr_b16 v[182:183], v41 offset:13056\n" \
    "ds_read_b64_tr_b16 v[184:185], v41 offset:18432\n" \
    "ds_read_b64_tr_b16 v[186:187], v41 offset:19200\n" \
    "ds_read_b64_tr_b16 v[220:221], v42 offset:12288\n" \
    "ds_read_b64_tr_b16 v[222:223], v42 offset:13056\n" \
    "ds_read_b64_tr_b16 v[224:225], v42 offset:18432\n" \
    "ds_read_b64_tr_b16 v[226:227], v42 offset:19200\n" \
    "s_waitcnt lgkmcnt(0)\n" \
    "s_waitcnt vmcnt(24)\n" \
    "s_barrier\n" \
    "ds_read_b64_tr_b16 v[140:141], v40 offset:36864\n" \
    "ds_read_b64_tr_b16 v[142:143], v40 offset:37632\n" \
    "ds_read_b64_tr_b16 v[144:145], v40 offset:43008\n" \
    "ds_read_b64_tr_b16 v[146:147], v40 offset:43776\n" \
    "ds_read_b64_tr_b16 v[188:189], v41 offset:36864\n" \
    "ds_read_b64_tr_b16 v[190:191], v41 offset:37632\n" \
    "ds_read_b64_tr_b16 v[192:193], v41 offset:43008\n" \
    "ds_read_b64_tr_b16 v[194:195], v41 offset:43776\n" \
    "ds_read_b64_tr_b16 v[228:229], v42 offset:36864\n" \
    "ds_read_b64_tr_b16 v[230:231], v42 offset:37632\n" \
    "ds_read_b64_tr_b16 v[232:233], v42 offset:43008\n" \
    "ds_read_b64_tr_b16 v[234:235], v42 offset:43776\n" \
    "s_waitcnt lgkmcnt(0)\n" \
    "s_waitcnt vmcnt(18)\n" \
    "s_barrier\n" \
    "ds_read_b64_tr_b16 v[156:157], v43\n" \
    "ds_read_b64_tr_b16 v[158:159], v43 offset:768\n" \
    "ds_read_b64_tr_b16 v[160:161], v43 offset:6144\n" \
    "ds_read_b64_tr_b16 v[162:163], v43 offset:6912\n" \
    "ds_read_b64_tr_b16 v[196:197], v44\n" \
    "ds_read_b64_tr_b16 v[198:199], v44 offset:768\n" \
    "ds_read_b64_tr_b16 v[200:201], v44 offset:6144\n" \
    "ds_read_b64_tr_b16 v[202:203], v44 offset:6912\n" \
    "ds_read_b64_tr_b16 v[244:245], v45\n" \
    "ds_read_b64_tr_b16 v[246:247], v45 offset:768\n" \
    "ds_read_b64_tr_b16 v[248:249], v45 offset:6144\n" \
    "ds_read_b64_tr_b16 v[250:251], v45 offset:6912\n" \
    "s_waitcnt lgkmcnt(0)\n" \
    "s_waitcnt vmcnt(12)\n" \
    "s_barrier\n" \
    "ds_read_b64_tr_b16 v[84:85], v24\n" \
    "ds_read_b64_tr_b16 v[86:87], v24 offset:768\n" \
    "ds_read_b64_tr_b16 v[88:89], v24 offset:6144\n" \
    "ds_read_b64_tr_b16 v[90:91], v24 offset:6912\n" \
    "s_waitcnt lgkmcnt(2)\n" \
    "ds_write_b128 %[vres], v[84:87]\n" \
    "s_waitcnt lgkmcnt(1)\n" \
    "ds_write_b128 %[vres], v[88:91] offset:1024\n" \
    "ds_read_b64_tr_b16 v[84:85], v25\n" \
    "ds_read_b64_tr_b16 v[86:87], v25 offset:768\n" \
    "ds_read_b64_tr_b16 v[88:89], v25 offset:6144\n" \
    "ds_read_b64_tr_b16 v[90:91], v25 offset:6912\n" \
    "s_waitcnt lgkmcnt(2)\n" \
    "ds_write_b128 %[vres], v[84:87] offset:2048\n" \
    "s_waitcnt lgkmcnt(1)\n" \
    "ds_write_b128 %[vres], v[88:91] offset:3072\n" \
    "ds_read_b64_tr_b16 v[84:85], v26\n" \
    "ds_read_b64_tr_b16 v[86:87], v26 offset:768\n" \
    "ds_read_b64_tr_b16 v[88:89], v26 offset:6144\n" \
    "ds_read_b64_tr_b16 v[90:91], v26 offset:6912\n" \
    "s_waitcnt lgkmcnt(2)\n" \
    "ds_write_b128 %[vres], v[84:87] offset:4096\n" \
    "s_waitcnt lgkmcnt(1)\n" \
    "ds_write_b128 %[vres], v[88:91] offset:5120\n" \
    "s_waitcnt lgkmcnt(0)\n" \
    "s_waitcnt vmcnt(6)\n" \
    "s_barrier\n" \
    "s_mov_b32 s67, 7077888\n" \
    "s_add_u32 m0, s70, 0\n" \
    "s_nop 0\n" \
    "buffer_load_dwordx4 v16, %[w0], s67 offen lds\n" \
    "s_add_u32 m0, s70, 1024\n" \
    "s_nop 0\n" \
    "buffer_load_dwordx4 v17, %[w0], s67 offen lds\n" \
    "s_add_u32 m0, s70, 2048\n" \
    "s_nop 0\n" \
    "buffer_load_dwordx4 v18, %[w0], s67 offen lds\n" \
    "s_add_u32 m0, s70, 12288\n" \
    "s_nop 0\n" \
    "buffer_load_dwordx4 v20, %[w1], s67 offen lds\n" \
    "s_add_u32 m0, s70, 13312\n" \
    "s_nop 0\n" \
    "buffer_load_dwordx4 v21, %[w1], s67 offen lds\n" \
    "s_add_u32 m0, s70, 14336\n" \
    "s_nop 0\n" \
    "buffer_load_dwordx4 v22, %[w1], s67 offen lds\n" \
    "ds_read_b64_tr_b16 v[84:85], v24 offset:24576\n" \
    "ds_read_b64_tr_b16 v[86:87], v24 offset:25344\n" \
    "ds_read_b64_tr_b16 v[88:89], v24 offset:30720\n" \
    "ds_read_b64_tr_b16 v[90:91], v24 offset:31488\n" \
    "s_waitcnt lgkmcnt(2)\n" \
    "ds_write_b128 %[vres], v[84:87] offset:6144\n" \
    "s_waitcnt lgkmcnt(1)\n" \
    "ds_write_b128 %[vres], v[88:91] offset:7168\n" \
    "ds_read_b64_tr_b16 v[84:85], v25 offset:24576\n" \
    "ds_read_b64_tr_b16 v[86:87], v25 offset:25344\n" \
    "ds_read_b64_tr_b16 v[88:89], v25 offset:30720\n" \
    "ds_read_b64_tr_b16 v[90:91], v25 offset:31488\n" \
    "s_waitcnt lgkmcnt(2)\n" \
    "ds_write_b128 %[vres], v[84:87] offset:8192\n" \
    "s_waitcnt lgkmcnt(1)\n" \
    "ds_write_b128 %[vres], v[88:91] offset:9216\n" \
    "ds_read_b64_tr_b16 v[84:85], v26 offset:24576\n" \
    "ds_read_b64_tr_b16 v[86:87], v26 offset:25344\n" \
    "ds_read_b64_tr_b16 v[88:89], v26 offset:30720\n" \
    "ds_read_b64_tr_b16 v[90:91], v26 offset:31488\n" \
    "s_waitcnt lgkmcnt(2)\n" \
    "ds_write_b128 %[vres], v[84:87] offset:10240\n" \
    "s_waitcnt lgkmcnt(1)\n" \
    "ds_write_b128 %[vres], v[88:91] offset:11264\n" \
    "s_waitcnt lgkmcnt(0)\n" \
    "s_waitcnt vmcnt(6)\n" \
    "s_barrier\n" \
    "ds_read_b64_tr_b16 v[84:85], v24 offset:49152\n" \
    "ds_read_b64_tr_b16 v[86:87], v24 offset:49920\n" \
    "ds_read_b64_tr_b16 v[88:89], v24 offset:55296\n" \
    "ds_read_b64_tr_b16 v[90:91], v24 offset:56064\n" \
    "s_waitcnt lgkmcnt(2)\n" \
    "ds_write_b128 %[vres], v[84:87] offset:12288\n" \
    "s_waitcnt lgkmcnt(1)\n" \
    "ds_write_b128 %[vres], v[88:91] offset:13312\n" \
    "ds_read_b64_tr_b16 v[84:85], v25 offset:49152\n" \
    "ds_read_b64_tr_b16 v[86:87], v25 offset:49920\n" \
    "ds_read_b64_tr_b16 v[88:89], v25 offset:55296\n" \
    "ds_read_b64_tr_b16 v[90:91], v25 offset:56064\n" \
    "s_waitcnt lgkmcnt(2)\n" \
    "ds_write_b128 %[vres], v[84:87] offset:14336\n" \
    "s_waitcnt lgkmcnt(1)\n" \
    "ds_write_b128 %[vres], v[88:91] offset:15360\n" \
    "ds_read_b64_tr_b16 v[84:85], v26 offset:49152\n" \
    "ds_read_b64_tr_b16 v[86:87], v26 offset:49920\n" \
    "ds_read_b64_tr_b16 v[88:89], v26 offset:55296\n" \
    "ds_read_b64_tr_b16 v[90:91], v26 offset:56064\n" \
    "s_waitcnt lgkmcnt(2)\n" \
    "ds_write_b128 %[vres], v[84:87] offset:16384\n" \
    "s_waitcnt lgkmcnt(1)\n" \
    "ds_write_b128 %[vres], v[88:91] offset:17408\n" \
    "s_waitcnt lgkmcnt(0)\n" \
    "s_waitcnt vmcnt(0)\n" \
    "s_barrier\n" \
    "ds_read_b64_tr_b16 v[148:149], v24\n" \
    "ds_read_b64_tr_b16 v[150:151], v24 offset:768\n" \
    "ds_read_b64_tr_b16 v[152:153], v24 offset:6144\n" \
    "ds_read_b64_tr_b16 v[154:155], v24 offset:6912\n" \
    "ds_read_b64_tr_b16 v[84:85], v25\n" \
    "ds_read_b64_tr_b16 v[86:87], v25 offset:768\n" \
    "ds_read_b64_tr_b16 v[88:89], v25 offset:6144\n" \
    "ds_read_b64_tr_b16 v[90:91], v25 offset:6912\n" \
    "s_waitcnt lgkmcnt(2)\n" \
    "ds_write_b128 %[vres], v[84:87] offset:18432\n" \
    "s_waitcnt lgkmcnt(1)\n" \
    "ds_write_b128 %[vres], v[88:91] offset:19456\n" \
    "ds_read_b64_tr_b16 v[236:237], v26\n" \
    "ds_read_b64_tr_b16 v[238:239], v26 offset:768\n" \
    "ds_read_b64_tr_b16 v[240:241], v26 offset:6144\n" \
    "ds_read_b64_tr_b16 v[242:243], v26 offset:6912\n" \
    "s_waitcnt lgkmcnt(0)\n" \
    "s_barrier\n" \
    "s_mov_b32 s60, 0\n" \
    "s_mov_b32 s61, 0\n" \
    "s_mov_b32 s66, 226492416\n" \
    "s_mov_b32 s75, 2200\n" \
    "s_mov_b32 s76, 0\n" \
    "v_readfirstlane_b32 s59, v3\n" \
    "s_mov_b32 s68, 0x3dd53b95\n" \
    "s_mov_b32 s69, 0xbdd53b95\n" \
    "Lqk_step:\n" \
    "s_nop 1\n" \
    "s_cmp_lt_u32 s60, s61\n" \
    "s_cbranch_scc0 Lqk_kwait\n" \
    "Lqk_kok:\n" \
    "s_lshl_b32 s64, s60, 12\n" \
    "s_and_b32 s64, s64, 0x2000\n" \
    "s_xor_b32 s64, s64, %[par]\n" \
    "s_lshl_b32 s79, s60, 20\n" \
    "s_add_u32 s64, s64, s79\n" \
    "s_add_u32 s64, s64, 75497472\n" \
    "buffer_load_dwordx4 v[72:75], v8, %[ws], s64 offen sc1\n" \
    "buffer_load_dwordx4 v[76:79], v8, %[ws], s64 offen offset:1024 sc1\n" \
    "buffer_load_dwordx4 v[80:83], v8, %[ws], s64 offen offset:2048 sc1\n" \
    "s_add_u32 s72, s60, 1\n" \
    "s_min_u32 s72, s72, 63\n" \
    "s_lshl_b32 s65, s72, 12\n" \
    "s_and_b32 s65, s65, 0x2000\n" \
    "s_xor_b32 s65, s65, %[par]\n" \
    "s_lshl_b32 s79, s72, 20\n" \
    "s_add_u32 s65, s65, s79\n" \
    "s_add_u32 s65, s65, 75497472\n" \
    "s_cmp_eq_u32 s60, 63\n" \
    "s_cselect_b32 s71, 1, %[wave]\n" \
    "v_and_b32 v67, 1023, v8\n" \
    "v_sub_u32 v66, v8, v67\n" \
    "v_min_u32 v67, 368, v67\n" \
    "v_lshl_add_u32 v66, v67, 3, v66\n" \
    "s_lshl_b32 s62, s60, 8\n" \
    "s_and_b32 s62, s62, 0x2000\n" \
    "s_xor_b32 s62, s62, %[par]\n" \
    "s_lshl_b32 s79, s60, 16\n" \
    "s_add_u32 s62, s62, s79\n" \
    "v_mov_b64 v[40:41], 0\n" \
    "v_mov_b64 v[42:43], 0\n" \
    "v_mov_b64 v[44:45], 0\n" \
    "v_mov_b64 v[46:47], 0\n" \
    "v_mov_b64 v[48:49], 0\n" \
    "v_mov_b64 v[50:51], 0\n" \
    "ds_read_b128 v[84:87], %[vres]\n" \
    "ds_read_b128 v[88:91], %[vres] offset:1024\n" \
    "ds_read_b128 v[92:95], %[vres] offset:2048\n" \
    "ds_read_b128 v[96:99], %[vres] offset:3072\n" \
    "ds_read_b128 v[100:103], %[vres] offset:4096\n" \
    "ds_read_b128 v[104:107], %[vres] offset:5120\n" \
    "s_add_u32 s72, s60, 1\n" \
    "s_lshl_b32 s72, s72, 1\n" \
    "v_mov_b32 v254, s72\n" \
    "s_mov_b32 m0, s59\n" \
    "s_lshr_b32 s72, s76, 3\n" \
    "v_mov_b32 v68, s72\n" \
    "s_waitcnt lgkmcnt(0)\n" \
    "s_lshl_b32 s79, s60, 1\n" \
    "s_cmp_lg_u32 %[wave], 1\n" \
    "s_cbranch_scc1 Lqk_dbs\n" \
    "s_mov_b32 s63, 1048576\n" \
    "s_branch Lqk_dbx\n" \
    "Lqk_dbw:\n" \
    "s_sub_u32 s63, s63, 1\n" \
    "s_cbranch_scc1 Lqk_tmo\n" \
    "buffer_load_dwordx2 v[54:55], v252, %[ws], s99 offen sc1\n" \
    "Lqk_dbx:\n" \
    "s_memrealtime s[96:97]\n" \
    "s_waitcnt vmcnt(0) lgkmcnt(0)\n" \
    "v_cmp_gt_u32 vcc, s79, v54\n" \
    "s_cbranch_vccnz Lqk_dbw\n" \
    "s_mul_i32 s96, s96, 24\n" \
    "v_and_b32 v54, 15, v55\n" \
    "v_lshl_add_u32 v55, v54, 5, v55\n" \
    "v_subrev_u32 v55, s96, v55\n" \
    "s_nop 1\n" \
    "v_max_i32_dpp v55, v55, v55 row_ror:8 row_mask:0xf bank_mask:0xf\n" \
    "s_nop 1\n" \
    "v_max_i32_dpp v55, v55, v55 row_ror:4 row_mask:0xf bank_mask:0xf\n" \
    "s_nop 1\n" \
    "v_max_i32_dpp v55, v55, v55 row_ror:2 row_mask:0xf bank_mask:0xf\n" \
    "s_nop 1\n" \
    "v_max_i32_dpp v55, v55, v55 row_ror:1 row_mask:0xf bank_mask:0xf\n" \
    "s_nop 1\n" \
    "v_readlane_b32 s97, v55, 0\n" \
    "v_readlane_b32 s98, v55, 16\n" \
    "s_nop 3\n" \
    "s_max_i32 s97, s97, s98\n" \
    "s_add_i32 s97, s97, %[d0]\n" \
    "s_min_i32 s97, s97, 8000\n" \
    "s_cmp_eq_u32 s60, 0\n" \
    "s_cselect_b32 s97, 0, s97\n" \
    "s_add_u32 s98, s96, s97\n" \
    "Lqk_dbd:\n" \
    "s_memtime s[56:57]\n" \
    "s_memrealtime s[44:45]\n" \
    "s_nop 2\n" \
    "s_memrealtime s[46:47]\n" \
    "s_nop 2\n" \
    "s_memrealtime s[48:49]\n" \
    "s_nop 2\n" \
    "s_memrealtime s[50:51]\n" \
    "s_nop 2\n" \
    "s_memrealtime s[52:53]\n" \
    "s_nop 2\n" \
    "s_memrealtime s[54:55]\n" \
    "s_waitcnt lgkmcnt(0)\n" \
    "s_add_u32 s44, s44, s46\n" \
    "s_add_u32 s48, s48, s50\n" \
    "s_add_u32 s52, s52, s54\n" \
    "s_add_u32 s44, s44, s48\n" \
    "s_add_u32 s44, s44, s52\n" \
    "s_lshl_b32 s96, s44, 2\n" \
    "s_sub_u32 s58, s96, s56\n" \
    "s_sub_i32 s97, s98, s96\n" \
    "s_cmp_gt_i32 s97, 420\n" \
    "s_cbranch_scc1 Lqk_dbd\n" \
    ".p2align 4\n" \
    "Lqk_dbf:\n" \
    "s_sub_i32 s97, s97, 28\n" \
    "s_cmpk_gt_i32 s97, 200\n" \
    "s_cbranch_scc1 Lqk_dbf\n" \
    "Lqk_dbs:\n" \
    "s_barrier\n" \
    "buffer_load_dwordx4 v[16:19], v2, %[ws], s62 offen sc1\n" \
    "buffer_load_dwordx4 v[20:23], v2, %[ws], s62 offen offset:1024 sc1\n" \
    "s_mov_b32 s63, 1048575\n" \
    "s_cmp_lt_u32 %[wave], 2\n" \
    "s_cbranch_scc1 Lqk_q3\n" \
    "s_branch Lqk_q2\n" \
    "Lqk_p2:\n" \
    "s_sub_u32 s63, s63, 1\n" \
    "s_cbranch_scc1 Lqk_tmo\n" \
    "buffer_load_dwordx4 v[16:19], v2, %[ws], s62 offen sc1\n" \
    "buffer_load_dwordx4 v[20:23], v2, %[ws], s62 offen offset:1024 sc1\n" \
    "Lqk_q2:\n" \
    "s_waitcnt vmcnt(1)\n" \
    "ds_write_b128 v3, v[16:19]\n" \
    "v_max_u32 v52, v16, v17\n" \
    "v_max3_u32 v52, v52, v18, v19\n" \
    "s_waitcnt vmcnt(0)\n" \
    "ds_write_b128 v3, v[20:23] offset:1024\n" \
    "v_max3_u32 v52, v52, v20, v21\n" \
    "v_max3_u32 v52, v52, v22, v23\n" \
    "v_cmp_eq_u32 vcc, -1, v52\n" \
    "s_cbranch_vccnz Lqk_p2\n" \
    "s_branch Lqk_got\n" \
    "Lqk_p3:\n" \
    "s_sub_u32 s63, s63, 1\n" \
    "s_cbranch_scc1 Lqk_tmo\n" \
    "buffer_load_dwordx4 v[16:19], v2, %[ws], s62 offen sc1\n" \
    "buffer_load_dwordx4 v[20:23], v2, %[ws], s62 offen offset:1024 sc1\n" \
    "Lqk_q3:\n" \
    "buffer_load_dwordx4 v[24:27], v2, %[ws], s62 offen offset:2048 sc1\n" \
    "s_waitcnt vmcnt(2)\n" \
    "ds_write_b128 v3, v[16:19]\n" \
    "v_max_u32 v52, v16, v17\n" \
    "v_max3_u32 v52, v52, v18, v19\n" \
    "s_waitcnt vmcnt(1)\n" \
    "ds_write_b128 v3, v[20:23] offset:1024\n" \
    "v_max3_u32 v52, v52, v20, v21\n" \
    "v_max3_u32 v52, v52, v22, v23\n" \
    "s_waitcnt vmcnt(0)\n" \
    "ds_write_b128 v3, v[24:27] offset:2048\n" \
    "v_max3_u32 v52, v52, v24, v25\n" \
    "v_max3_u32 v52, v52, v26, v27\n" \
    "v_cmp_eq_u32 vcc, -1, v52\n" \
    "s_cbranch_vccnz Lqk_p3\n" \
    "Lqk_got:\n" \
    "s_waitcnt lgkmcnt(0)\n" \
    "s_barrier\n" \
    "s_memtime s[96:97]\n" \
    "ds_read_b64_tr_b16 v[16:17], v4\n" \
    "ds_read_b64_tr_b16 v[18:19], v4 offset:32\n" \
    "ds_read_b64_tr_b16 v[20:21], v4 offset:512\n" \
    "ds_read_b64_tr_b16 v[22:23], v4 offset:544\n" \
    "s_waitcnt lgkmcnt(0)\n" \
    "ds_read_b64_tr_b16 v[24:25], v4 offset:1024\n" \
    "ds_read_b64_tr_b16 v[26:27], v4 offset:1056\n" \
    "ds_read_b64_tr_b16 v[28:29], v4 offset:1536\n" \
    "ds_read_b64_tr_b16 v[30:31], v4 offset:1568\n" \
    "v_smfmac_f32_16x16x64_bf16 v[40:43], v[16:19], a[0:7], v5\n" \
    "ds_read_b64_tr_b16 v[32:33], v4 offset:2048\n" \
    "ds_read_b64_tr_b16 v[34:35], v4 offset:2080\n" \
    "v_smfmac_f32_16x16x64_bf16 v[44:47], v[16:19], a[8:15], v5\n" \
    "s_add_u32 s74, s96, s58\n" \
    "s_add_u32 s73, s74, s75\n" \
    "v_smfmac_f32_16x16x64_bf16 v[48:51], v[16:19], a[16:23], v5\n" \
    "v_and_or_b32 v255, s73, -16, v68\n" \
    "buffer_store_dwordx2 v[254:255], v253, %[ws], s99 offen sc1\n" \
    "v_smfmac_f32_16x16x64_bf16 v[40:43], v[20:23], a[24:31], v5\n" \
    "ds_read_b64_tr_b16 v[36:37], v4 offset:2560\n" \
    "ds_read_b64_tr_b16 v[38:39], v4 offset:2592\n" \
    "v_smfmac_f32_16x16x64_bf16 v[44:47], v[20:23], a[32:39], v5\n" \
    "v_smfmac_f32_16x16x64_bf16 v[48:51], v[20:23], a[40:47], v5\n" \
    "buffer_store_dwordx4 v[60:63], v9, %[ws], s66 offen sc1\n" \
    "s_waitcnt lgkmcnt(6)\n" \
    "v_smfmac_f32_16x16x64_bf16 v[40:43], v[24:27], v[84:91], v5\n" \
    "ds_read_b64_tr_b16 v[16:17], v4 offset:3072\n" \
    "ds_read_b64_tr_b16 v[18:19], v4 offset:3104\n" \
    "v_smfmac_f32_16x16x64_bf16 v[44:47], v[24:27], v[92:99], v5\n" \
    "v_smfmac_f32_16x16x64_bf16 v[48:51], v[24:27], v[100:107], v5\n" \
    "s_waitcnt lgkmcnt(6)\n" \
    "v_smfmac_f32_16x16x64_bf16 v[40:43], v[28:31], a[48:55], v5\n" \
    "ds_read_b64_tr_b16 v[20:21], v4 offset:3584\n" \
    "ds_read_b64_tr_b16 v[22:23], v4 offset:3616\n" \
    "v_smfmac_f32_16x16x64_bf16 v[44:47], v[28:31], a[56:63], v5\n" \
    "ds_read_b128 v[84:87], %[vres] offset:6144\n" \
    "ds_read_b128 v[88:91], %[vres] offset:7168\n" \
    "v_smfmac_f32_16x16x64_bf16 v[48:51], v[28:31], a[64:71], v5\n" \
    "buffer_store_dwordx2 v[64:65], v9, %[ws], s66 offen offset:512 sc1\n" \
    "s_waitcnt lgkmcnt(8)\n" \
    "v_smfmac_f32_16x16x64_bf16 v[40:43], v[32:35], a[72:79], v5\n" \
    "ds_read_b64_tr_b16 v[24:25], v4 offset:4096\n" \
    "ds_read_b64_tr_b16 v[26:27], v4 offset:4128\n" \
    "v_smfmac_f32_16x16x64_bf16 v[44:47], v[32:35], a[80:87], v5\n" \
    "ds_read_b128 v[92:95], %[vres] offset:8192\n" \
    "ds_read_b128 v[96:99], %[vres] offset:9216\n" \
    "v_smfmac_f32_16x16x64_bf16 v[48:51], v[32:35], a[88:95], v5\n" \
    "s_waitcnt lgkmcnt(10)\n" \
    "v_smfmac_f32_16x16x64_bf16 v[40:43], v[36:39], a[96:103], v5\n" \
    "ds_read_b64_tr_b16 v[28:29], v4 offset:4608\n" \
    "ds_read_b64_tr_b16 v[30:31], v4 offset:4640\n" \
    "v_smfmac_f32_16x16x64_bf16 v[44:47], v[36:39], a[104:111], v5\n" \
    "s_waitcnt lgkmcnt(8)\n" \
    "ds_read_b128 v[100:103], %[vres] offset:10240\n" \
    "ds_read_b128 v[104:107], %[vres] offset:11264\n" \
    "v_smfmac_f32_16x16x64_bf16 v[48:51], v[36:39], a[112:119], v5\n" \
    "buffer_load_dword v255, v66, %[ws], s65 offen sc1\n" \
    "v_smfmac_f32_16x16x64_bf16 v[40:43], v[16:19], a[120:127], v5\n" \
    "ds_read_b64_tr_b16 v[32:33], v4 offset:5120\n" \
    "ds_read_b64_tr_b16 v[34:35], v4 offset:5152\n" \
    "v_smfmac_f32_16x16x64_bf16 v[44:47], v[16:19], a[128:135], v5\n" \
    "v_smfmac_f32_16x16x64_bf16 v[48:51], v[16:19], a[136:143], v5\n" \
    "s_waitcnt lgkmcnt(10)\n" \
    "v_smfmac_f32_16x16x64_bf16 v[40:43], v[20:23], v[84:91], v5\n" \
    "ds_read_b64_tr_b16 v[36:37], v4 offset:5632\n" \
    "ds_read_b64_tr_b16 v[38:39], v4 offset:5664\n" \
    "s_waitcnt lgkmcnt(8)\n" \
    "v_smfmac_f32_16x16x64_bf16 v[44:47], v[20:23], v[92:99], v5\n" \
    "s_waitcnt lgkmcnt(4)\n" \
    "v_smfmac_f32_16x16x64_bf16 v[48:51], v[20:23], v[100:107], v5\n" \
    "v_smfmac_f32_16x16x64_bf16 v[40:43], v[24:27], a[144:151], v5\n" \
    "ds_read_b64_tr_b16 v[16:17], v4 offset:6144\n" \
    "ds_read_b64_tr_b16 v[18:19], v4 offset:6176\n" \
    "v_smfmac_f32_16x16x64_bf16 v[44:47], v[24:27], a[152:159], v5\n" \
    "ds_read_b128 v[84:87], %[vres] offset:12288\n" \
    "ds_read_b128 v[88:91], %[vres] offset:13312\n" \
    "v_smfmac_f32_16x16x64_bf16 v[48:51], v[24:27], a[160:167], v5\n" \
    "v_smfmac_f32_16x16x64_bf16 v[40:43], v[28:31], a[168:175], v5\n" \
    "ds_read_b64_tr_b16 v[20:21], v4 offset:6656\n" \
    "ds_read_b64_tr_b16 v[22:23], v4 offset:6688\n" \
    "v_smfmac_f32_16x16x64_bf16 v[44:47], v[28:31], a[176:183], v5\n" \
    "ds_read_b128 v[92:95], %[vres] offset:14336\n" \
    "ds_read_b128 v[96:99], %[vres] offset:15360\n" \
    "v_smfmac_f32_16x16x64_bf16 v[48:51], v[28:31], a[184:191], v5\n" \
    "s_waitcnt lgkmcnt(10)\n" \
    "v_smfmac_f32_16x16x64_bf16 v[40:43], v[32:35], a[192:199], v5\n" \
    "ds_read_b64_tr_b16 v[24:25], v4 offset:7168\n" \
    "ds_read_b64_tr_b16 v[26:27], v4 offset:7200\n" \
    "v_smfmac_f32_16x16x64_bf16 v[44:47], v[32:35], a[200:207], v5\n" \
    "s_waitcnt lgkmcnt(8)\n" \
    "ds_read_b128 v[100:103], %[vres] offset:16384\n" \
    "ds_read_b128 v[104:107], %[vres] offset:17408\n" \
    "v_smfmac_f32_16x16x64_bf16 v[48:51], v[32:35], a[208:215], v5\n" \
    "v_smfmac_f32_16x16x64_bf16 v[40:43], v[36:39], a[216:223], v5\n" \
    "ds_read_b64_tr_b16 v[28:29], v4 offset:7680\n" \
    "ds_read_b64_tr_b16 v[30:31], v4 offset:7712\n" \
    "v_smfmac_f32_16x16x64_bf16 v[44:47], v[36:39], a[224:231], v5\n" \
    "v_smfmac_f32_16x16x64_bf16 v[48:51], v[36:39], a[232:239], v5\n" \
    "s_waitcnt lgkmcnt(8)\n" \
    "ds_read_b64_tr_b16 v[32:33], v4 offset:8192\n" \
    "ds_read_b64_tr_b16 v[34:35], v4 offset:8224\n" \
    "v_smfmac_f32_16x16x64_bf16 v[40:43], v[16:19], v[84:91], v5\n" \
    "ds_read_b64_tr_b16 v[36:37], v4 offset:8704\n" \
    "ds_read_b64_tr_b16 v[38:39], v4 offset:8736\n" \
    "s_waitcnt lgkmcnt(10)\n" \
    "v_smfmac_f32_16x16x64_bf16 v[44:47], v[16:19], v[92:99], v5\n" \
    "s_waitcnt lgkmcnt(6)\n" \
    "v_smfmac_f32_16x16x64_bf16 v[48:51], v[16:19], v[100:107], v5\n" \
    "ds_read_b64_tr_b16 v[16:17], v4 offset:9216\n" \
    "ds_read_b64_tr_b16 v[18:19], v4 offset:9248\n" \
    "v_smfmac_f32_16x16x64_bf16 v[40:43], v[20:23], a[240:247], v5\n" \
    "v_smfmac_f32_16x16x64_bf16 v[44:47], v[20:23], a[248:255], v5\n" \
    "ds_read_b128 v[84:87], %[vres] offset:18432\n" \
    "ds_read_b128 v[88:91], %[vres] offset:19456\n" \
    "v_smfmac_f32_16x16x64_bf16 v[48:51], v[20:23], v[108:115], v5\n" \
    "ds_read_b64_tr_b16 v[20:21], v4 offset:9728\n" \
    "ds_read_b64_tr_b16 v[22:23], v4 offset:9760\n" \
    "v_smfmac_f32_16x16x64_bf16 v[40:43], v[24:27], v[116:123], v5\n" \
    "s_waitcnt lgkmcnt(10)\n" \
    "v_smfmac_f32_16x16x64_bf16 v[40:43], v[28:31], v[124:131], v5\n" \
    "s_waitcnt lgkmcnt(8)\n" \
    "v_smfmac_f32_16x16x64_bf16 v[40:43], v[32:35], v[132:139], v5\n" \
    "s_waitcnt lgkmcnt(6)\n" \
    "v_smfmac_f32_16x16x64_bf16 v[40:43], v[36:39], v[140:147], v5\n" \
    "s_waitcnt lgkmcnt(4)\n" \
    "v_smfmac_f32_16x16x64_bf16 v[40:43], v[16:19], v[148:155], v5\n" \
    "s_waitcnt lgkmcnt(0)\n" \
    "v_smfmac_f32_16x16x64_bf16 v[40:43], v[20:23], v[156:163], v5\n" \
    "s_nop 1\n" \
    "v_smfmac_f32_16x16x64_bf16 v[44:47], v[24:27], v[164:171], v5\n" \
    "s_nop 1\n" \
    "v_smfmac_f32_16x16x64_bf16 v[44:47], v[28:31], v[172:179], v5\n" \
    "s_nop 1\n" \
    "v_smfmac_f32_16x16x64_bf16 v[44:47], v[32:35], v[180:187], v5\n" \
    "v_cvt_pk_bf16_f32 v40, v40, v41\n" \
    "v_cvt_pk_bf16_f32 v41, v42, v43\n" \
    "v_smfmac_f32_16x16x64_bf16 v[44:47], v[36:39], v[188:195], v5\n" \
    "s_nop 0\n" \
    "v_mfma_f32_16x16x16_bf16 v[52:55], v[40:41], v[6:7], 0\n" \
    "s_nop 1\n" \
    "v_smfmac_f32_16x16x64_bf16 v[44:47], v[16:19], v[84:91], v5\n" \
    "s_nop 1\n" \
    "v_smfmac_f32_16x16x64_bf16 v[44:47], v[20:23], v[196:203], v5\n" \
    "s_nop 1\n" \
    "v_smfmac_f32_16x16x64_bf16 v[48:51], v[24:27], v[204:211], v5\n" \
    "v_cvt_pk_bf16_f32 v42, v52, v53\n" \
    "v_cvt_pk_bf16_f32 v43, v54, v55\n" \
    "v_smfmac_f32_16x16x64_bf16 v[48:51], v[28:31], v[212:219], v5\n" \
    "s_nop 0\n" \
    "v_mfma_f32_16x16x16_bf16 v[60:63], v[42:43], v[42:43], 0\n" \
    "v_mfma_f32_16x16x16_bf16 v[64:67], v[72:73], v[42:43], 0\n" \
    "v_mfma_f32_16x16x16_bf16 v[68:71], v[74:75], v[42:43], 0\n" \
    "v_cvt_pk_bf16_f32 v44, v44, v45\n" \
    "v_cvt_pk_bf16_f32 v45, v46, v47\n" \
    "v_smfmac_f32_16x16x64_bf16 v[48:51], v[32:35], v[220:227], v5\n" \
    "s_nop 0\n" \
    "v_mfma_f32_16x16x16_bf16 v[56:59], v[44:45], v[6:7], 0\n" \
    "s_nop 1\n" \
    "v_smfmac_f32_16x16x64_bf16 v[48:51], v[36:39], v[228:235], v5\n" \
    "s_nop 1\n" \
    "v_smfmac_f32_16x16x64_bf16 v[48:51], v[16:19], v[236:243], v5\n" \
    "s_nop 1\n" \
    "v_smfmac_f32_16x16x64_bf16 v[48:51], v[20:23], v[244:251], v5\n" \
    "v_cvt_pk_bf16_f32 v46, v56, v57\n" \
    "v_cvt_pk_bf16_f32 v47, v58, v59\n" \
    "s_nop 1\n" \
    "v_mfma_f32_16x16x16_bf16 v[60:63], v[46:47], v[46:47], v[60:63]\n" \
    "v_mfma_f32_16x16x16_bf16 v[64:67], v[76:77], v[46:47], v[64:67]\n" \
    "v_mfma_f32_16x16x16_bf16 v[68:71], v[78:79], v[46:47], v[68:71]\n" \
    "s_nop 0\n" \
    "v_cvt_pk_bf16_f32 v48, v48, v49\n" \
    "v_cvt_pk_bf16_f32 v49, v50, v51\n" \
    "s_nop 1\n" \
    "v_mfma_f32_16x16x16_bf16 v[52:55], v[48:49], v[6:7], 0\n" \
    "s_nop 7\n" \
    "v_cvt_pk_bf16_f32 v50, v52, v53\n" \
    "v_cvt_pk_bf16_f32 v51, v54, v55\n" \
    "s_nop 1\n" \
    "v_mfma_f32_16x16x16_bf16 v[60:63], v[50:51], v[50:51], v[60:63]\n" \
    "v_mfma_f32_16x16x16_bf16 v[64:67], v[80:81], v[50:51], v[64:67]\n" \
    "v_mfma_f32_16x16x16_bf16 v[68:71], v[82:83], v[50:51], v[68:71]\n" \
    "s_cmp_lg_u32 s71, 0\n" \
    "s_nop 4\n" \
    "ds_write_b128 v10, v[60:63]\n" \
    "ds_write_b128 v11, v[64:67]\n" \
    "ds_write_b128 v11, v[68:71] offset:32\n" \
    "s_waitcnt lgkmcnt(0)\n" \
    "s_barrier\n" \
    "s_cbranch_scc1 Lqk_b3\n" \
    "ds_read_b128 v[84:87], v12\n" \
    "ds_read_b128 v[88:91], v12 offset:1280\n" \
    "ds_read2st64_b32 v[100:101], v13 offset0:0 offset1:10\n" \
    "ds_read_b128 v[92:95], v12 offset:2560\n" \
    "ds_read_b128 v[96:99], v12 offset:3840\n" \
    "ds_read2st64_b32 v[102:103], v13 offset0:5 offset1:15\n" \
    "s_waitcnt lgkmcnt(4)\n" \
    "v_pk_add_f32 v[84:85], v[84:85], v[88:89]\n" \
    "v_pk_add_f32 v[86:87], v[86:87], v[90:91]\n" \
    "s_waitcnt lgkmcnt(1)\n" \
    "v_pk_add_f32 v[92:93], v[92:93], v[96:97]\n" \
    "v_pk_add_f32 v[94:95], v[94:95], v[98:99]\n" \
    "s_waitcnt lgkmcnt(0)\n" \
    "v_pk_add_f32 v[100:101], v[100:101], v[102:103]\n" \
    "v_pk_add_f32 v[84:85], v[84:85], v[92:93]\n" \
    "v_add_f32 v100, v100, v101\n" \
    "v_pk_add_f32 v[86:87], v[86:87], v[94:95]\n" \
    "v_max3_f32 v88, v84, v85, v100\n" \
    "v_max3_f32 v88, v88, v86, v87\n" \
    "v_mul_f32 v92, s69, v88\n" \
    "v_fma_f32 v84, v84, s68, v92\n" \
    "v_max_f32_dpp v89, v88, v88 quad_perm:[1,0,3,2] row_mask:0xf bank_mask:0xf\n" \
    "v_fma_f32 v85, v85, s68, v92\n" \
    "v_exp_f32 v84, v84\n" \
    "v_max_f32_dpp v89, v89, v89 quad_perm:[2,3,0,1] row_mask:0xf bank_mask:0xf\n" \
    "v_fma_f32 v86, v86, s68, v92\n" \
    "v_exp_f32 v85, v85\n" \
    "v_max_f32_dpp v89, v89, v89 row_half_mirror row_mask:0xf bank_mask:0xf\n" \
    "v_fma_f32 v87, v87, s68, v92\n" \
    "v_exp_f32 v86, v86\n" \
    "v_fma_f32 v90, v89, s69, -v92\n" \
    "v_exp_f32 v87, v87\n" \
    "v_fma_f32 v100, v100, s68, v92\n" \
    "v_exp_f32 v90, v90\n" \
    "v_add_f32 v94, v84, v85\n" \
    "v_exp_f32 v100, v100\n" \
    "v_add_f32 v93, v86, v87\n" \
    "v_add_f32 v94, v94, v100\n" \
    "v_add_f32 v93, v93, v94\n" \
    "v_mul_f32 v93, v93, v90\n" \
    "v_mul_f32 v100, v100, v90\n" \
    "v_pk_mul_f32 v[84:85], v[84:85], v[90:91] op_sel_hi:[1,0]\n" \
    "v_add_f32_dpp v93, v93, v93 quad_perm:[1,0,3,2] row_mask:0xf bank_mask:0xf\n" \
    "v_pk_mul_f32 v[86:87], v[86:87], v[90:91] op_sel_hi:[1,0]\n" \
    "v_mov_b32_dpp v101, v100 quad_perm:[1,0,3,2] row_mask:0xf bank_mask:0xf\n" \
    "v_add_f32_dpp v93, v93, v93 quad_perm:[2,3,0,1] row_mask:0xf bank_mask:0xf\n" \
    "s_nop 1\n" \
    "v_add_f32_dpp v93, v93, v93 row_half_mirror row_mask:0xf bank_mask:0xf\n" \
    "v_rcp_f32 v90, v93\n" \
    "s_nop 0\n" \
    "v_pk_mul_f32 v[84:85], v[84:85], v[90:91] op_sel_hi:[1,0]\n" \
    "v_pk_mul_f32 v[86:87], v[86:87], v[90:91] op_sel_hi:[1,0]\n" \
    "v_cvt_pk_bf16_f32 v84, v84, v85\n" \
    "v_cvt_pk_bf16_f32 v85, v86, v87\n" \
    "v_min_u32 v16, -2, v84\n" \
    "v_min_u32 v17, -2, v85\n" \
    "buffer_store_dwordx2 v[16:17], v14, %[ws], s62 offen sc1\n" \
    "v_pk_mul_f32 v[100:101], v[100:101], v[90:91] op_sel_hi:[1,0]\n" \
    "v_cvt_pk_bf16_f32 v100, v100, v101\n" \
    "v_min_u32 v18, -2, v100\n" \
    "buffer_store_dword v18, v15, %[ws], s62 offen sc1\n" \
    "Lqk_b3:\n" \
    "s_cmp_lg_u32 %[wave], 1\n" \
    "s_cbranch_scc1 Lqk_b3a\n" \
    "buffer_load_dwordx2 v[54:55], v252, %[ws], s99 offen sc1\n" \
    "Lqk_b3a:\n" \
    "v_min_u32 v60, -2, v42\n" \
    "v_min_u32 v61, -2, v43\n" \
    "v_min_u32 v62, -2, v46\n" \
    "v_min_u32 v63, -2, v47\n" \
    "v_min_u32 v64, -2, v50\n" \
    "v_min_u32 v65, -2, v51\n" \
    "s_lshl_b32 s66, s60, 10\n" \
    "s_and_b32 s66, s66, 0x2000\n" \
    "s_xor_b32 s66, s66, %[par]\n" \
    "s_lshl_b32 s79, s60, 18\n" \
    "s_add_u32 s66, s66, s79\n" \
    "s_add_u32 s66, s66, 142606336\n" \
    "s_cmp_lt_u32 s60, 58\n" \
    "s_cbranch_scc1 Lqk_b4\n" \
    "buffer_store_dwordx4 v[60:63], v9, %[ws], s66 offen sc1\n" \
    "buffer_store_dwordx2 v[64:65], v9, %[ws], s66 offen offset:512 sc1\n" \
    "Lqk_b4:\n" \
    "s_barrier\n" \
    "s_waitcnt lgkmcnt(0)\n" \
    "s_memrealtime s[44:45]\n" \
    "s_nop 2\n" \
    "s_memrealtime s[46:47]\n" \
    "s_nop 2\n" \
    "s_memrealtime s[48:49]\n" \
    "s_nop 2\n" \
    "s_memrealtime s[50:51]\n" \
    "s_nop 2\n" \
    "s_memrealtime s[52:53]\n" \
    "s_nop 2\n" \
    "s_memrealtime s[54:55]\n" \
    "s_waitcnt lgkmcnt(0)\n" \
    "s_add_u32 s44, s44, s46\n" \
    "s_add_u32 s48, s48, s50\n" \
    "s_add_u32 s52, s52, s54\n" \
    "s_add_u32 s44, s44, s48\n" \
    "s_add_u32 s44, s44, s52\n" \
    "s_lshl_b32 s96, s44, 2\n" \
    "s_sub_u32 s97, s74, s98\n" \
    "s_sub_u32 s97, s97, 1250\n" \
    "s_cmp_lt_u32 s97, 900\n" \
    "s_cselect_b32 s97, 16, -2\n" \
    "s_cmp_lt_u32 s60, 5\n" \
    "s_cselect_b32 s97, 0, s97\n" \
    "s_add_i32 s76, s76, s97\n" \
    "s_max_i32 s76, s76, 0\n" \
    "s_min_i32 s76, s76, 127\n" \
    "s_sub_u32 s96, s96, s74\n" \
    "s_min_u32 s96, s96, 2900\n" \
    "s_sub_i32 s96, s96, s75\n" \
    "s_ashr_i32 s96, s96, 1\n" \
    "s_add_i32 s75, s75, s96\n" \
    "Lqk_next:\n" \
    "s_add_u32 s60, s60, 1\n" \
    "s_cmp_lt_u32 s60, 64\n" \
    "s_cbranch_scc1 Lqk_step\n" \
    "s_branch Lqk_end\n" \
    "Lqk_kwait:\n" \
    "s_mov_b32 s63, 1048576\n" \
    "v_mov_b32 v25, 0\n" \
    "Lqk_kw2:\n" \
    "s_sub_u32 s63, s63, 1\n" \
    "s_cbranch_scc1 Lqk_tmo\n" \
    "buffer_load_dword v24, v25, %[ws], %[flag] offen sc1\n" \
    "s_waitcnt vmcnt(0)\n" \
    "v_readfirstlane_b32 s61, v24\n" \
    "s_nop 3\n" \
    "s_cmp_lt_u32 s60, s61\n" \
    "s_cbranch_scc1 Lqk_kok\n" \
    "s_sleep 2\n" \
    "s_branch Lqk_kw2\n" \
    "Lqk_tmo:\n" \
    "s_add_u32 s78, s60, 512\n" \
    "v_mov_b32 v16, s78\n" \
    "v_mov_b32 v17, 0\n" \
    "s_mov_b32 s78, 253757696\n" \
    "s_nop 4\n" \
    "buffer_store_dword v16, v17, %[ws], s78 offen sc1\n" \
    "s_endpgm\n" \
    "Lqk_end:\n"
#define ASM_VO \
    "ds_read_b128 v[2:5], %[vres]\n" \
    "ds_read_b128 v[6:9], %[vres] offset:1024\n" \
    "ds_read_b128 v[12:15], %[vres] offset:2048\n" \
    "ds_read_b128 v[16:19], %[vres] offset:3072\n" \
    "ds_read_b128 v[36:39], %[vres] offset:4096\n" \
    "ds_read_b128 v[40:43], %[vres] offset:5120\n" \
    "ds_read_b128 v[44:47], %[vres] offset:6144\n" \
    "ds_read_b128 v[20:23], %[vres] offset:7168\n" \
    "ds_read_b128 v[24:27], %[vres] offset:8192\n" \
    "s_waitcnt lgkmcnt(0)\n" \
    "s_barrier\n" \
    "v_mov_b32 v11, v15\n" \
    "v_mov_b32 v252, v19\n" \
    "s_xor_b32 s99, %[par], 0x2000\n" \
    "s_add_u32 s99, s99, 6291456\n" \
    "s_mul_i32 s70, %[wave], 10240\n" \
    "s_mul_i32 s71, %[wave], 3072\n" \
    "v_add_u32 v68, 61440, v20\n" \
    "v_add_u32 v73, 122880, v20\n" \
    "v_add_u32 v69, 61440, v21\n" \
    "v_add_u32 v74, 122880, v21\n" \
    "v_add_u32 v70, 61440, v22\n" \
    "v_add_u32 v75, 122880, v22\n" \
    "v_add_u32 v71, 61440, v23\n" \
    "v_add_u32 v76, 122880, v23\n" \
    "v_add_u32 v72, 61440, v24\n" \
    "v_add_u32 v77, 122880, v24\n" \
    "v_add_u32 v28, 61440, v16\n" \
    "v_add_u32 v31, 122880, v16\n" \
    "v_add_u32 v29, 61440, v17\n" \
    "v_add_u32 v32, 122880, v17\n" \
    "v_add_u32 v30, 61440, v18\n" \
    "v_add_u32 v33, 122880, v18\n" \
    "s_mov_b32 s67, 0\n" \
    "s_add_u32 m0, s70, 0\n" \
    "s_nop 0\n" \
    "buffer_load_dwordx4 v36, %[w1], s67 offen lds\n" \
    "s_add_u32 m0, s70, 1024\n" \
    "s_nop 0\n" \
    "buffer_load_dwordx4 v37, %[w1], s67 offen lds\n" \
    "s_add_u32 m0, s70, 2048\n" \
    "s_nop 0\n" \
    "buffer_load_dwordx4 v38, %[w1], s67 offen lds\n" \
    "s_add_u32 m0, s70, 3072\n" \
    "s_nop 0\n" \
    "buffer_load_dwordx4 v39, %[w1], s67 offen lds\n" \
    "s_add_u32 m0, s70, 4096\n" \
    "s_nop 0\n" \
    "buffer_load_dwordx4 v40, %[w1], s67 offen lds\n" \
    "s_add_u32 m0, s70, 5120\n" \
    "s_nop 0\n" \
    "buffer_load_dwordx4 v41, %[w1], s67 offen lds\n" \
    "s_add_u32 m0, s70, 6144\n" \
    "s_nop 0\n" \
    "buffer_load_dwordx4 v42, %[w1], s67 offen lds\n" \
    "s_add_u32 m0, s70, 7168\n" \
    "s_nop 0\n" \
    "buffer_load_dwordx4 v43, %[w1], s67 offen lds\n" \
    "s_add_u32 m0, s70, 8192\n" \
    "s_nop 0\n" \
    "buffer_load_dwordx4 v44, %[w1], s67 offen lds\n" \
    "s_add_u32 m0, s70, 9216\n" \
    "s_nop 0\n" \
    "buffer_load_dwordx4 v45, %[w1], s67 offen lds\n" \
    "s_mov_b32 s67, 40960\n" \
    "s_add_u32 m0, s70, 40960\n" \
    "s_nop 0\n" \
    "buffer_load_dwordx4 v36, %[w1], s67 offen lds\n" \
    "s_add_u32 m0, s70, 41984\n" \
    "s_nop 0\n" \
    "buffer_load_dwordx4 v37, %[w1], s67 offen lds\n" \
    "s_add_u32 m0, s70, 43008\n" \
    "s_nop 0\n" \
    "buffer_load_dwordx4 v38, %[w1], s67 offen lds\n" \
    "s_add_u32 m0, s70, 44032\n" \
    "s_nop 0\n" \
    "buffer_load_dwordx4 v39, %[w1], s67 offen lds\n" \
    "s_add_u32 m0, s70, 45056\n" \
    "s_nop 0\n" \
    "buffer_load_dwordx4 v40, %[w1], s67 offen lds\n" \
    "s_add_u32 m0, s70, 46080\n" \
    "s_nop 0\n" \
    "buffer_load_dwordx4 v41, %[w1], s67 offen lds\n" \
    "s_add_u32 m0, s70, 47104\n" \
    "s_nop 0\n" \
    "buffer_load_dwordx4 v42, %[w1], s67 offen lds\n" \
    "s_add_u32 m0, s70, 48128\n" \
    "s_nop 0\n" \
    "buffer_load_dwordx4 v43, %[w1], s67 offen lds\n" \
    "s_add_u32 m0, s70, 49152\n" \
    "s_nop 0\n" \
    "buffer_load_dwordx4 v44, %[w1], s67 offen lds\n" \
    "s_add_u32 m0, s70, 50176\n" \
    "s_nop 0\n" \
    "buffer_load_dwordx4 v45, %[w1], s67 offen lds\n" \
    "s_mov_b32 s67, 81920\n" \
    "s_add_u32 m0, s70, 81920\n" \
    "s_nop 0\n" \
    "buffer_load_dwordx4 v36, %[w1], s67 offen lds\n" \
    "s_add_u32 m0, s70, 82944\n" \
    "s_nop 0\n" \
    "buffer_load_dwordx4 v37, %[w1], s67 offen lds\n" \
    "s_add_u32 m0, s70, 83968\n" \
    "s_nop 0\n" \
    "buffer_load_dwordx4 v38, %[w1], s67 offen lds\n" \
    "s_add_u32 m0, s70, 84992\n" \
    "s_nop 0\n" \
    "buffer_load_dwordx4 v39, %[w1], s67 offen lds\n" \
    "s_add_u32 m0, s70, 86016\n" \
    "s_nop 0\n" \
    "buffer_load_dwordx4 v40, %[w1], s67 offen lds\n" \
    "s_add_u32 m0, s70, 87040\n" \
    "s_nop 0\n" \
    "buffer_load_dwordx4 v41, %[w1], s67 offen lds\n" \
    "s_add_u32 m0, s70, 88064\n" \
    "s_nop 0\n" \
    "buffer_load_dwordx4 v42, %[w1], s67 offen lds\n" \
    "s_add_u32 m0, s70, 89088\n" \
    "s_nop 0\n" \
    "buffer_load_dwordx4 v43, %[w1], s67 offen lds\n" \
    "s_add_u32 m0, s70, 90112\n" \
    "s_nop 0\n" \
    "buffer_load_dwordx4 v44, %[w1], s67 offen lds\n" \
    "s_add_u32 m0, s70, 91136\n" \
    "s_nop 0\n" \
    "buffer_load_dwordx4 v45, %[w1], s67 offen lds\n" \
    "s_mov_b32 s67, 122880\n" \
    "s_add_u32 m0, s70, 122880\n" \
    "s_nop 0\n" \
    "buffer_load_dwordx4 v36, %[w1], s67 offen lds\n" \
    "s_add_u32 m0, s70, 123904\n" \
    "s_nop 0\n" \
    "buffer_load_dwordx4 v37, %[w1], s67 offen lds\n" \
    "s_add_u32 m0, s70, 124928\n" \
    "s_nop 0\n" \
    "buffer_load_dwordx4 v38, %[w1], s67 offen lds\n" \
    "s_add_u32 m0, s70, 125952\n" \
    "s_nop 0\n" \
    "buffer_load_dwordx4 v39, %[w1], s67 offen lds\n" \
    "s_add_u32 m0, s70, 126976\n" \
    "s_nop 0\n" \
    "buffer_load_dwordx4 v40, %[w1], s67 offen lds\n" \
    "s_add_u32 m0, s70, 128000\n" \
    "s_nop 0\n" \
    "buffer_load_dwordx4 v41, %[w1], s67 offen lds\n" \
    "s_add_u32 m0, s70, 129024\n" \
    "s_nop 0\n" \
    "buffer_load_dwordx4 v42, %[w1], s67 offen lds\n" \
    "s_add_u32 m0, s70, 130048\n" \
    "s_nop 0\n" \
    "buffer_load_dwordx4 v43, %[w1], s67 offen lds\n" \
    "s_add_u32 m0, s70, 131072\n" \
    "s_nop 0\n" \
    "buffer_load_dwordx4 v44, %[w1], s67 offen lds\n" \
    "s_add_u32 m0, s70, 132096\n" \
    "s_nop 0\n" \
    "buffer_load_dwordx4 v45, %[w1], s67 offen lds\n" \
    "s_waitcnt vmcnt(30)\n" \
    "s_barrier\n" \
    "ds_read_b64_tr_b16 a[184:185], v20\n" \
    "ds_read_b64_tr_b16 a[186:187], v20 offset:2560\n" \
    "ds_read_b64_tr_b16 a[188:189], v20 offset:20480\n" \
    "ds_read_b64_tr_b16 a[190:191], v20 offset:23040\n" \
    "ds_read_b64_tr_b16 a[232:233], v21\n" \
    "ds_read_b64_tr_b16 a[234:235], v21 offset:2560\n" \
    "ds_read_b64_tr_b16 a[236:237], v21 offset:20480\n" \
    "ds_read_b64_tr_b16 a[238:239], v21 offset:23040\n" \
    "ds_read_b64_tr_b16 v[108:109], v22\n" \
    "ds_read_b64_tr_b16 v[110:111], v22 offset:2560\n" \
    "ds_read_b64_tr_b16 v[112:113], v22 offset:20480\n" \
    "ds_read_b64_tr_b16 v[114:115], v22 offset:23040\n" \
    "s_waitcnt lgkmcnt(8)\n" \
    "ds_read_b64_tr_b16 v[156:157], v23\n" \
    "ds_read_b64_tr_b16 v[158:159], v23 offset:2560\n" \
    "ds_read_b64_tr_b16 v[160:161], v23 offset:20480\n" \
    "ds_read_b64_tr_b16 v[162:163], v23 offset:23040\n" \
    "s_waitcnt lgkmcnt(8)\n" \
    "ds_read_b64_tr_b16 v[204:205], v24\n" \
    "ds_read_b64_tr_b16 v[206:207], v24 offset:2560\n" \
    "ds_read_b64_tr_b16 v[208:209], v24 offset:20480\n" \
    "ds_read_b64_tr_b16 v[210:211], v24 offset:23040\n" \
    "s_waitcnt lgkmcnt(0)\n" \
    "s_waitcnt vmcnt(20)\n" \
    "s_barrier\n" \
    "s_mov_b32 s67, 0\n" \
    "s_add_u32 m0, s71, 0\n" \
    "s_nop 0\n" \
    "buffer_load_dwordx4 v12, %[w0], s67 offen lds\n" \
    "s_add_u32 m0, s71, 1024\n" \
    "s_nop 0\n" \
    "buffer_load_dwordx4 v13, %[w0], s67 offen lds\n" \
    "s_add_u32 m0, s71, 2048\n" \
    "s_nop 0\n" \
    "buffer_load_dwordx4 v14, %[w0], s67 offen lds\n" \
    "s_mov_b32 s67, 393216\n" \
    "s_add_u32 m0, s71, 12288\n" \
    "s_nop 0\n" \
    "buffer_load_dwordx4 v12, %[w0], s67 offen lds\n" \
    "s_add_u32 m0, s71, 13312\n" \
    "s_nop 0\n" \
    "buffer_load_dwordx4 v13, %[w0], s67 offen lds\n" \
    "s_add_u32 m0, s71, 14336\n" \
    "s_nop 0\n" \
    "buffer_load_dwordx4 v14, %[w0], s67 offen lds\n" \
    "ds_read_b64_tr_b16 a[192:193], v20 offset:40960\n" \
    "ds_read_b64_tr_b16 a[194:195], v20 offset:43520\n" \
    "ds_read_b64_tr_b16 a[196:197], v68\n" \
    "ds_read_b64_tr_b16 a[198:199], v68 offset:2560\n" \
    "ds_read_b64_tr_b16 a[240:241], v21 offset:40960\n" \
    "ds_read_b64_tr_b16 a[242:243], v21 offset:43520\n" \
    "ds_read_b64_tr_b16 a[244:245], v69\n" \
    "ds_read_b64_tr_b16 a[246:247], v69 offset:2560\n" \
    "ds_read_b64_tr_b16 v[116:117], v22 offset:40960\n" \
    "ds_read_b64_tr_b16 v[118:119], v22 offset:43520\n" \
    "ds_read_b64_tr_b16 v[120:121], v70\n" \
    "ds_read_b64_tr_b16 v[122:123], v70 offset:2560\n" \
    "s_waitcnt lgkmcnt(8)\n" \
    "ds_read_b64_tr_b16 v[164:165], v23 offset:40960\n" \
    "ds_read_b64_tr_b16 v[166:167], v23 offset:43520\n" \
    "ds_read_b64_tr_b16 v[168:169], v71\n" \
    "ds_read_b64_tr_b16 v[170:171], v71 offset:2560\n" \
    "s_waitcnt lgkmcnt(8)\n" \
    "ds_read_b64_tr_b16 v[212:213], v24 offset:40960\n" \
    "ds_read_b64_tr_b16 v[214:215], v24 offset:43520\n" \
    "ds_read_b64_tr_b16 v[216:217], v72\n" \
    "ds_read_b64_tr_b16 v[218:219], v72 offset:2560\n" \
    "s_waitcnt lgkmcnt(0)\n" \
    "s_waitcnt vmcnt(16)\n" \
    "s_barrier\n" \
    "s_mov_b32 s67, 3145728\n" \
    "s_add_u32 m0, s71, 24576\n" \
    "s_nop 0\n" \
    "buffer_load_dwordx4 v12, %[w0], s67 offen lds\n" \
    "s_add_u32 m0, s71, 25600\n" \
    "s_nop 0\n" \
    "buffer_load_dwordx4 v13, %[w0], s67 offen lds\n" \
    "s_add_u32 m0, s71, 26624\n" \
    "s_nop 0\n" \
    "buffer_load_dwordx4 v14, %[w0], s67 offen lds\n" \
    "s_mov_b32 s67, 3538944\n" \
    "s_add_u32 m0, s71, 36864\n" \
    "s_nop 0\n" \
    "buffer_load_dwordx4 v12, %[w0], s67 offen lds\n" \
    "s_add_u32 m0, s71, 37888\n" \
    "s_nop 0\n" \
    "buffer_load_dwordx4 v13, %[w0], s67 offen lds\n" \
    "s_add_u32 m0, s71, 38912\n" \
    "s_nop 0\n" \
    "buffer_load_dwordx4 v14, %[w0], s67 offen lds\n" \
    "s_mov_b32 s67, 6291456\n" \
    "s_add_u32 m0, s71, 49152\n" \
    "s_nop 0\n" \
    "buffer_load_dwordx4 v12, %[w0], s67 offen lds\n" \
    "s_add_u32 m0, s71, 50176\n" \
    "s_nop 0\n" \
    "buffer_load_dwordx4 v13, %[w0], s67 offen lds\n" \
    "s_add_u32 m0, s71, 51200\n" \
    "s_nop 0\n" \
    "buffer_load_dwordx4 v14, %[w0], s67 offen lds\n" \
    "s_mov_b32 s67, 6684672\n" \
    "s_add_u32 m0, s71, 61440\n" \
    "s_nop 0\n" \
    "buffer_load_dwordx4 v12, %[w0], s67 offen lds\n" \
    "s_add_u32 m0, s71, 62464\n" \
    "s_nop 0\n" \
    "buffer_load_dwordx4 v13, %[w0], s67 offen lds\n" \
    "s_add_u32 m0, s71, 63488\n" \
    "s_nop 0\n" \
    "buffer_load_dwordx4 v14, %[w0], s67 offen lds\n" \
    "ds_read_b64_tr_b16 a[200:201], v68 offset:20480\n" \
    "ds_read_b64_tr_b16 a[202:203], v68 offset:23040\n" \
    "ds_read_b64_tr_b16 a[204:205], v68 offset:40960\n" \
    "ds_read_b64_tr_b16 a[206:207], v68 offset:43520\n" \
    "ds_read_b64_tr_b16 a[248:249], v69 offset:20480\n" \
    "ds_read_b64_tr_b16 a[250:251], v69 offset:23040\n" \
    "ds_read_b64_tr_b16 a[252:253], v69 offset:40960\n" \
    "ds_read_b64_tr_b16 a[254:255], v69 offset:43520\n" \
    "ds_read_b64_tr_b16 v[124:125], v70 offset:20480\n" \
    "ds_read_b64_tr_b16 v[126:127], v70 offset:23040\n" \
    "ds_read_b64_tr_b16 v[128:129], v70 offset:40960\n" \
    "ds_read_b64_tr_b16 v[130:131], v70 offset:43520\n" \
    "s_waitcnt lgkmcnt(8)\n" \
    "ds_read_b64_tr_b16 v[172:173], v71 offset:20480\n" \
    "ds_read_b64_tr_b16 v[174:175], v71 offset:23040\n" \
    "ds_read_b64_tr_b16 v[176:177], v71 offset:40960\n" \
    "ds_read_b64_tr_b16 v[178:179], v71 offset:43520\n" \
    "s_waitcnt lgkmcnt(8)\n" \
    "ds_read_b64_tr_b16 v[220:221], v72 offset:20480\n" \
    "ds_read_b64_tr_b16 v[222:223], v72 offset:23040\n" \
    "ds_read_b64_tr_b16 v[224:225], v72 offset:40960\n" \
    "ds_read_b64_tr_b16 v[226:227], v72 offset:43520\n" \
    "s_waitcnt lgkmcnt(0)\n" \
    "s_waitcnt vmcnt(18)\n" \
    "s_barrier\n" \
    "s_mov_b32 s67, 163840\n" \
    "s_add_u32 m0, s70, 81920\n" \
    "s_nop 0\n" \
    "buffer_load_dwordx4 v36, %[w1], s67 offen lds\n" \
    "s_add_u32 m0, s70, 82944\n" \
    "s_nop 0\n" \
    "buffer_load_dwordx4 v37, %[w1], s67 offen lds\n" \
    "s_add_u32 m0, s70, 83968\n" \
    "s_nop 0\n" \
    "buffer_load_dwordx4 v38, %[w1], s67 offen lds\n" \
    "s_add_u32 m0, s70, 84992\n" \
    "s_nop 0\n" \
    "buffer_load_dwordx4 v39, %[w1], s67 offen lds\n" \
    "s_add_u32 m0, s70, 86016\n" \
    "s_nop 0\n" \
    "buffer_load_dwordx4 v40, %[w1], s67 offen lds\n" \
    "s_add_u32 m0, s70, 87040\n" \
    "s_nop 0\n" \
    "buffer_load_dwordx4 v41, %[w1], s67 offen lds\n" \
    "s_add_u32 m0, s70, 88064\n" \
    "s_nop 0\n" \
    "buffer_load_dwordx4 v42, %[w1], s67 offen lds\n" \
    "s_add_u32 m0, s70, 89088\n" \
    "s_nop 0\n" \
    "buffer_load_dwordx4 v43, %[w1], s67 offen lds\n" \
    "s_add_u32 m0, s70, 90112\n" \
    "s_nop 0\n" \
    "buffer_load_dwordx4 v44, %[w1], s67 offen lds\n" \
    "s_add_u32 m0, s70, 91136\n" \
    "s_nop 0\n" \
    "buffer_load_dwordx4 v45, %[w1], s67 offen lds\n" \
    "ds_read_b64_tr_b16 a[208:209], v73\n" \
    "ds_read_b64_tr_b16 a[210:211], v73 offset:2560\n" \
    "ds_read_b64_tr_b16 a[212:213], v73 offset:20480\n" \
    "ds_read_b64_tr_b16 a[214:215], v73 offset:23040\n" \
    "ds_read_b64_tr_b16 v[84:85], v74\n" \
    "ds_read_b64_tr_b16 v[86:87], v74 offset:2560\n" \
    "ds_read_b64_tr_b16 v[88:89], v74 offset:20480\n" \
    "ds_read_b64_tr_b16 v[90:91], v74 offset:23040\n" \
    "ds_read_b64_tr_b16 v[132:133], v75\n" \
    "ds_read_b64_tr_b16 v[134:135], v75 offset:2560\n" \
    "ds_read_b64_tr_b16 v[136:137], v75 offset:20480\n" \
    "ds_read_b64_tr_b16 v[138:139], v75 offset:23040\n" \
    "s_waitcnt lgkmcnt(8)\n" \
    "ds_read_b64_tr_b16 v[180:181], v76\n" \
    "ds_read_b64_tr_b16 v[182:183], v76 offset:2560\n" \
    "ds_read_b64_tr_b16 v[184:185], v76 offset:20480\n" \
    "ds_read_b64_tr_b16 v[186:187], v76 offset:23040\n" \
    "s_waitcnt lgkmcnt(8)\n" \
    "ds_read_b64_tr_b16 v[228:229], v77\n" \
    "ds_read_b64_tr_b16 v[230:231], v77 offset:2560\n" \
    "ds_read_b64_tr_b16 v[232:233], v77 offset:20480\n" \
    "ds_read_b64_tr_b16 v[234:235], v77 offset:23040\n" \
    "s_waitcnt lgkmcnt(0)\n" \
    "s_waitcnt vmcnt(22)\n" \
    "s_barrier\n" \
    "s_mov_b32 s67, 204800\n" \
    "s_add_u32 m0, s70, 122880\n" \
    "s_nop 0\n" \
    "buffer_load_dwordx4 v36, %[w1], s67 offen lds\n" \
    "s_add_u32 m0, s70, 123904\n" \
    "s_nop 0\n" \
    "buffer_load_dwordx4 v37, %[w1], s67 offen lds\n" \
    "s_add_u32 m0, s70, 124928\n" \
    "s_nop 0\n" \
    "buffer_load_dwordx4 v38, %[w1], s67 offen lds\n" \
    "s_add_u32 m0, s70, 125952\n" \
    "s_nop 0\n" \
    "buffer_load_dwordx4 v39, %[w1], s67 offen lds\n" \
    "s_add_u32 m0, s70, 126976\n" \
    "s_nop 0\n" \
    "buffer_load_dwordx4 v40, %[w1], s67 offen lds\n" \
    "s_add_u32 m0, s70, 128000\n" \
    "s_nop 0\n" \
    "buffer_load_dwordx4 v41, %[w1], s67 offen lds\n" \
    "s_add_u32 m0, s70, 129024\n" \
    "s_nop 0\n" \
    "buffer_load_dwordx4 v42, %[w1], s67 offen lds\n" \
    "s_add_u32 m0, s70, 130048\n" \
    "s_nop 0\n" \
    "buffer_load_dwordx4 v43, %[w1], s67 offen lds\n" \
    "s_add_u32 m0, s70, 131072\n" \
    "s_nop 0\n" \
    "buffer_load_dwordx4 v44, %[w1], s67 offen lds\n" \
    "s_add_u32 m0, s70, 132096\n" \
    "s_nop 0\n" \
    "buffer_load_dwordx4 v45, %[w1], s67 offen lds\n" \
    "ds_read_b64_tr_b16 a[0:1], v16\n" \
    "ds_read_b64_tr_b16 a[2:3], v16 offset:1536\n" \
    "ds_read_b64_tr_b16 a[4:5], v16 offset:12288\n" \
    "ds_read_b64_tr_b16 a[6:7], v16 offset:13824\n" \
    "ds_read_b64_tr_b16 a[8:9], v17\n" \
    "ds_read_b64_tr_b16 a[10:11], v17 offset:1536\n" \
    "ds_read_b64_tr_b16 a[12:13], v17 offset:12288\n" \
    "ds_read_b64_tr_b16 a[14:15], v17 offset:13824\n" \
    "ds_read_b64_tr_b16 a[16:17], v18\n" \
    "ds_read_b64_tr_b16 a[18:19], v18 offset:1536\n" \
    "ds_read_b64_tr_b16 a[20:21], v18 offset:12288\n" \
    "ds_read_b64_tr_b16 a[22:23], v18 offset:13824\n" \
    "s_waitcnt lgkmcnt(0)\n" \
    "s_waitcnt vmcnt(26)\n" \
    "s_barrier\n" \
    "s_mov_b32 s67, 786432\n" \
    "s_add_u32 m0, s71, 0\n" \
    "s_nop 0\n" \
    "buffer_load_dwordx4 v12, %[w0], s67 offen lds\n" \
    "s_add_u32 m0, s71, 1024\n" \
    "s_nop 0\n" \
    "buffer_load_dwordx4 v13, %[w0], s67 offen lds\n" \
    "s_add_u32 m0, s71, 2048\n" \
    "s_nop 0\n" \
    "buffer_load_dwordx4 v14, %[w0], s67 offen lds\n" \
    "s_mov_b32 s67, 1179648\n" \
    "s_add_u32 m0, s71, 12288\n" \
    "s_nop 0\n" \
    "buffer_load_dwordx4 v12, %[w0], s67 offen lds\n" \
    "s_add_u32 m0, s71, 13312\n" \
    "s_nop 0\n" \
    "buffer_load_dwordx4 v13, %[w0], s67 offen lds\n" \
    "s_add_u32 m0, s71, 14336\n" \
    "s_nop 0\n" \
    "buffer_load_dwordx4 v14, %[w0], s67 offen lds\n" \
    "ds_read_b64_tr_b16 a[72:73], v16 offset:24576\n" \
    "ds_read_b64_tr_b16 a[74:75], v16 offset:26112\n" \
    "ds_read_b64_tr_b16 a[76:77], v16 offset:36864\n" \
    "ds_read_b64_tr_b16 a[78:79], v16 offset:38400\n" \
    "ds_read_b64_tr_b16 a[80:81], v17 offset:24576\n" \
    "ds_read_b64_tr_b16 a[82:83], v17 offset:26112\n" \
    "ds_read_b64_tr_b16 a[84:85], v17 offset:36864\n" \
    "ds_read_b64_tr_b16 a[86:87], v17 offset:38400\n" \
    "ds_read_b64_tr_b16 a[88:89], v18 offset:24576\n" \
    "ds_read_b64_tr_b16 a[90:91], v18 offset:26112\n" \
    "ds_read_b64_tr_b16 a[92:93], v18 offset:36864\n" \
    "ds_read_b64_tr_b16 a[94:95], v18 offset:38400\n" \
    "s_waitcnt lgkmcnt(0)\n" \
    "s_waitcnt vmcnt(26)\n" \
    "s_barrier\n" \
    "s_mov_b32 s67, 1572864\n" \
    "s_add_u32 m0, s71, 24576\n" \
    "s_nop 0\n" \
    "buffer_load_dwordx4 v12, %[w0], s67 offen lds\n" \
    "s_add_u32 m0, s71, 25600\n" \
    "s_nop 0\n" \
    "buffer_load_dwordx4 v13, %[w0], s67 offen lds\n" \
    "s_add_u32 m0, s71, 26624\n" \
    "s_nop 0\n" \
    "buffer_load_dwordx4 v14, %[w0], s67 offen lds\n" \
    "s_mov_b32 s67, 1966080\n" \
    "s_add_u32 m0, s71, 36864\n" \
    "s_nop 0\n" \
    "buffer_load_dwordx4 v12, %[w0], s67 offen lds\n" \
    "s_add_u32 m0, s71, 37888\n" \
    "s_nop 0\n" \
    "buffer_load_dwordx4 v13, %[w0], s67 offen lds\n" \
    "s_add_u32 m0, s71, 38912\n" \
    "s_nop 0\n" \
    "buffer_load_dwordx4 v14, %[w0], s67 offen lds\n" \
    "ds_read_b64_tr_b16 a[144:145], v16 offset:49152\n" \
    "ds_read_b64_tr_b16 a[146:147], v16 offset:50688\n" \
    "ds_read_b64_tr_b16 a[148:149], v28\n" \
    "ds_read_b64_tr_b16 a[150:151], v28 offset:1536\n" \
    "ds_read_b64_tr_b16 a[152:153], v17 offset:49152\n" \
    "ds_read_b64_tr_b16 a[154:155], v17 offset:50688\n" \
    "ds_read_b64_tr_b16 a[156:157], v29\n" \
    "ds_read_b64_tr_b16 a[158:159], v29 offset:1536\n" \
    "ds_read_b64_tr_b16 a[160:161], v18 offset:49152\n" \
    "ds_read_b64_tr_b16 a[162:163], v18 offset:50688\n" \
    "ds_read_b64_tr_b16 a[164:165], v30\n" \
    "ds_read_b64_tr_b16 a[166:167], v30 offset:1536\n" \
    "s_waitcnt lgkmcnt(0)\n" \
    "s_waitcnt vmcnt(22)\n" \
    "s_barrier\n" \
    "s_mov_b32 s67, 2359296\n" \
    "s_add_u32 m0, s71, 49152\n" \
    "s_nop 0\n" \
    "buffer_load_dwordx4 v12, %[w0], s67 offen lds\n" \
    "s_add_u32 m0, s71, 50176\n" \
    "s_nop 0\n" \
    "buffer_load_dwordx4 v13, %[w0], s67 offen lds\n" \
    "s_add_u32 m0, s71, 51200\n" \
    "s_nop 0\n" \
    "buffer_load_dwordx4 v14, %[w0], s67 offen lds\n" \
    "s_mov_b32 s67, 2752512\n" \
    "s_add_u32 m0, s71, 61440\n" \
    "s_nop 0\n" \
    "buffer_load_dwordx4 v12, %[w0], s67 offen lds\n" \
    "s_add_u32 m0, s71, 62464\n" \
    "s_nop 0\n" \
    "buffer_load_dwordx4 v13, %[w0], s67 offen lds\n" \
    "s_add_u32 m0, s71, 63488\n" \
    "s_nop 0\n" \
    "buffer_load_dwordx4 v14, %[w0], s67 offen lds\n" \
    "ds_read_b64_tr_b16 a[216:217], v68 offset:20480\n" \
    "ds_read_b64_tr_b16 a[218:219], v68 offset:23040\n" \
    "ds_read_b64_tr_b16 a[220:221], v68 offset:40960\n" \
    "ds_read_b64_tr_b16 a[222:223], v68 offset:43520\n" \
    "ds_read_b64_tr_b16 v[92:93], v69 offset:20480\n" \
    "ds_read_b64_tr_b16 v[94:95], v69 offset:23040\n" \
    "ds_read_b64_tr_b16 v[96:97], v69 offset:40960\n" \
    "ds_read_b64_tr_b16 v[98:99], v69 offset:43520\n" \
    "ds_read_b64_tr_b16 v[140:141], v70 offset:20480\n" \
    "ds_read_b64_tr_b16 v[142:143], v70 offset:23040\n" \
    "ds_read_b64_tr_b16 v[144:145], v70 offset:40960\n" \
    "ds_read_b64_tr_b16 v[146:147], v70 offset:43520\n" \
    "s_waitcnt lgkmcnt(8)\n" \
    "ds_read_b64_tr_b16 v[188:189], v71 offset:20480\n" \
    "ds_read_b64_tr_b16 v[190:191], v71 offset:23040\n" \
    "ds_read_b64_tr_b16 v[192:193], v71 offset:40960\n" \
    "ds_read_b64_tr_b16 v[194:195], v71 offset:43520\n" \
    "s_waitcnt lgkmcnt(8)\n" \
    "ds_read_b64_tr_b16 v[236:237], v72 offset:20480\n" \
    "ds_read_b64_tr_b16 v[238:239], v72 offset:23040\n" \
    "ds_read_b64_tr_b16 v[240:241], v72 offset:40960\n" \
    "ds_read_b64_tr_b16 v[242:243], v72 offset:43520\n" \
    "s_waitcnt lgkmcnt(0)\n" \
    "s_waitcnt vmcnt(18)\n" \
    "s_barrier\n" \
    "ds_read_b64_tr_b16 a[224:225], v73\n" \
    "ds_read_b64_tr_b16 a[226:227], v73 offset:2560\n" \
    "ds_read_b64_tr_b16 a[228:229], v73 offset:20480\n" \
    "ds_read_b64_tr_b16 a[230:231], v73 offset:23040\n" \
    "ds_read_b64_tr_b16 v[100:101], v74\n" \
    "ds_read_b64_tr_b16 v[102:103], v74 offset:2560\n" \
    "ds_read_b64_tr_b16 v[104:105], v74 offset:20480\n" \
    "ds_read_b64_tr_b16 v[106:107], v74 offset:23040\n" \
    "ds_read_b64_tr_b16 v[148:149], v75\n" \
    "ds_read_b64_tr_b16 v[150:151], v75 offset:2560\n" \
    "ds_read_b64_tr_b16 v[152:153], v75 offset:20480\n" \
    "ds_read_b64_tr_b16 v[154:155], v75 offset:23040\n" \
    "s_waitcnt lgkmcnt(8)\n" \
    "ds_read_b64_tr_b16 v[196:197], v76\n" \
    "ds_read_b64_tr_b16 v[198:199], v76 offset:2560\n" \
    "ds_read_b64_tr_b16 v[200:201], v76 offset:20480\n" \
    "ds_read_b64_tr_b16 v[202:203], v76 offset:23040\n" \
    "s_waitcnt lgkmcnt(8)\n" \
    "ds_read_b64_tr_b16 v[244:245], v77\n" \
    "ds_read_b64_tr_b16 v[246:247], v77 offset:2560\n" \
    "ds_read_b64_tr_b16 v[248:249], v77 offset:20480\n" \
    "ds_read_b64_tr_b16 v[250:251], v77 offset:23040\n" \
    "s_waitcnt lgkmcnt(0)\n" \
    "s_waitcnt vmcnt(12)\n" \
    "s_barrier\n" \
    "ds_read_b64_tr_b16 v[60:61], v16\n" \
    "ds_read_b64_tr_b16 v[62:63], v16 offset:1536\n" \
    "ds_read_b64_tr_b16 v[64:65], v16 offset:12288\n" \
    "ds_read_b64_tr_b16 v[66:67], v16 offset:13824\n" \
    "s_waitcnt lgkmcnt(2)\n" \
    "ds_write_b128 %[vres], v[60:63]\n" \
    "s_waitcnt lgkmcnt(1)\n" \
    "ds_write_b128 %[vres], v[64:67] offset:1024\n" \
    "ds_read_b64_tr_b16 a[24:25], v17\n" \
    "ds_read_b64_tr_b16 a[26:27], v17 offset:1536\n" \
    "ds_read_b64_tr_b16 a[28:29], v17 offset:12288\n" \
    "ds_read_b64_tr_b16 a[30:31], v17 offset:13824\n" \
    "ds_read_b64_tr_b16 a[32:33], v18\n" \
    "ds_read_b64_tr_b16 a[34:35], v18 offset:1536\n" \
    "ds_read_b64_tr_b16 a[36:37], v18 offset:12288\n" \
    "ds_read_b64_tr_b16 a[38:39], v18 offset:13824\n" \
    "s_waitcnt lgkmcnt(0)\n" \
    "s_waitcnt vmcnt(6)\n" \
    "s_barrier\n" \
    "s_mov_b32 s67, 3932160\n" \
    "s_add_u32 m0, s71, 0\n" \
    "s_nop 0\n" \
    "buffer_load_dwordx4 v12, %[w0], s67 offen lds\n" \
    "s_add_u32 m0, s71, 1024\n" \
    "s_nop 0\n" \
    "buffer_load_dwordx4 v13, %[w0], s67 offen lds\n" \
    "s_add_u32 m0, s71, 2048\n" \
    "s_nop 0\n" \
    "buffer_load_dwordx4 v14, %[w0], s67 offen lds\n" \
    "s_mov_b32 s67, 4325376\n" \
    "s_add_u32 m0, s71, 12288\n" \
    "s_nop 0\n" \
    "buffer_load_dwordx4 v12, %[w0], s67 offen lds\n" \
    "s_add_u32 m0, s71, 13312\n" \
    "s_nop 0\n" \
    "buffer_load_dwordx4 v13, %[w0], s67 offen lds\n" \
    "s_add_u32 m0, s71, 14336\n" \
    "s_nop 0\n" \
    "buffer_load_dwordx4 v14, %[w0], s67 offen lds\n" \
    "ds_read_b64_tr_b16 a[40:41], v16 offset:24576\n" \
    "ds_read_b64_tr_b16 a[42:43], v16 offset:26112\n" \
    "ds_read_b64_tr_b16 a[44:45], v16 offset:36864\n" \
    "ds_read_b64_tr_b16 a[46:47], v16 offset:38400\n" \
    "ds_read_b64_tr_b16 v[60:61], v17 offset:24576\n" \
    "ds_read_b64_tr_b16 v[62:63], v17 offset:26112\n" \
    "ds_read_b64_tr_b16 v[64:65], v17 offset:36864\n" \
    "ds_read_b64_tr_b16 v[66:67], v17 offset:38400\n" \
    "s_waitcnt lgkmcnt(2)\n" \
    "ds_write_b128 %[vres], v[60:63] offset:2048\n" \
    "s_waitcnt lgkmcnt(1)\n" \
    "ds_write_b128 %[vres], v[64:67] offset:3072\n" \
    "ds_read_b64_tr_b16 a[48:49], v18 offset:24576\n" \
    "ds_read_b64_tr_b16 a[50:51], v18 offset:26112\n" \
    "ds_read_b64_tr_b16 a[52:53], v18 offset:36864\n" \
    "ds_read_b64_tr_b16 a[54:55], v18 offset:38400\n" \
    "s_waitcnt lgkmcnt(0)\n" \
    "s_waitcnt vmcnt(6)\n" \
    "s_barrier\n" \
    "s_mov_b32 s67, 4718592\n" \
    "s_add_u32 m0, s71, 24576\n" \
    "s_nop 0\n" \
    "buffer_load_dwordx4 v12, %[w0], s67 offen lds\n" \
    "s_add_u32 m0, s71, 25600\n" \
    "s_nop 0\n" \
    "buffer_load_dwordx4 v13, %[w0], s67 offen lds\n" \
    "s_add_u32 m0, s71, 26624\n" \
    "s_nop 0\n" \
    "buffer_load_dwordx4 v14, %[w0], s67 offen lds\n" \
    "s_mov_b32 s67, 5111808\n" \
    "s_add_u32 m0, s71, 36864\n" \
    "s_nop 0\n" \
    "buffer_load_dwordx4 v12, %[w0], s67 offen lds\n" \
    "s_add_u32 m0, s71, 37888\n" \
    "s_nop 0\n" \
    "buffer_load_dwordx4 v13, %[w0], s67 offen lds\n" \
    "s_add_u32 m0, s71, 38912\n" \
    "s_nop 0\n" \
    "buffer_load_dwordx4 v14, %[w0], s67 offen lds\n" \
    "ds_read_b64_tr_b16 a[56:57], v16 offset:49152\n" \
    "ds_read_b64_tr_b16 a[58:59], v16 offset:50688\n" \
    "ds_read_b64_tr_b16 a[60:61], v28\n" \
    "ds_read_b64_tr_b16 a[62:63], v28 offset:1536\n" \
    "ds_read_b64_tr_b16 a[64:65], v17 offset:49152\n" \
    "ds_read_b64_tr_b16 a[66:67], v17 offset:50688\n" \
    "ds_read_b64_tr_b16 a[68:69], v29\n" \
    "ds_read_b64_tr_b16 a[70:71], v29 offset:1536\n" \
    "ds_read_b64_tr_b16 v[60:61], v18 offset:49152\n" \
    "ds_read_b64_tr_b16 v[62:63], v18 offset:50688\n" \
    "ds_read_b64_tr_b16 v[64:65], v30\n" \
    "ds_read_b64_tr_b16 v[66:67], v30 offset:1536\n" \
    "s_waitcnt lgkmcnt(2)\n" \
    "ds_write_b128 %[vres], v[60:63] offset:4096\n" \
    "s_waitcnt lgkmcnt(1)\n" \
    "ds_write_b128 %[vres], v[64:67] offset:5120\n" \
    "s_waitcnt lgkmcnt(0)\n" \
    "s_waitcnt vmcnt(6)\n" \
    "s_barrier\n" \
    "s_mov_b32 s67, 5505024\n" \
    "s_add_u32 m0, s71, 49152\n" \
    "s_nop 0\n" \
    "buffer_load_dwordx4 v12, %[w0], s67 offen lds\n" \
    "s_add_u32 m0, s71, 50176\n" \
    "s_nop 0\n" \
    "buffer_load_dwordx4 v13, %[w0], s67 offen lds\n" \
    "s_add_u32 m0, s71, 51200\n" \
    "s_nop 0\n" \
    "buffer_load_dwordx4 v14, %[w0], s67 offen lds\n" \
    "s_mov_b32 s67, 5898240\n" \
    "s_add_u32 m0, s71, 61440\n" \
    "s_nop 0\n" \
    "buffer_load_dwordx4 v12, %[w0], s67 offen lds\n" \
    "s_add_u32 m0, s71, 62464\n" \
    "s_nop 0\n" \
    "buffer_load_dwordx4 v13, %[w0], s67 offen lds\n" \
    "s_add_u32 m0, s71, 63488\n" \
    "s_nop 0\n" \
    "buffer_load_dwordx4 v14, %[w0], s67 offen lds\n" \
    "ds_read_b64_tr_b16 v[60:61], v16\n" \
    "ds_read_b64_tr_b16 v[62:63], v16 offset:1536\n" \
    "ds_read_b64_tr_b16 v[64:65], v16 offset:12288\n" \
    "ds_read_b64_tr_b16 v[66:67], v16 offset:13824\n" \
    "s_waitcnt lgkmcnt(2)\n" \
    "ds_write_b128 %[vres], v[60:63] offset:6144\n" \
    "s_waitcnt lgkmcnt(1)\n" \
    "ds_write_b128 %[vres], v[64:67] offset:7168\n" \
    "ds_read_b64_tr_b16 a[96:97], v17\n" \
    "ds_read_b64_tr_b16 a[98:99], v17 offset:1536\n" \
    "ds_read_b64_tr_b16 a[100:101], v17 offset:12288\n" \
    "ds_read_b64_tr_b16 a[102:103], v17 offset:13824\n" \
    "ds_read_b64_tr_b16 a[104:105], v18\n" \
    "ds_read_b64_tr_b16 a[106:107], v18 offset:1536\n" \
    "ds_read_b64_tr_b16 a[108:109], v18 offset:12288\n" \
    "ds_read_b64_tr_b16 a[110:111], v18 offset:13824\n" \
    "s_waitcnt lgkmcnt(0)\n" \
    "s_waitcnt vmcnt(6)\n" \
    "s_barrier\n" \
    "s_mov_b32 s67, 7077888\n" \
    "s_add_u32 m0, s71, 0\n" \
    "s_nop 0\n" \
    "buffer_load_dwordx4 v12, %[w0], s67 offen lds\n" \
    "s_add_u32 m0, s71, 1024\n" \
    "s_nop 0\n" \
    "buffer_load_dwordx4 v13, %[w0], s67 offen lds\n" \
    "s_add_u32 m0, s71, 2048\n" \
    "s_nop 0\n" \
    "buffer_load_dwordx4 v14, %[w0], s67 offen lds\n" \
    "s_mov_b32 s67, 7471104\n" \
    "s_add_u32 m0, s71, 12288\n" \
    "s_nop 0\n" \
    "buffer_load_dwordx4 v12, %[w0], s67 offen lds\n" \
    "s_add_u32 m0, s71, 13312\n" \
    "s_nop 0\n" \
    "buffer_load_dwordx4 v13, %[w0], s67 offen lds\n" \
    "s_add_u32 m0, s71, 14336\n" \
    "s_nop 0\n" \
    "buffer_load_dwordx4 v14, %[w0], s67 offen lds\n" \
    "ds_read_b64_tr_b16 a[112:113], v16 offset:24576\n" \
    "ds_read_b64_tr_b16 a[114:115], v16 offset:26112\n" \
    "ds_read_b64_tr_b16 a[116:117], v16 offset:36864\n" \
    "ds_read_b64_tr_b16 a[118:119], v16 offset:38400\n" \
    "ds_read_b64_tr_b16 v[60:61], v17 offset:24576\n" \
    "ds_read_b64_tr_b16 v[62:63], v17 offset:26112\n" \
    "ds_read_b64_tr_b16 v[64:65], v17 offset:36864\n" \
    "ds_read_b64_tr_b16 v[66:67], v17 offset:38400\n" \
    "s_waitcnt lgkmcnt(2)\n" \
    "ds_write_b128 %[vres], v[60:63] offset:8192\n" \
    "s_waitcnt lgkmcnt(1)\n" \
    "ds_write_b128 %[vres], v[64:67] offset:9216\n" \
    "ds_read_b64_tr_b16 a[120:121], v18 offset:24576\n" \
    "ds_read_b64_tr_b16 a[122:123], v18 offset:26112\n" \
    "ds_read_b64_tr_b16 a[124:125], v18 offset:36864\n" \
    "ds_read_b64_tr_b16 a[126:127], v18 offset:38400\n" \
    "s_waitcnt lgkmcnt(0)\n" \
    "s_waitcnt vmcnt(6)\n" \
    "s_barrier\n" \
    "ds_read_b64_tr_b16 a[128:129], v16 offset:49152\n" \
    "ds_read_b64_tr_b16 a[130:131], v16 offset:50688\n" \
    "ds_read_b64_tr_b16 a[132:133], v28\n" \
    "ds_read_b64_tr_b16 a[134:135], v28 offset:1536\n" \
    "ds_read_b64_tr_b16 a[136:137], v17 offset:49152\n" \
    "ds_read_b64_tr_b16 a[138:139], v17 offset:50688\n" \
    "ds_read_b64_tr_b16 a[140:141], v29\n" \
    "ds_read_b64_tr_b16 a[142:143], v29 offset:1536\n" \
    "ds_read_b64_tr_b16 v[60:61], v18 offset:49152\n" \
    "ds_read_b64_tr_b16 v[62:63], v18 offset:50688\n" \
    "ds_read_b64_tr_b16 v[64:65], v30\n" \
    "ds_read_b64_tr_b16 v[66:67], v30 offset:1536\n" \
    "s_waitcnt lgkmcnt(2)\n" \
    "ds_write_b128 %[vres], v[60:63] offset:10240\n" \
    "s_waitcnt lgkmcnt(1)\n" \
    "ds_write_b128 %[vres], v[64:67] offset:11264\n" \
    "s_waitcnt lgkmcnt(0)\n" \
    "s_waitcnt vmcnt(0)\n" \
    "s_barrier\n" \
    "ds_read_b64_tr_b16 v[60:61], v16\n" \
    "ds_read_b64_tr_b16 v[62:63], v16 offset:1536\n" \
    "ds_read_b64_tr_b16 v[64:65], v16 offset:12288\n" \
    "ds_read_b64_tr_b16 v[66:67], v16 offset:13824\n" \
    "s_waitcnt lgkmcnt(2)\n" \
    "ds_write_b128 %[vres], v[60:63] offset:12288\n" \
    "s_waitcnt lgkmcnt(1)\n" \
    "ds_write_b128 %[vres], v[64:67] offset:13312\n" \
    "ds_read_b64_tr_b16 a[168:169], v17\n" \
    "ds_read_b64_tr_b16 a[170:171], v17 offset:1536\n" \
    "ds_read_b64_tr_b16 a[172:173], v17 offset:12288\n" \
    "ds_read_b64_tr_b16 a[174:175], v17 offset:13824\n" \
    "ds_read_b64_tr_b16 a[176:177], v18\n" \
    "ds_read_b64_tr_b16 a[178:179], v18 offset:1536\n" \
    "ds_read_b64_tr_b16 a[180:181], v18 offset:12288\n" \
    "ds_read_b64_tr_b16 a[182:183], v18 offset:13824\n" \
    "s_waitcnt lgkmcnt(0)\n" \
    "s_barrier\n" \
    "s_mov_b32 s60, 0\n" \
    "s_mul_i32 s61, %[wave], 163840\n" \
    "s_add_u32 s61, s61, 8388608\n" \
    "Lvo_step:\n" \
    "s_nop 1\n" \
    "s_lshl_b32 s62, s60, 8\n" \
    "s_and_b32 s62, s62, 0x2000\n" \
    "s_xor_b32 s62, s62, %[par]\n" \
    "s_lshl_b32 s79, s60, 16\n" \
    "s_add_u32 s62, s62, s79\n" \
    "s_lshl_b32 s64, s60, 12\n" \
    "s_and_b32 s64, s64, 0x2000\n" \
    "s_xor_b32 s64, s64, %[par]\n" \
    "s_lshl_b32 s79, s60, 20\n" \
    "s_add_u32 s64, s64, s79\n" \
    "s_add_u32 s64, s64, s61\n" \
    "s_add_u32 s65, s64, 65536\n" \
    "s_add_u32 s66, s64, 131072\n" \
    "v_mov_b64 v[36:37], 0\n" \
    "v_mov_b64 v[38:39], 0\n" \
    "v_mov_b64 v[40:41], 0\n" \
    "v_mov_b64 v[42:43], 0\n" \
    "v_mov_b64 v[44:45], 0\n" \
    "v_mov_b64 v[46:47], 0\n" \
    "v_mov_b64 v[48:49], 0\n" \
    "v_mov_b64 v[50:51], 0\n" \
    "v_mov_b64 v[52:53], 0\n" \
    "v_mov_b64 v[54:55], 0\n" \
    "ds_read_b128 v[60:63], %[vres]\n" \
    "ds_read_b128 v[64:67], %[vres] offset:1024\n" \
    "ds_read_b128 v[68:71], %[vres] offset:2048\n" \
    "ds_read_b128 v[72:75], %[vres] offset:3072\n" \
    "ds_read_b128 v[76:79], %[vres] offset:4096\n" \
    "ds_read_b128 v[80:83], %[vres] offset:5120\n" \
    "s_waitcnt lgkmcnt(0)\n" \
    "s_lshl_b32 s79, s60, 1\n" \
    "s_cmp_lg_u32 %[wave], 0\n" \
    "s_cbranch_scc1 Lvo_dbs\n" \
    "s_mov_b32 s63, 1048576\n" \
    "Lvo_dbw:\n" \
    "s_sub_u32 s63, s63, 1\n" \
    "s_cbranch_scc1 Lvo_tmo\n" \
    "buffer_load_dwordx2 v[56:57], v252, %[ws], s99 offen sc1\n" \
    "Lvo_dbx:\n" \
    "s_memrealtime s[96:97]\n" \
    "s_waitcnt vmcnt(0) lgkmcnt(0)\n" \
    "v_cmp_gt_u32 vcc, s79, v56\n" \
    "s_cbranch_vccnz Lvo_dbw\n" \
    "s_mul_i32 s96, s96, 24\n" \
    "v_and_b32 v56, 15, v57\n" \
    "v_lshl_add_u32 v57, v56, 5, v57\n" \
    "v_subrev_u32 v57, s96, v57\n" \
    "s_nop 1\n" \
    "v_max_i32_dpp v57, v57, v57 row_ror:8 row_mask:0xf bank_mask:0xf\n" \
    "s_nop 1\n" \
    "v_max_i32_dpp v57, v57, v57 row_ror:4 row_mask:0xf bank_mask:0xf\n" \
    "s_nop 1\n" \
    "v_max_i32_dpp v57, v57, v57 row_ror:2 row_mask:0xf bank_mask:0xf\n" \
    "s_nop 1\n" \
    "v_max_i32_dpp v57, v57, v57 row_ror:1 row_mask:0xf bank_mask:0xf\n" \
    "s_nop 1\n" \
    "v_readlane_b32 s97, v57, 0\n" \
    "v_readlane_b32 s98, v57, 16\n" \
    "s_nop 3\n" \
    "s_max_i32 s97, s97, s98\n" \
    "s_add_i32 s97, s97, %[d0]\n" \
    "s_min_i32 s97, s97, 8000\n" \
    "s_cmp_eq_u32 s60, 0\n" \
    "s_cselect_b32 s97, 0, s97\n" \
    "s_add_u32 s98, s96, s97\n" \
    "Lvo_dbd:\n" \
    "s_memrealtime s[44:45]\n" \
    "s_nop 2\n" \
    "s_memrealtime s[46:47]\n" \
    "s_nop 2\n" \
    "s_memrealtime s[48:49]\n" \
    "s_nop 2\n" \
    "s_memrealtime s[50:51]\n" \
    "s_nop 2\n" \
    "s_memrealtime s[52:53]\n" \
    "s_nop 2\n" \
    "s_memrealtime s[54:55]\n" \
    "s_waitcnt lgkmcnt(0)\n" \
    "s_add_u32 s44, s44, s46\n" \
    "s_add_u32 s48, s48, s50\n" \
    "s_add_u32 s52, s52, s54\n" \
    "s_add_u32 s44, s44, s48\n" \
    "s_add_u32 s44, s44, s52\n" \
    "s_lshl_b32 s96, s44, 2\n" \
    "s_sub_i32 s97, s98, s96\n" \
    "s_cmp_gt_i32 s97, 420\n" \
    "s_cbranch_scc1 Lvo_dbd\n" \
    ".p2align 4\n" \
    "Lvo_dbf:\n" \
    "s_sub_i32 s97, s97, 28\n" \
    "s_cmpk_gt_i32 s97, 200\n" \
    "s_cbranch_scc1 Lvo_dbf\n" \
    "Lvo_dbs:\n" \
    "s_barrier\n" \
    "buffer_load_dwordx4 v[12:15], v2, %[ws], s62 offen sc1\n" \
    "buffer_load_dwordx4 v[16:19], v2, %[ws], s62 offen offset:1024 sc1\n" \
    "s_mov_b32 s63, 1048575\n" \
    "s_cmp_lt_u32 %[wave], 2\n" \
    "s_cbranch_scc1 Lvo_q3\n" \
    "s_branch Lvo_q2\n" \
    "Lvo_p2:\n" \
    "s_sub_u32 s63, s63, 1\n" \
    "s_cbranch_scc1 Lvo_tmo\n" \
    "buffer_load_dwordx4 v[12:15], v2, %[ws], s62 offen sc1\n" \
    "buffer_load_dwordx4 v[16:19], v2, %[ws], s62 offen offset:1024 sc1\n" \
    "Lvo_q2:\n" \
    "s_waitcnt vmcnt(1)\n" \
    "ds_write_b128 v3, v[12:15]\n" \
    "v_max_u32 v10, v12, v13\n" \
    "v_max3_u32 v10, v10, v14, v15\n" \
    "s_waitcnt vmcnt(0)\n" \
    "ds_write_b128 v3, v[16:19] offset:1024\n" \
    "v_max3_u32 v10, v10, v16, v17\n" \
    "v_max3_u32 v10, v10, v18, v19\n" \
    "v_cmp_eq_u32 vcc, -1, v10\n" \
    "s_cbranch_vccnz Lvo_p2\n" \
    "s_branch Lvo_got\n" \
    "Lvo_p3:\n" \
    "s_sub_u32 s63, s63, 1\n" \
    "s_cbranch_scc1 Lvo_tmo\n" \
    "buffer_load_dwordx4 v[12:15], v2, %[ws], s62 offen sc1\n" \
    "buffer_load_dwordx4 v[16:19], v2, %[ws], s62 offen offset:1024 sc1\n" \
    "Lvo_q3:\n" \
    "buffer_load_dwordx4 v[20:23], v2, %[ws], s62 offen offset:2048 sc1\n" \
    "s_waitcnt vmcnt(2)\n" \
    "ds_write_b128 v3, v[12:15]\n" \
    "v_max_u32 v10, v12, v13\n" \
    "v_max3_u32 v10, v10, v14, v15\n" \
    "s_waitcnt vmcnt(1)\n" \
    "ds_write_b128 v3, v[16:19] offset:1024\n" \
    "v_max3_u32 v10, v10, v16, v17\n" \
    "v_max3_u32 v10, v10, v18, v19\n" \
    "s_waitcnt vmcnt(0)\n" \
    "ds_write_b128 v3, v[20:23] offset:2048\n" \
    "v_max3_u32 v10, v10, v20, v21\n" \
    "v_max3_u32 v10, v10, v22, v23\n" \
    "v_cmp_eq_u32 vcc, -1, v10\n" \
    "s_cbranch_vccnz Lvo_p3\n" \
    "Lvo_got:\n" \
    "s_waitcnt lgkmcnt(0)\n" \
    "s_barrier\n" \
    "ds_read_b64_tr_b16 v[12:13], v4\n" \
    "ds_read_b64_tr_b16 v[14:15], v4 offset:64\n" \
    "ds_read_b64_tr_b16 v[16:17], v4 offset:1024\n" \
    "ds_read_b64_tr_b16 v[18:19], v4 offset:1088\n" \
    "ds_read_b64_tr_b16 v[20:21], v4 offset:2048\n" \
    "ds_read_b64_tr_b16 v[22:23], v4 offset:2112\n" \
    "ds_read_b64_tr_b16 v[24:25], v4 offset:3072\n" \
    "ds_read_b64_tr_b16 v[26:27], v4 offset:3136\n" \
    "s_waitcnt lgkmcnt(6)\n" \
    "v_smfmac_f32_16x16x64_bf16 v[44:47], v[12:15], a[0:7], v5\n" \
    "ds_read_b64_tr_b16 v[28:29], v4 offset:4096\n" \
    "ds_read_b64_tr_b16 v[30:31], v4 offset:4160\n" \
    "v_smfmac_f32_16x16x64_bf16 v[48:51], v[12:15], a[8:15], v5\n" \
    "v_smfmac_f32_16x16x64_bf16 v[52:55], v[12:15], a[16:23], v5\n" \
    "s_waitcnt lgkmcnt(6)\n" \
    "v_smfmac_f32_16x16x64_bf16 v[44:47], v[16:19], v[60:67], v5\n" \
    "ds_read_b64_tr_b16 v[32:33], v4 offset:5120\n" \
    "ds_read_b64_tr_b16 v[34:35], v4 offset:5184\n" \
    "v_smfmac_f32_16x16x64_bf16 v[48:51], v[16:19], a[24:31], v5\n" \
    "v_smfmac_f32_16x16x64_bf16 v[52:55], v[16:19], a[32:39], v5\n" \
    "ds_read_b128 v[60:63], %[vres] offset:6144\n" \
    "ds_read_b128 v[64:67], %[vres] offset:7168\n" \
    "s_waitcnt lgkmcnt(8)\n" \
    "v_smfmac_f32_16x16x64_bf16 v[44:47], v[20:23], a[40:47], v5\n" \
    "ds_read_b64_tr_b16 v[12:13], v4 offset:6144\n" \
    "ds_read_b64_tr_b16 v[14:15], v4 offset:6208\n" \
    "v_smfmac_f32_16x16x64_bf16 v[48:51], v[20:23], v[68:75], v5\n" \
    "v_smfmac_f32_16x16x64_bf16 v[52:55], v[20:23], a[48:55], v5\n" \
    "s_waitcnt lgkmcnt(8)\n" \
    "v_smfmac_f32_16x16x64_bf16 v[44:47], v[24:27], a[56:63], v5\n" \
    "ds_read_b64_tr_b16 v[16:17], v4 offset:7168\n" \
    "ds_read_b64_tr_b16 v[18:19], v4 offset:7232\n" \
    "ds_read_b128 v[68:71], %[vres] offset:8192\n" \
    "ds_read_b128 v[72:75], %[vres] offset:9216\n" \
    "v_smfmac_f32_16x16x64_bf16 v[48:51], v[24:27], a[64:71], v5\n" \
    "v_smfmac_f32_16x16x64_bf16 v[52:55], v[24:27], v[76:83], v5\n" \
    "s_waitcnt lgkmcnt(10)\n" \
    "v_smfmac_f32_16x16x64_bf16 v[44:47], v[28:31], a[72:79], v5\n" \
    "ds_read_b64_tr_b16 v[20:21], v4 offset:8192\n" \
    "ds_read_b64_tr_b16 v[22:23], v4 offset:8256\n" \
    "v_smfmac_f32_16x16x64_bf16 v[48:51], v[28:31], a[80:87], v5\n" \
    "s_waitcnt lgkmcnt(8)\n" \
    "ds_read_b128 v[76:79], %[vres] offset:10240\n" \
    "ds_read_b128 v[80:83], %[vres] offset:11264\n" \
    "v_smfmac_f32_16x16x64_bf16 v[52:55], v[28:31], a[88:95], v5\n" \
    "v_smfmac_f32_16x16x64_bf16 v[44:47], v[32:35], v[60:67], v5\n" \
    "ds_read_b64_tr_b16 v[24:25], v4 offset:9216\n" \
    "ds_read_b64_tr_b16 v[26:27], v4 offset:9280\n" \
    "v_smfmac_f32_16x16x64_bf16 v[48:51], v[32:35], a[96:103], v5\n" \
    "v_smfmac_f32_16x16x64_bf16 v[52:55], v[32:35], a[104:111], v5\n" \
    "s_waitcnt lgkmcnt(8)\n" \
    "ds_read_b128 v[60:63], %[vres] offset:12288\n" \
    "ds_read_b128 v[64:67], %[vres] offset:13312\n" \
    "v_smfmac_f32_16x16x64_bf16 v[44:47], v[12:15], a[112:119], v5\n" \
    "s_waitcnt lgkmcnt(8)\n" \
    "v_smfmac_f32_16x16x64_bf16 v[48:51], v[12:15], v[68:75], v5\n" \
    "v_smfmac_f32_16x16x64_bf16 v[52:55], v[12:15], a[120:127], v5\n" \
    "v_smfmac_f32_16x16x64_bf16 v[44:47], v[16:19], a[128:135], v5\n" \
    "v_smfmac_f32_16x16x64_bf16 v[48:51], v[16:19], a[136:143], v5\n" \
    "s_waitcnt lgkmcnt(4)\n" \
    "v_smfmac_f32_16x16x64_bf16 v[52:55], v[16:19], v[76:83], v5\n" \
    "v_smfmac_f32_16x16x64_bf16 v[44:47], v[20:23], a[144:151], v5\n" \
    "v_smfmac_f32_16x16x64_bf16 v[48:51], v[20:23], a[152:159], v5\n" \
    "s_waitcnt lgkmcnt(0)\n" \
    "v_smfmac_f32_16x16x64_bf16 v[44:47], v[24:27], v[60:67], v5\n" \
    "v_smfmac_f32_16x16x64_bf16 v[48:51], v[24:27], a[168:175], v5\n" \
    "v_smfmac_f32_16x16x64_bf16 v[52:55], v[20:23], a[160:167], v5\n" \
    "v_smfmac_f32_16x16x64_bf16 v[52:55], v[24:27], a[176:183], v5\n" \
    "s_nop 5\n" \
    "v_permlane32_swap_b32 v44, v48\n" \
    "v_permlane32_swap_b32 v45, v49\n" \
    "v_permlane32_swap_b32 v46, v50\n" \
    "v_permlane32_swap_b32 v47, v51\n" \
    "v_permlane32_swap_b32 v52, v54\n" \
    "v_permlane32_swap_b32 v53, v55\n" \
    "v_pk_add_f32 v[44:45], v[44:45], v[48:49]\n" \
    "v_pk_add_f32 v[46:47], v[46:47], v[50:51]\n" \
    "v_pk_add_f32 v[52:53], v[52:53], v[54:55]\n" \
    "v_cvt_pk_bf16_f32 v56, v44, v45\n" \
    "v_cvt_pk_bf16_f32 v57, v46, v47\n" \
    "v_cvt_pk_bf16_f32 v58, v52, v53\n" \
    "ds_write_b64 v6, v[56:57]\n" \
    "ds_write_b32 v7, v58\n" \
    "v_mov_b64 v[44:45], 0\n" \
    "v_mov_b64 v[46:47], 0\n" \
    "v_mov_b64 v[48:49], 0\n" \
    "v_mov_b64 v[50:51], 0\n" \
    "v_mov_b64 v[52:53], 0\n" \
    "v_mov_b64 v[54:55], 0\n" \
    "s_waitcnt lgkmcnt(0)\n" \
    "s_barrier\n" \
    "ds_read_b64_tr_b16 v[12:13], v11\n" \
    "ds_read_b64_tr_b16 v[14:15], v11 offset:64\n" \
    "ds_read_b64_tr_b16 v[16:17], v11 offset:512\n" \
    "ds_read_b64_tr_b16 v[18:19], v11 offset:576\n" \
    "ds_read_b64_tr_b16 v[20:21], v11 offset:1024\n" \
    "ds_read_b64_tr_b16 v[22:23], v11 offset:1088\n" \
    "ds_read_b64_tr_b16 v[24:25], v11 offset:1536\n" \
    "ds_read_b64_tr_b16 v[26:27], v11 offset:1600\n" \
    "ds_read_b64_tr_b16 v[28:29], v11 offset:2048\n" \
    "ds_read_b64_tr_b16 v[30:31], v11 offset:2112\n" \
    "ds_read_b64_tr_b16 v[32:33], v11 offset:2560\n" \
    "ds_read_b64_tr_b16 v[34:35], v11 offset:2624\n" \
    "s_waitcnt lgkmcnt(10)\n" \
    "v_smfmac_f32_16x16x64_bf16 v[36:39], v[12:15], a[184:191], v5\n" \
    "s_waitcnt lgkmcnt(8)\n" \
    "v_smfmac_f32_16x16x64_bf16 v[36:39], v[16:19], a[192:199], v5\n" \
    "s_waitcnt lgkmcnt(6)\n" \
    "v_smfmac_f32_16x16x64_bf16 v[36:39], v[20:23], a[200:207], v5\n" \
    "s_waitcnt lgkmcnt(4)\n" \
    "v_smfmac_f32_16x16x64_bf16 v[36:39], v[24:27], a[208:215], v5\n" \
    "s_waitcnt lgkmcnt(2)\n" \
    "v_smfmac_f32_16x16x64_bf16 v[36:39], v[28:31], a[216:223], v5\n" \
    "s_waitcnt lgkmcnt(0)\n" \
    "v_smfmac_f32_16x16x64_bf16 v[36:39], v[32:35], a[224:231], v5\n" \
    "s_nop 1\n" \
    "v_smfmac_f32_16x16x64_bf16 v[40:43], v[12:15], a[232:239], v5\n" \
    "s_nop 1\n" \
    "v_smfmac_f32_16x16x64_bf16 v[40:43], v[16:19], a[240:247], v5\n" \
    "s_nop 1\n" \
    "v_smfmac_f32_16x16x64_bf16 v[40:43], v[20:23], a[248:255], v5\n" \
    "v_cvt_pk_bf16_f32 v36, v36, v37\n" \
    "v_cvt_pk_bf16_f32 v38, v38, v39\n" \
    "v_smfmac_f32_16x16x64_bf16 v[40:43], v[24:27], v[84:91], v5\n" \
    "v_min_u32 v36, -2, v36\n" \
    "v_min_u32 v38, -2, v38\n" \
    "v_smfmac_f32_16x16x64_bf16 v[40:43], v[28:31], v[92:99], v5\n" \
    "v_smfmac_f32_16x16x64_bf16 v[40:43], v[32:35], v[100:107], v5\n" \
    "s_nop 1\n" \
    "v_smfmac_f32_16x16x64_bf16 v[44:47], v[12:15], v[108:115], v5\n" \
    "s_nop 1\n" \
    "v_smfmac_f32_16x16x64_bf16 v[44:47], v[16:19], v[116:123], v5\n" \
    "s_nop 1\n" \
    "v_smfmac_f32_16x16x64_bf16 v[44:47], v[20:23], v[124:131], v5\n" \
    "v_cvt_pk_bf16_f32 v37, v40, v41\n" \
    "v_cvt_pk_bf16_f32 v39, v42, v43\n" \
    "v_smfmac_f32_16x16x64_bf16 v[44:47], v[24:27], v[132:139], v5\n" \
    "v_min_u32 v37, -2, v37\n" \
    "v_min_u32 v39, -2, v39\n" \
    "v_smfmac_f32_16x16x64_bf16 v[44:47], v[28:31], v[140:147], v5\n" \
    "v_permlane16_swap_b32 v36, v37\n" \
    "v_smfmac_f32_16x16x64_bf16 v[44:47], v[32:35], v[148:155], v5\n" \
    "v_permlane16_swap_b32 v38, v39\n" \
    "v_smfmac_f32_16x16x64_bf16 v[48:51], v[12:15], v[156:163], v5\n" \
    "buffer_store_dwordx4 v[36:39], v8, %[ws], s64 offen sc1\n" \
    "s_nop 0\n" \
    "v_smfmac_f32_16x16x64_bf16 v[48:51], v[16:19], v[164:171], v5\n" \
    "s_nop 1\n" \
    "v_smfmac_f32_16x16x64_bf16 v[48:51], v[20:23], v[172:179], v5\n" \
    "v_cvt_pk_bf16_f32 v44, v44, v45\n" \
    "v_cvt_pk_bf16_f32 v46, v46, v47\n" \
    "v_smfmac_f32_16x16x64_bf16 v[48:51], v[24:27], v[180:187], v5\n" \
    "v_min_u32 v44, -2, v44\n" \
    "v_min_u32 v46, -2, v46\n" \
    "v_smfmac_f32_16x16x64_bf16 v[48:51], v[28:31], v[188:195], v5\n" \
    "v_smfmac_f32_16x16x64_bf16 v[48:51], v[32:35], v[196:203], v5\n" \
    "s_nop 1\n" \
    "v_smfmac_f32_16x16x64_bf16 v[52:55], v[12:15], v[204:211], v5\n" \
    "s_nop 1\n" \
    "v_smfmac_f32_16x16x64_bf16 v[52:55], v[16:19], v[212:219], v5\n" \
    "s_nop 1\n" \
    "v_smfmac_f32_16x16x64_bf16 v[52:55], v[20:23], v[220:227], v5\n" \
    "v_cvt_pk_bf16_f32 v45, v48, v49\n" \
    "v_cvt_pk_bf16_f32 v47, v50, v51\n" \
    "v_smfmac_f32_16x16x64_bf16 v[52:55], v[24:27], v[228:235], v5\n" \
    "v_min_u32 v45, -2, v45\n" \
    "v_min_u32 v47, -2, v47\n" \
    "v_smfmac_f32_16x16x64_bf16 v[52:55], v[28:31], v[236:243], v5\n" \
    "v_permlane16_swap_b32 v44, v45\n" \
    "v_smfmac_f32_16x16x64_bf16 v[52:55], v[32:35], v[244:251], v5\n" \
    "v_permlane16_swap_b32 v46, v47\n" \
    "buffer_store_dwordx4 v[44:47], v8, %[ws], s65 offen sc1\n" \
    "s_nop 5\n" \
    "v_cvt_pk_bf16_f32 v52, v52, v53\n" \
    "v_cvt_pk_bf16_f32 v53, v54, v55\n" \
    "v_min_u32 v52, -2, v52\n" \
    "v_min_u32 v53, -2, v53\n" \
    "s_nop 1\n" \
    "v_permlane16_swap_b32 v52, v53\n" \
    "buffer_store_dwordx2 v[52:53], v9, %[ws], s66 offen sc1\n" \
    "s_cmp_ge_u32 s60, 60\n" \
    "s_cbranch_scc1 Lvo_nopo\n" \
    "v_mov_b32 v56, -1\n" \
    "v_mov_b32 v57, -1\n" \
    "v_mov_b32 v58, -1\n" \
    "v_mov_b32 v59, -1\n" \
    "s_add_u32 s72, s60, 4\n" \
    "s_lshl_b32 s67, s72, 12\n" \
    "s_and_b32 s67, s67, 0x2000\n" \
    "s_xor_b32 s67, s67, %[par]\n" \
    "s_lshl_b32 s79, s72, 20\n" \
    "s_add_u32 s67, s67, s79\n" \
    "s_add_u32 s67, s67, s61\n" \
    "buffer_store_dwordx4 v[56:59], v8, %[ws], s67 offen sc1\n" \
    "s_add_u32 s67, s67, 65536\n" \
    "buffer_store_dwordx4 v[56:59], v8, %[ws], s67 offen sc1\n" \
    "s_add_u32 s67, s67, 65536\n" \
    "buffer_store_dwordx2 v[56:57], v9, %[ws], s67 offen sc1\n" \
    "s_lshl_b32 s67, s72, 10\n" \
    "s_and_b32 s67, s67, 0x2000\n" \
    "s_xor_b32 s67, s67, %[par]\n" \
    "s_lshl_b32 s79, s72, 18\n" \
    "s_add_u32 s67, s67, s79\n" \
    "s_add_u32 s67, s67, %[kmb]\n" \
    "v_and_b32 v253, 1023, %[vres]\n" \
    "buffer_store_dwordx4 v[56:59], v253, %[ws], s67 offen sc1\n" \
    "Lvo_nopo:\n" \
    "s_add_u32 s60, s60, 1\n" \
    "s_cmp_lt_u32 s60, 64\n" \
    "s_cbranch_scc1 Lvo_step\n" \
    "s_branch Lvo_end\n" \
    "Lvo_tmo:\n" \
    "s_add_u32 s78, s60, 768\n" \
    "v_mov_b32 v12, s78\n" \
    "v_mov_b32 v13, 0\n" \
    "s_mov_b32 s78, 253757696\n" \
    "s_nop 4\n" \
    "buffer_store_dword v12, v13, %[ws], s78 offen sc1\n" \
    "s_endpgm\n" \
    "Lvo_end:\n"
#define ASM_R \
    "s_mov_b32 s60, 0\n" \
    "s_mov_b32 s61, 0\n" \
    "s_xor_b32 s99, %[par], 0x2000\n" \
    "s_add_u32 s99, s99, 6291456\n" \
    "v_mov_b32 v146, -1\n" \
    "v_mov_b32 v147, -1\n" \
    "v_mov_b32 v148, -1\n" \
    "v_mov_b32 v149, -1\n" \
    "v_mov_b32 v145, 0\n" \
    "s_cmp_lg_u32 %[wave], 2\n" \
    "s_cbranch_scc1 Lr_go\n" \
    "Lr_ctl:\n" \
    "s_add_u32 s60, s60, 1\n" \
    "s_lshl_b32 s79, s60, 1\n" \
    "s_mov_b32 s63, 1048576\n" \
    "buffer_load_dwordx2 v[142:143], %[dbp], %[ws], s99 offen sc1\n" \
    "s_barrier\n" \
    "s_branch Lr_dbx\n" \
    "Lr_dbw:\n" \
    "s_sub_u32 s63, s63, 1\n" \
    "s_cbranch_scc1 Lr_tmo\n" \
    "buffer_load_dwordx2 v[142:143], %[dbp], %[ws], s99 offen sc1\n" \
    "Lr_dbx:\n" \
    "s_memrealtime s[96:97]\n" \
    "s_waitcnt vmcnt(0) lgkmcnt(0)\n" \
    "v_cmp_gt_u32 vcc, s79, v142\n" \
    "s_cbranch_vccnz Lr_dbw\n" \
    "s_mul_i32 s96, s96, 24\n" \
    "v_and_b32 v142, 15, v143\n" \
    "v_subrev_u32 v143, s96, v143\n" \
    "s_nop 1\n" \
    "v_max_i32_dpp v143, v143, v143 row_ror:8 row_mask:0xf bank_mask:0xf\n" \
    "s_nop 1\n" \
    "v_max_i32_dpp v143, v143, v143 row_ror:4 row_mask:0xf bank_mask:0xf\n" \
    "s_nop 1\n" \
    "v_max_i32_dpp v143, v143, v143 row_ror:2 row_mask:0xf bank_mask:0xf\n" \
    "s_nop 1\n" \
    "v_max_i32_dpp v143, v143, v143 row_ror:1 row_mask:0xf bank_mask:0xf\n" \
    "s_nop 1\n" \
    "v_readlane_b32 s97, v143, 0\n" \
    "v_readlane_b32 s98, v143, 16\n" \
    "s_nop 3\n" \
    "s_max_i32 s97, s97, s98\n" \
    "s_add_i32 s97, s97, %[d0]\n" \
    "s_min_i32 s97, s97, 8000\n" \
    "s_add_u32 s98, s96, s97\n" \
    "Lr_dbd:\n" \
    "s_memrealtime s[44:45]\n" \
    "s_nop 2\n" \
    "s_memrealtime s[46:47]\n" \
    "s_nop 2\n" \
    "s_memrealtime s[48:49]\n" \
    "s_nop 2\n" \
    "s_memrealtime s[50:51]\n" \
    "s_nop 2\n" \
    "s_memrealtime s[52:53]\n" \
    "s_nop 2\n" \
    "s_memrealtime s[54:55]\n" \
    "s_waitcnt lgkmcnt(0)\n" \
    "s_add_u32 s44, s44, s46\n" \
    "s_add_u32 s48, s48, s50\n" \
    "s_add_u32 s52, s52, s54\n" \
    "s_add_u32 s44, s44, s48\n" \
    "s_add_u32 s44, s44, s52\n" \
    "s_lshl_b32 s96, s44, 2\n" \
    "s_sub_i32 s97, s98, s96\n" \
    "s_cmp_gt_i32 s97, 420\n" \
    "s_cbranch_scc1 Lr_dbd\n" \
    ".p2align 4\n" \
    "Lr_dbf:\n" \
    "s_sub_i32 s97, s97, 28\n" \
    "s_cmpk_gt_i32 s97, 200\n" \
    "s_cbranch_scc1 Lr_dbf\n" \
    "Lr_dbs:\n" \
    "s_barrier\n" \
    "s_add_u32 s98, s98, %[d2]\n" \
    "v_max_u32_dpp v142, v142, v142 row_ror:8 row_mask:0xf bank_mask:0xf\n" \
    "s_nop 1\n" \
    "v_max_u32_dpp v142, v142, v142 row_ror:4 row_mask:0xf bank_mask:0xf\n" \
    "s_nop 1\n" \
    "v_max_u32_dpp v142, v142, v142 row_ror:2 row_mask:0xf bank_mask:0xf\n" \
    "s_nop 1\n" \
    "v_max_u32_dpp v142, v142, v142 row_ror:1 row_mask:0xf bank_mask:0xf\n" \
    "s_nop 1\n" \
    "v_readlane_b32 s96, v142, 0\n" \
    "v_readlane_b32 s97, v142, 16\n" \
    "s_nop 3\n" \
    "s_max_u32 s96, s96, s97\n" \
    "s_lshl_b32 s96, s96, 5\n" \
    "s_add_u32 s98, s98, s96\n" \
    "Lr_csl:\n" \
    "s_sleep 1\n" \
    "s_memrealtime s[96:97]\n" \
    "s_waitcnt lgkmcnt(0)\n" \
    "s_mul_i32 s96, s96, 24\n" \
    "s_sub_u32 s97, s96, s98\n" \
    "s_cmp_lt_i32 s97, 0\n" \
    "s_cbranch_scc1 Lr_csl\n" \
    "s_cmp_lt_u32 s60, 63\n" \
    "s_cbranch_scc1 Lr_ctl\n" \
    "s_endpgm\n" \
    "Lr_go:\n" \
    "s_mov_b32 s60, %[wave]\n" \
    "s_mov_b32 s74, 0\n" \
    "s_cmp_eq_u32 %[wave], 0\n" \
    "s_cbranch_scc1 Lr_late\n" \
    "Lr_step:\n" \
    "s_nop 1\n" \
    "s_cmp_lt_u32 s60, s61\n" \
    "s_cbranch_scc1 Lr_b0\n" \
    "s_mov_b32 s67, 253775872\n" \
    "buffer_load_dword v144, %[fl], %[ws], s67 offen sc1\n" \
    "s_waitcnt vmcnt(0)\n" \
    "v_min_u32_dpp v144, v144, v144 row_ror:8 row_mask:0xf bank_mask:0xf\n" \
    "s_nop 1\n" \
    "v_min_u32_dpp v144, v144, v144 row_ror:4 row_mask:0xf bank_mask:0xf\n" \
    "s_nop 1\n" \
    "v_min_u32_dpp v144, v144, v144 row_ror:2 row_mask:0xf bank_mask:0xf\n" \
    "s_nop 1\n" \
    "v_min_u32_dpp v144, v144, v144 row_ror:1 row_mask:0xf bank_mask:0xf\n" \
    "s_nop 1\n" \
    "v_readlane_b32 s61, v144, 0\n" \
    "v_readlane_b32 s79, v144, 16\n" \
    "s_nop 3\n" \
    "s_min_u32 s61, s61, s79\n" \
    "s_cmp_lt_u32 s60, s61\n" \
    "s_cbranch_scc0 Lr_b1\n" \
    "Lr_b0:\n" \
    "s_mul_i32 s66, s60, 2621440\n" \
    "s_add_u32 s66, s66, %[ub]\n" \
    "s_lshr_b32 s79, s66, 8\n" \
    "s_andn2_b32 s79, 0x2000, s79\n" \
    "s_xor_b32 s79, s79, %[par]\n" \
    "s_add_u32 s66, s66, s79\n" \
    "buffer_load_dwordx4 a[0:3], %[l16], %[ws], s66 offen sc1\n" \
    "buffer_load_dwordx4 a[4:7], %[l16], %[ws], s66 offen offset:1024 sc1\n" \
    "buffer_load_dwordx4 a[8:11], %[l16], %[ws], s66 offen offset:2048 sc1\n" \
    "buffer_load_dwordx4 a[12:15], %[l16], %[ws], s66 offen offset:3072 sc1\n" \
    "s_add_u32 s66, s66, 4096\n" \
    "buffer_load_dwordx4 a[16:19], %[l16], %[ws], s66 offen sc1\n" \
    "buffer_load_dwordx4 a[20:23], %[l16], %[ws], s66 offen offset:1024 sc1\n" \
    "buffer_load_dwordx4 a[24:27], %[l16], %[ws], s66 offen offset:2048 sc1\n" \
    "buffer_load_dwordx4 a[28:31], %[l16], %[ws], s66 offen offset:3072 sc1\n" \
    "s_add_u32 s66, s66, 12288\n" \
    "buffer_load_dwordx4 a[32:35], %[l16], %[ws], s66 offen sc1\n" \
    "buffer_load_dwordx4 a[36:39], %[l16], %[ws], s66 offen offset:1024 sc1\n" \
    "buffer_load_dwordx4 a[40:43], %[l16], %[ws], s66 offen offset:2048 sc1\n" \
    "buffer_load_dwordx4 a[44:47], %[l16], %[ws], s66 offen offset:3072 sc1\n" \
    "s_add_u32 s66, s66, 4096\n" \
    "buffer_load_dwordx4 a[48:51], %[l16], %[ws], s66 offen sc1\n" \
    "buffer_load_dwordx4 a[52:55], %[l16], %[ws], s66 offen offset:1024 sc1\n" \
    "buffer_load_dwordx4 a[56:59], %[l16], %[ws], s66 offen offset:2048 sc1\n" \
    "buffer_load_dwordx4 a[60:63], %[l16], %[ws], s66 offen offset:3072 sc1\n" \
    "s_add_u32 s66, s66, 12288\n" \
    "buffer_load_dwordx4 a[64:67], %[l16], %[ws], s66 offen sc1\n" \
    "buffer_load_dwordx4 a[68:71], %[l16], %[ws], s66 offen offset:1024 sc1\n" \
    "buffer_load_dwordx4 a[72:75], %[l16], %[ws], s66 offen offset:2048 sc1\n" \
    "buffer_load_dwordx4 a[76:79], %[l16], %[ws], s66 offen offset:3072 sc1\n" \
    "s_add_u32 s66, s66, 4096\n" \
    "buffer_load_dwordx4 a[80:83], %[l16], %[ws], s66 offen sc1\n" \
    "buffer_load_dwordx4 a[84:87], %[l16], %[ws], s66 offen offset:1024 sc1\n" \
    "buffer_load_dwordx4 a[88:91], %[l16], %[ws], s66 offen offset:2048 sc1\n" \
    "buffer_load_dwordx4 a[92:95], %[l16], %[ws], s66 offen offset:3072 sc1\n" \
    "s_barrier\n" \
    "s_add_u32 s66, s66, 12288\n" \
    "buffer_load_dwordx4 a[96:99], %[l16], %[ws], s66 offen sc1\n" \
    "buffer_load_dwordx4 a[100:103], %[l16], %[ws], s66 offen offset:1024 sc1\n" \
    "buffer_load_dwordx4 a[104:107], %[l16], %[ws], s66 offen offset:2048 sc1\n" \
    "buffer_load_dwordx4 a[108:111], %[l16], %[ws], s66 offen offset:3072 sc1\n" \
    "s_add_u32 s66, s66, 4096\n" \
    "buffer_load_dwordx4 a[112:115], %[l16], %[ws], s66 offen sc1\n" \
    "buffer_load_dwordx4 a[116:119], %[l16], %[ws], s66 offen offset:1024 sc1\n" \
    "buffer_load_dwordx4 a[120:123], %[l16], %[ws], s66 offen offset:2048 sc1\n" \
    "buffer_load_dwordx4 a[124:127], %[l16], %[ws], s66 offen offset:3072 sc1\n" \
    "s_barrier\n" \
    "s_branch Lr_have\n" \
    "Lr_b1:\n" \
    "s_barrier\n" \
    "s_mov_b32 s63, 1048576\n" \
    "s_lshl_b32 s72, s60, 1\n" \
    "Lr_b2:\n" \
    "s_sub_u32 s63, s63, 1\n" \
    "s_cbranch_scc1 Lr_tmo\n" \
    "buffer_load_dword v138, %[dbp], %[ws], s99 offen sc1\n" \
    "s_mov_b32 s67, 253775872\n" \
    "buffer_load_dword v144, %[fl], %[ws], s67 offen sc1\n" \
    "s_waitcnt vmcnt(0)\n" \
    "v_min_u32_dpp v144, v144, v144 row_ror:8 row_mask:0xf bank_mask:0xf\n" \
    "s_nop 1\n" \
    "v_min_u32_dpp v144, v144, v144 row_ror:4 row_mask:0xf bank_mask:0xf\n" \
    "s_nop 1\n" \
    "v_min_u32_dpp v144, v144, v144 row_ror:2 row_mask:0xf bank_mask:0xf\n" \
    "s_nop 1\n" \
    "v_min_u32_dpp v144, v144, v144 row_ror:1 row_mask:0xf bank_mask:0xf\n" \
    "s_nop 1\n" \
    "v_readlane_b32 s61, v144, 0\n" \
    "v_readlane_b32 s79, v144, 16\n" \
    "s_nop 3\n" \
    "s_min_u32 s61, s61, s79\n" \
    "s_cmp_lt_u32 s60, s61\n" \
    "s_cbranch_scc0 Lr_b3\n" \
    "s_mul_i32 s66, s60, 2621440\n" \
    "s_add_u32 s66, s66, %[ub]\n" \
    "s_lshr_b32 s79, s66, 8\n" \
    "s_andn2_b32 s79, 0x2000, s79\n" \
    "s_xor_b32 s79, s79, %[par]\n" \
    "s_add_u32 s66, s66, s79\n" \
    "buffer_load_dwordx4 a[0:3], %[l16], %[ws], s66 offen sc1\n" \
    "buffer_load_dwordx4 a[4:7], %[l16], %[ws], s66 offen offset:1024 sc1\n" \
    "buffer_load_dwordx4 a[8:11], %[l16], %[ws], s66 offen offset:2048 sc1\n" \
    "buffer_load_dwordx4 a[12:15], %[l16], %[ws], s66 offen offset:3072 sc1\n" \
    "s_add_u32 s66, s66, 4096\n" \
    "buffer_load_dwordx4 a[16:19], %[l16], %[ws], s66 offen sc1\n" \
    "buffer_load_dwordx4 a[20:23], %[l16], %[ws], s66 offen offset:1024 sc1\n" \
    "buffer_load_dwordx4 a[24:27], %[l16], %[ws], s66 offen offset:2048 sc1\n" \
    "buffer_load_dwordx4 a[28:31], %[l16], %[ws], s66 offen offset:3072 sc1\n" \
    "s_add_u32 s66, s66, 12288\n" \
    "buffer_load_dwordx4 a[32:35], %[l16], %[ws], s66 offen sc1\n" \
    "buffer_load_dwordx4 a[36:39], %[l16], %[ws], s66 offen offset:1024 sc1\n" \
    "buffer_load_dwordx4 a[40:43], %[l16], %[ws], s66 offen offset:2048 sc1\n" \
    "buffer_load_dwordx4 a[44:47], %[l16], %[ws], s66 offen offset:3072 sc1\n" \
    "s_add_u32 s66, s66, 4096\n" \
    "buffer_load_dwordx4 a[48:51], %[l16], %[ws], s66 offen sc1\n" \
    "buffer_load_dwordx4 a[52:55], %[l16], %[ws], s66 offen offset:1024 sc1\n" \
    "buffer_load_dwordx4 a[56:59], %[l16], %[ws], s66 offen offset:2048 sc1\n" \
    "buffer_load_dwordx4 a[60:63], %[l16], %[ws], s66 offen offset:3072 sc1\n" \
    "s_add_u32 s66, s66, 12288\n" \
    "buffer_load_dwordx4 a[64:67], %[l16], %[ws], s66 offen sc1\n" \
    "buffer_load_dwordx4 a[68:71], %[l16], %[ws], s66 offen offset:1024 sc1\n" \
    "buffer_load_dwordx4 a[72:75], %[l16], %[ws], s66 offen offset:2048 sc1\n" \
    "buffer_load_dwordx4 a[76:79], %[l16], %[ws], s66 offen offset:3072 sc1\n" \
    "s_add_u32 s66, s66, 4096\n" \
    "buffer_load_dwordx4 a[80:83], %[l16], %[ws], s66 offen sc1\n" \
    "buffer_load_dwordx4 a[84:87], %[l16], %[ws], s66 offen offset:1024 sc1\n" \
    "buffer_load_dwordx4 a[88:91], %[l16], %[ws], s66 offen offset:2048 sc1\n" \
    "buffer_load_dwordx4 a[92:95], %[l16], %[ws], s66 offen offset:3072 sc1\n" \
    "s_add_u32 s66, s66, 12288\n" \
    "buffer_load_dwordx4 a[96:99], %[l16], %[ws], s66 offen sc1\n" \
    "buffer_load_dwordx4 a[100:103], %[l16], %[ws], s66 offen offset:1024 sc1\n" \
    "buffer_load_dwordx4 a[104:107], %[l16], %[ws], s66 offen offset:2048 sc1\n" \
    "buffer_load_dwordx4 a[108:111], %[l16], %[ws], s66 offen offset:3072 sc1\n" \
    "s_add_u32 s66, s66, 4096\n" \
    "buffer_load_dwordx4 a[112:115], %[l16], %[ws], s66 offen sc1\n" \
    "buffer_load_dwordx4 a[116:119], %[l16], %[ws], s66 offen offset:1024 sc1\n" \
    "buffer_load_dwordx4 a[120:123], %[l16], %[ws], s66 offen offset:2048 sc1\n" \
    "buffer_load_dwordx4 a[124:127], %[l16], %[ws], s66 offen offset:3072 sc1\n" \
    "s_barrier\n" \
    "s_branch Lr_have\n" \
    "Lr_b3:\n" \
    "v_cmp_gt_u32 vcc, s72, v138\n" \
    "s_cbranch_vccnz Lr_b2\n" \
    "s_barrier\n" \
    "s_sleep 20\n" \
    "Lr_late:\n" \
    "s_mov_b32 s63, 1048576\n" \
    "Lr_uw:\n" \
    "s_sub_u32 s63, s63, 1\n" \
    "s_cbranch_scc1 Lr_tmo\n" \
    "s_mov_b32 s67, 253775872\n" \
    "buffer_load_dword v144, %[fl], %[ws], s67 offen sc1\n" \
    "s_waitcnt vmcnt(0)\n" \
    "v_min_u32_dpp v144, v144, v144 row_ror:8 row_mask:0xf bank_mask:0xf\n" \
    "s_nop 1\n" \
    "v_min_u32_dpp v144, v144, v144 row_ror:4 row_mask:0xf bank_mask:0xf\n" \
    "s_nop 1\n" \
    "v_min_u32_dpp v144, v144, v144 row_ror:2 row_mask:0xf bank_mask:0xf\n" \
    "s_nop 1\n" \
    "v_min_u32_dpp v144, v144, v144 row_ror:1 row_mask:0xf bank_mask:0xf\n" \
    "s_nop 1\n" \
    "v_readlane_b32 s61, v144, 0\n" \
    "v_readlane_b32 s79, v144, 16\n" \
    "s_nop 3\n" \
    "s_min_u32 s61, s61, s79\n" \
    "s_cmp_lt_u32 s60, s61\n" \
    "s_cbranch_scc0 Lr_uw\n" \
    "s_mul_i32 s66, s60, 2621440\n" \
    "s_add_u32 s66, s66, %[ub]\n" \
    "s_lshr_b32 s79, s66, 8\n" \
    "s_andn2_b32 s79, 0x2000, s79\n" \
    "s_xor_b32 s79, s79, %[par]\n" \
    "s_add_u32 s66, s66, s79\n" \
    "buffer_load_dwordx4 a[0:3], %[l16], %[ws], s66 offen sc1\n" \
    "buffer_load_dwordx4 a[4:7], %[l16], %[ws], s66 offen offset:1024 sc1\n" \
    "buffer_load_dwordx4 a[8:11], %[l16], %[ws], s66 offen offset:2048 sc1\n" \
    "buffer_load_dwordx4 a[12:15], %[l16], %[ws], s66 offen offset:3072 sc1\n" \
    "s_add_u32 s66, s66, 4096\n" \
    "buffer_load_dwordx4 a[16:19], %[l16], %[ws], s66 offen sc1\n" \
    "buffer_load_dwordx4 a[20:23], %[l16], %[ws], s66 offen offset:1024 sc1\n" \
    "buffer_load_dwordx4 a[24:27], %[l16], %[ws], s66 offen offset:2048 sc1\n" \
    "buffer_load_dwordx4 a[28:31], %[l16], %[ws], s66 offen offset:3072 sc1\n" \
    "s_add_u32 s66, s66, 12288\n" \
    "buffer_load_dwordx4 a[32:35], %[l16], %[ws], s66 offen sc1\n" \
    "buffer_load_dwordx4 a[36:39], %[l16], %[ws], s66 offen offset:1024 sc1\n" \
    "buffer_load_dwordx4 a[40:43], %[l16], %[ws], s66 offen offset:2048 sc1\n" \
    "buffer_load_dwordx4 a[44:47], %[l16], %[ws], s66 offen offset:3072 sc1\n" \
    "s_add_u32 s66, s66, 4096\n" \
    "buffer_load_dwordx4 a[48:51], %[l16], %[ws], s66 offen sc1\n" \
    "buffer_load_dwordx4 a[52:55], %[l16], %[ws], s66 offen offset:1024 sc1\n" \
    "buffer_load_dwordx4 a[56:59], %[l16], %[ws], s66 offen offset:2048 sc1\n" \
    "buffer_load_dwordx4 a[60:63], %[l16], %[ws], s66 offen offset:3072 sc1\n" \
    "s_add_u32 s66, s66, 12288\n" \
    "buffer_load_dwordx4 a[64:67], %[l16], %[ws], s66 offen sc1\n" \
    "buffer_load_dwordx4 a[68:71], %[l16], %[ws], s66 offen offset:1024 sc1\n" \
    "buffer_load_dwordx4 a[72:75], %[l16], %[ws], s66 offen offset:2048 sc1\n" \
    "buffer_load_dwordx4 a[76:79], %[l16], %[ws], s66 offen offset:3072 sc1\n" \
    "s_add_u32 s66, s66, 4096\n" \
    "buffer_load_dwordx4 a[80:83], %[l16], %[ws], s66 offen sc1\n" \
    "buffer_load_dwordx4 a[84:87], %[l16], %[ws], s66 offen offset:1024 sc1\n" \
    "buffer_load_dwordx4 a[88:91], %[l16], %[ws], s66 offen offset:2048 sc1\n" \
    "buffer_load_dwordx4 a[92:95], %[l16], %[ws], s66 offen offset:3072 sc1\n" \
    "s_add_u32 s66, s66, 12288\n" \
    "buffer_load_dwordx4 a[96:99], %[l16], %[ws], s66 offen sc1\n" \
    "buffer_load_dwordx4 a[100:103], %[l16], %[ws], s66 offen offset:1024 sc1\n" \
    "buffer_load_dwordx4 a[104:107], %[l16], %[ws], s66 offen offset:2048 sc1\n" \
    "buffer_load_dwordx4 a[108:111], %[l16], %[ws], s66 offen offset:3072 sc1\n" \
    "s_add_u32 s66, s66, 4096\n" \
    "buffer_load_dwordx4 a[112:115], %[l16], %[ws], s66 offen sc1\n" \
    "buffer_load_dwordx4 a[116:119], %[l16], %[ws], s66 offen offset:1024 sc1\n" \
    "buffer_load_dwordx4 a[120:123], %[l16], %[ws], s66 offen offset:2048 sc1\n" \
    "buffer_load_dwordx4 a[124:127], %[l16], %[ws], s66 offen offset:3072 sc1\n" \
    "Lr_have:\n" \
    "s_barrier\n" \
    "s_add_u32 s72, s60, 2\n" \
    "s_min_u32 s72, s72, 63\n" \
    "s_cmp_lt_u32 s72, s61\n" \
    "s_cbranch_scc1 Lr_nf\n" \
    "s_mov_b32 s67, 253775872\n" \
    "buffer_load_dword v144, %[fl], %[ws], s67 offen sc1\n" \
    "Lr_nf:\n" \
    "s_lshl_b32 s62, s60, 8\n" \
    "s_and_b32 s62, s62, 0x2000\n" \
    "s_xor_b32 s62, s62, %[par]\n" \
    "s_lshl_b32 s79, s60, 16\n" \
    "s_add_u32 s62, s62, s79\n" \
    "s_add_u32 s72, s60, 1\n" \
    "s_lshl_b32 s65, s72, 8\n" \
    "s_and_b32 s65, s65, 0x2000\n" \
    "s_xor_b32 s65, s65, %[par]\n" \
    "s_lshl_b32 s79, s72, 16\n" \
    "s_add_u32 s65, s65, s79\n" \
    "s_add_u32 s73, s62, 32768\n" \
    "s_add_u32 s68, s62, 0\n" \
    "s_add_u32 s69, s62, 4096\n" \
    "s_add_u32 s70, s62, 16384\n" \
    "s_add_u32 s71, s62, 20480\n" \
    "s_lshl_b32 s64, s60, 12\n" \
    "s_and_b32 s64, s64, 0x2000\n" \
    "s_xor_b32 s64, s64, %[par]\n" \
    "s_lshl_b32 s79, s60, 20\n" \
    "s_add_u32 s64, s64, s79\n" \
    "s_add_u32 s64, s64, %[yb]\n" \
    "s_add_u32 s67, s64, 4096\n" \
    "v_mov_b64 v[128:129], 0\n" \
    "v_mov_b64 v[130:131], 0\n" \
    "s_mov_b32 s63, 1048576\n" \
    "s_waitcnt vmcnt(0)\n" \
    "v_min_u32_dpp v144, v144, v144 row_ror:8 row_mask:0xf bank_mask:0xf\n" \
    "s_nop 1\n" \
    "v_min_u32_dpp v144, v144, v144 row_ror:4 row_mask:0xf bank_mask:0xf\n" \
    "s_nop 1\n" \
    "v_min_u32_dpp v144, v144, v144 row_ror:2 row_mask:0xf bank_mask:0xf\n" \
    "s_nop 1\n" \
    "v_min_u32_dpp v144, v144, v144 row_ror:1 row_mask:0xf bank_mask:0xf\n" \
    "s_nop 1\n" \
    "v_readlane_b32 s61, v144, 0\n" \
    "v_readlane_b32 s79, v144, 16\n" \
    "s_nop 3\n" \
    "s_min_u32 s61, s61, s79\n" \
    "s_barrier\n" \
    "buffer_load_dwordx4 v[16:19], %[l16], %[ws], s64 offen sc1\n" \
    "buffer_load_dwordx4 v[20:23], %[l16], %[ws], s64 offen offset:1024 sc1\n" \
    "buffer_load_dwordx4 v[24:27], %[l16], %[ws], s64 offen offset:2048 sc1\n" \
    "buffer_load_dwordx4 v[28:31], %[l16], %[ws], s64 offen offset:3072 sc1\n" \
    "buffer_load_dwordx4 v[32:35], %[l16], %[ws], s67 offen sc1\n" \
    "buffer_load_dwordx4 v[36:39], %[l16], %[ws], s67 offen offset:1024 sc1\n" \
    "buffer_load_dwordx4 v[40:43], %[l16], %[ws], s67 offen offset:2048 sc1\n" \
    "buffer_load_dwordx4 v[44:47], %[l16], %[ws], s67 offen offset:3072 sc1\n" \
    "s_mov_b32 s79, %[d1]\n" \
    ".p2align 3\n" \
    "Lr_dbe:\n" \
    "s_sub_u32 s79, s79, 1\n" \
    "s_cbranch_scc0 Lr_dbe\n" \
    "buffer_load_dwordx4 v[64:67], %[mx], %[ws], s68 offen sc1\n" \
    "buffer_load_dwordx4 v[68:71], %[mx], %[ws], s68 offen offset:1024 sc1\n" \
    "buffer_load_dwordx4 v[72:75], %[mx], %[ws], s68 offen offset:2048 sc1\n" \
    "buffer_load_dwordx4 v[76:79], %[mx], %[ws], s68 offen offset:3072 sc1\n" \
    "buffer_load_dwordx4 v[80:83], %[mx], %[ws], s69 offen sc1\n" \
    "buffer_load_dwordx4 v[84:87], %[mx], %[ws], s69 offen offset:1024 sc1\n" \
    "buffer_load_dwordx4 v[88:91], %[mx], %[ws], s69 offen offset:2048 sc1\n" \
    "buffer_load_dwordx4 v[92:95], %[mx], %[ws], s69 offen offset:3072 sc1\n" \
    "buffer_load_dwordx4 v[96:99], %[mx], %[ws], s70 offen sc1\n" \
    "buffer_load_dwordx4 v[100:103], %[mx], %[ws], s70 offen offset:1024 sc1\n" \
    "buffer_load_dwordx4 v[104:107], %[mx], %[ws], s70 offen offset:2048 sc1\n" \
    "buffer_load_dwordx4 v[108:111], %[mx], %[ws], s70 offen offset:3072 sc1\n" \
    "buffer_load_dwordx4 v[112:115], %[mx], %[ws], s71 offen sc1\n" \
    "buffer_load_dwordx4 v[116:119], %[mx], %[ws], s71 offen offset:1024 sc1\n" \
    "buffer_load_dwordx4 v[120:123], %[mx], %[ws], s71 offen offset:2048 sc1\n" \
    "buffer_load_dwordx4 v[124:127], %[mx], %[ws], s71 offen offset:3072 sc1\n" \
    "buffer_load_dwordx2 v[48:49], %[mm], %[ws], s73 offen sc1\n" \
    "buffer_load_dwordx2 v[50:51], %[mm], %[ws], s73 offen offset:128 sc1\n" \
    "buffer_load_dwordx2 v[52:53], %[mm], %[ws], s73 offen offset:1024 sc1\n" \
    "buffer_load_dwordx2 v[54:55], %[mm], %[ws], s73 offen offset:1152 sc1\n" \
    "buffer_load_dwordx2 v[56:57], %[mm], %[ws], s73 offen offset:2048 sc1\n" \
    "buffer_load_dwordx2 v[58:59], %[mm], %[ws], s73 offen offset:2176 sc1\n" \
    "buffer_load_dwordx2 v[60:61], %[mm], %[ws], s73 offen offset:3072 sc1\n" \
    "buffer_load_dwordx2 v[62:63], %[mm], %[ws], s73 offen offset:3200 sc1\n" \
    "s_waitcnt vmcnt(23)\n" \
    "v_smfmac_f32_16x16x64_bf16 v[128:131], v[64:67], a[0:7], %[idx]\n" \
    "s_waitcnt vmcnt(22)\n" \
    "v_smfmac_f32_16x16x64_bf16 v[128:131], v[68:71], a[8:15], %[idx]\n" \
    "s_waitcnt vmcnt(21)\n" \
    "v_smfmac_f32_16x16x64_bf16 v[128:131], v[72:75], a[16:23], %[idx]\n" \
    "s_waitcnt vmcnt(20)\n" \
    "v_smfmac_f32_16x16x64_bf16 v[128:131], v[76:79], a[24:31], %[idx]\n" \
    "s_waitcnt vmcnt(19)\n" \
    "v_smfmac_f32_16x16x64_bf16 v[128:131], v[80:83], a[32:39], %[idx]\n" \
    "s_waitcnt vmcnt(18)\n" \
    "v_smfmac_f32_16x16x64_bf16 v[128:131], v[84:87], a[40:47], %[idx]\n" \
    "s_waitcnt vmcnt(17)\n" \
    "v_smfmac_f32_16x16x64_bf16 v[128:131], v[88:91], a[48:55], %[idx]\n" \
    "s_waitcnt vmcnt(16)\n" \
    "v_smfmac_f32_16x16x64_bf16 v[128:131], v[92:95], a[56:63], %[idx]\n" \
    "s_waitcnt vmcnt(15)\n" \
    "v_smfmac_f32_16x16x64_bf16 v[128:131], v[96:99], a[64:71], %[idx]\n" \
    "s_waitcnt vmcnt(14)\n" \
    "v_smfmac_f32_16x16x64_bf16 v[128:131], v[100:103], a[72:79], %[idx]\n" \
    "s_waitcnt vmcnt(13)\n" \
    "v_smfmac_f32_16x16x64_bf16 v[128:131], v[104:107], a[80:87], %[idx]\n" \
    "s_waitcnt vmcnt(12)\n" \
    "v_smfmac_f32_16x16x64_bf16 v[128:131], v[108:111], a[88:95], %[idx]\n" \
    "s_waitcnt vmcnt(11)\n" \
    "v_smfmac_f32_16x16x64_bf16 v[128:131], v[112:115], a[96:103], %[idx]\n" \
    "s_waitcnt vmcnt(10)\n" \
    "v_smfmac_f32_16x16x64_bf16 v[128:131], v[116:119], a[104:111], %[idx]\n" \
    "s_waitcnt vmcnt(9)\n" \
    "v_smfmac_f32_16x16x64_bf16 v[128:131], v[120:123], a[112:119], %[idx]\n" \
    "s_waitcnt vmcnt(8)\n" \
    "v_smfmac_f32_16x16x64_bf16 v[128:131], v[124:127], a[120:127], %[idx]\n" \
    "s_waitcnt vmcnt(6)\n" \
    "v_smfmac_f32_16x16x64_bf16 v[128:131], v[48:51], v[16:23], %[idx]\n" \
    "s_waitcnt vmcnt(4)\n" \
    "v_smfmac_f32_16x16x64_bf16 v[128:131], v[52:55], v[24:31], %[idx]\n" \
    "s_waitcnt vmcnt(2)\n" \
    "v_smfmac_f32_16x16x64_bf16 v[128:131], v[56:59], v[32:39], %[idx]\n" \
    "s_waitcnt vmcnt(0)\n" \
    "v_smfmac_f32_16x16x64_bf16 v[128:131], v[60:63], v[40:47], %[idx]\n" \
    "s_nop 7\n" \
    "v_add_f32 v128, v128, v129\n" \
    "v_add_f32 v130, v130, v131\n" \
    "v_cmp_u_f32 vcc, v128, v130\n" \
    "v_cvt_pk_bf16_f32 v134, v128, v130\n" \
    "v_cvt_pk_bf16_f32 v135, v128, v130\n" \
    "s_nop 1\n" \
    "v_permlane16_swap_b32 v134, v135\n" \
    "s_cbranch_vccnz Lr_y\n" \
    "Lr_pub:\n" \
    "buffer_store_dwordx2 v[134:135], %[mo], %[ws], s65 offen sc1\n" \
    "s_add_u32 s72, s60, 5\n" \
    "s_lshl_b32 s67, s72, 8\n" \
    "s_and_b32 s67, s67, 0x2000\n" \
    "s_xor_b32 s67, s67, %[par]\n" \
    "s_lshl_b32 s79, s72, 16\n" \
    "s_add_u32 s67, s67, s79\n" \
    "buffer_store_dwordx4 v[146:149], %[po], %[ws], s67 offen sc1\n" \
    "s_add_u32 s60, s60, 2\n" \
    "s_cmp_lt_u32 s60, 63\n" \
    "s_cbranch_scc1 Lr_step\n" \
    "s_branch Lr_end\n" \
    "Lr_y:\n" \
    "s_sub_u32 s63, s63, 1\n" \
    "s_cbranch_scc1 Lr_tmo\n" \
    "buffer_load_dwordx4 v[64:67], %[mx], %[ws], s68 offen sc1\n" \
    "buffer_load_dwordx4 v[68:71], %[mx], %[ws], s68 offen offset:1024 sc1\n" \
    "buffer_load_dwordx4 v[72:75], %[mx], %[ws], s68 offen offset:2048 sc1\n" \
    "buffer_load_dwordx4 v[76:79], %[mx], %[ws], s68 offen offset:3072 sc1\n" \
    "buffer_load_dwordx4 v[80:83], %[mx], %[ws], s69 offen sc1\n" \
    "buffer_load_dwordx4 v[84:87], %[mx], %[ws], s69 offen offset:1024 sc1\n" \
    "buffer_load_dwordx4 v[88:91], %[mx], %[ws], s69 offen offset:2048 sc1\n" \
    "buffer_load_dwordx4 v[92:95], %[mx], %[ws], s69 offen offset:3072 sc1\n" \
    "buffer_load_dwordx4 v[96:99], %[mx], %[ws], s70 offen sc1\n" \
    "buffer_load_dwordx4 v[100:103], %[mx], %[ws], s70 offen offset:1024 sc1\n" \
    "buffer_load_dwordx4 v[104:107], %[mx], %[ws], s70 offen offset:2048 sc1\n" \
    "buffer_load_dwordx4 v[108:111], %[mx], %[ws], s70 offen offset:3072 sc1\n" \
    "buffer_load_dwordx4 v[112:115], %[mx], %[ws], s71 offen sc1\n" \
    "buffer_load_dwordx4 v[116:119], %[mx], %[ws], s71 offen offset:1024 sc1\n" \
    "buffer_load_dwordx4 v[120:123], %[mx], %[ws], s71 offen offset:2048 sc1\n" \
    "buffer_load_dwordx4 v[124:127], %[mx], %[ws], s71 offen offset:3072 sc1\n" \
    "buffer_load_dwordx2 v[48:49], %[mm], %[ws], s73 offen sc1\n" \
    "buffer_load_dwordx2 v[50:51], %[mm], %[ws], s73 offen offset:128 sc1\n" \
    "buffer_load_dwordx2 v[52:53], %[mm], %[ws], s73 offen offset:1024 sc1\n" \
    "buffer_load_dwordx2 v[54:55], %[mm], %[ws], s73 offen offset:1152 sc1\n" \
    "buffer_load_dwordx2 v[56:57], %[mm], %[ws], s73 offen offset:2048 sc1\n" \
    "buffer_load_dwordx2 v[58:59], %[mm], %[ws], s73 offen offset:2176 sc1\n" \
    "buffer_load_dwordx2 v[60:61], %[mm], %[ws], s73 offen offset:3072 sc1\n" \
    "buffer_load_dwordx2 v[62:63], %[mm], %[ws], s73 offen offset:3200 sc1\n" \
    "Lr_y2:\n" \
    "v_max_u32 v132, v16, v17\n" \
    "v_max3_u32 v132, v132, v18, v19\n" \
    "v_max3_u32 v132, v132, v20, v21\n" \
    "v_max3_u32 v132, v132, v22, v23\n" \
    "v_max3_u32 v132, v132, v24, v25\n" \
    "v_max3_u32 v132, v132, v26, v27\n" \
    "v_max3_u32 v132, v132, v28, v29\n" \
    "v_max3_u32 v132, v132, v30, v31\n" \
    "v_max3_u32 v132, v132, v32, v33\n" \
    "v_max3_u32 v132, v132, v34, v35\n" \
    "v_max3_u32 v132, v132, v36, v37\n" \
    "v_max3_u32 v132, v132, v38, v39\n" \
    "v_max3_u32 v132, v132, v40, v41\n" \
    "v_max3_u32 v132, v132, v42, v43\n" \
    "v_max3_u32 v132, v132, v44, v45\n" \
    "v_max3_u32 v132, v132, v46, v47\n" \
    "v_cmp_eq_u32 vcc, -1, v132\n" \
    "s_cbranch_vccz Lr_q\n" \
    "s_sub_u32 s63, s63, 1\n" \
    "s_cbranch_scc1 Lr_tmo\n" \
    "buffer_load_dwordx4 v[16:19], %[l16], %[ws], s64 offen sc1\n" \
    "buffer_load_dwordx4 v[20:23], %[l16], %[ws], s64 offen offset:1024 sc1\n" \
    "buffer_load_dwordx4 v[24:27], %[l16], %[ws], s64 offen offset:2048 sc1\n" \
    "buffer_load_dwordx4 v[28:31], %[l16], %[ws], s64 offen offset:3072 sc1\n" \
    "buffer_load_dwordx4 v[32:35], %[l16], %[ws], s67 offen sc1\n" \
    "buffer_load_dwordx4 v[36:39], %[l16], %[ws], s67 offen offset:1024 sc1\n" \
    "buffer_load_dwordx4 v[40:43], %[l16], %[ws], s67 offen offset:2048 sc1\n" \
    "buffer_load_dwordx4 v[44:47], %[l16], %[ws], s67 offen offset:3072 sc1\n" \
    "s_waitcnt vmcnt(0)\n" \
    "s_branch Lr_y2\n" \
    "Lr_q:\n" \
    "v_mov_b64 v[128:129], 0\n" \
    "v_mov_b64 v[130:131], 0\n" \
    "s_waitcnt vmcnt(23)\n" \
    "s_nop 0\n" \
    "v_smfmac_f32_16x16x64_bf16 v[128:131], v[64:67], a[0:7], %[idx]\n" \
    "s_waitcnt vmcnt(22)\n" \
    "v_smfmac_f32_16x16x64_bf16 v[128:131], v[68:71], a[8:15], %[idx]\n" \
    "s_waitcnt vmcnt(21)\n" \
    "v_smfmac_f32_16x16x64_bf16 v[128:131], v[72:75], a[16:23], %[idx]\n" \
    "s_waitcnt vmcnt(20)\n" \
    "v_smfmac_f32_16x16x64_bf16 v[128:131], v[76:79], a[24:31], %[idx]\n" \
    "s_waitcnt vmcnt(19)\n" \
    "v_smfmac_f32_16x16x64_bf16 v[128:131], v[80:83], a[32:39], %[idx]\n" \
    "s_waitcnt vmcnt(18)\n" \
    "v_smfmac_f32_16x16x64_bf16 v[128:131], v[84:87], a[40:47], %[idx]\n" \
    "s_waitcnt vmcnt(17)\n" \
    "v_smfmac_f32_16x16x64_bf16 v[128:131], v[88:91], a[48:55], %[idx]\n" \
    "s_waitcnt vmcnt(16)\n" \
    "v_smfmac_f32_16x16x64_bf16 v[128:131], v[92:95], a[56:63], %[idx]\n" \
    "s_waitcnt vmcnt(15)\n" \
    "v_smfmac_f32_16x16x64_bf16 v[128:131], v[96:99], a[64:71], %[idx]\n" \
    "s_waitcnt vmcnt(14)\n" \
    "v_smfmac_f32_16x16x64_bf16 v[128:131], v[100:103], a[72:79], %[idx]\n" \
    "s_waitcnt vmcnt(13)\n" \
    "v_smfmac_f32_16x16x64_bf16 v[128:131], v[104:107], a[80:87], %[idx]\n" \
    "s_waitcnt vmcnt(12)\n" \
    "v_smfmac_f32_16x16x64_bf16 v[128:131], v[108:111], a[88:95], %[idx]\n" \
    "s_waitcnt vmcnt(11)\n" \
    "v_smfmac_f32_16x16x64_bf16 v[128:131], v[112:115], a[96:103], %[idx]\n" \
    "s_waitcnt vmcnt(10)\n" \
    "v_smfmac_f32_16x16x64_bf16 v[128:131], v[116:119], a[104:111], %[idx]\n" \
    "s_waitcnt vmcnt(9)\n" \
    "v_smfmac_f32_16x16x64_bf16 v[128:131], v[120:123], a[112:119], %[idx]\n" \
    "s_waitcnt vmcnt(8)\n" \
    "v_smfmac_f32_16x16x64_bf16 v[128:131], v[124:127], a[120:127], %[idx]\n" \
    "s_waitcnt vmcnt(6)\n" \
    "v_smfmac_f32_16x16x64_bf16 v[128:131], v[48:51], v[16:23], %[idx]\n" \
    "s_waitcnt vmcnt(4)\n" \
    "v_smfmac_f32_16x16x64_bf16 v[128:131], v[52:55], v[24:31], %[idx]\n" \
    "s_waitcnt vmcnt(2)\n" \
    "v_smfmac_f32_16x16x64_bf16 v[128:131], v[56:59], v[32:39], %[idx]\n" \
    "s_waitcnt vmcnt(0)\n" \
    "v_smfmac_f32_16x16x64_bf16 v[128:131], v[60:63], v[40:47], %[idx]\n" \
    "s_nop 7\n" \
    "v_add_f32 v128, v128, v129\n" \
    "v_add_f32 v130, v130, v131\n" \
    "v_cmp_u_f32 vcc, v128, v130\n" \
    "v_cvt_pk_bf16_f32 v134, v128, v130\n" \
    "v_min_u32 v135, -2, v134\n" \
    "v_min_u32 v134, -2, v134\n" \
    "s_nop 1\n" \
    "v_permlane16_swap_b32 v134, v135\n" \
    "s_cbranch_vccz Lr_pub\n" \
    "v_max_u32 v132, v48, v49\n" \
    "v_max3_u32 v132, v132, v50, v51\n" \
    "v_max3_u32 v132, v132, v52, v53\n" \
    "v_max3_u32 v132, v132, v54, v55\n" \
    "v_max3_u32 v132, v132, v56, v57\n" \
    "v_max3_u32 v132, v132, v58, v59\n" \
    "v_max3_u32 v132, v132, v60, v61\n" \
    "v_max3_u32 v132, v132, v62, v63\n" \
    "v_max3_u32 v132, v132, v64, v65\n" \
    "v_max3_u32 v132, v132, v66, v67\n" \
    "v_max3_u32 v132, v132, v68, v69\n" \
    "v_max3_u32 v132, v132, v70, v71\n" \
    "v_max3_u32 v132, v132, v72, v73\n" \
    "v_max3_u32 v132, v132, v74, v75\n" \
    "v_max3_u32 v132, v132, v76, v77\n" \
    "v_max3_u32 v132, v132, v78, v79\n" \
    "v_max3_u32 v132, v132, v80, v81\n" \
    "v_max3_u32 v132, v132, v82, v83\n" \
    "v_max3_u32 v132, v132, v84, v85\n" \
    "v_max3_u32 v132, v132, v86, v87\n" \
    "v_max3_u32 v132, v132, v88, v89\n" \
    "v_max3_u32 v132, v132, v90, v91\n" \
    "v_max3_u32 v132, v132, v92, v93\n" \
    "v_max3_u32 v132, v132, v94, v95\n" \
    "v_max3_u32 v132, v132, v96, v97\n" \
    "v_max3_u32 v132, v132, v98, v99\n" \
    "v_max3_u32 v132, v132, v100, v101\n" \
    "v_max3_u32 v132, v132, v102, v103\n" \
    "v_max3_u32 v132, v132, v104, v105\n" \
    "v_max3_u32 v132, v132, v106, v107\n" \
    "v_max3_u32 v132, v132, v108, v109\n" \
    "v_max3_u32 v132, v132, v110, v111\n" \
    "v_max3_u32 v132, v132, v112, v113\n" \
    "v_max3_u32 v132, v132, v114, v115\n" \
    "v_max3_u32 v132, v132, v116, v117\n" \
    "v_max3_u32 v132, v132, v118, v119\n" \
    "v_max3_u32 v132, v132, v120, v121\n" \
    "v_max3_u32 v132, v132, v122, v123\n" \
    "v_max3_u32 v132, v132, v124, v125\n" \
    "v_max3_u32 v132, v132, v126, v127\n" \
    "v_cmp_eq_u32 vcc, -1, v132\n" \
    "s_cbranch_vccnz Lr_y\n" \
    "s_branch Lr_pub\n" \
    "Lr_tmo:\n" \
    "s_add_u32 s78, s60, 1280\n" \
    "v_mov_b32 v134, s78\n" \
    "v_mov_b32 v135, 0\n" \
    "s_mov_b32 s78, 253757696\n" \
    "s_nop 4\n" \
    "buffer_store_dword v134, v135, %[ws], s78 offen sc1\n" \
    "s_endpgm\n" \
    "Lr_end:\n"
#define ASM_PROJ \
    "s_mul_i32 s70, %[wave], 10240\n" \
    "s_add_u32 s63, s70, 250609664\n" \
    "s_add_u32 m0, s70, 0\n" \
    "s_nop 0\n" \
    "buffer_load_dwordx4 %[l16], %[ws], s63 offen lds\n" \
    "buffer_load_dwordx4 %[l16], %[ws], s63 offen offset:1024 lds\n" \
    "buffer_load_dwordx4 %[l16], %[ws], s63 offen offset:2048 lds\n" \
    "buffer_load_dwordx4 %[l16], %[ws], s63 offen offset:3072 lds\n" \
    "s_add_u32 m0, s70, 4096\n" \
    "s_add_u32 s63, s63, 4096\n" \
    "buffer_load_dwordx4 %[l16], %[ws], s63 offen lds\n" \
    "buffer_load_dwordx4 %[l16], %[ws], s63 offen offset:1024 lds\n" \
    "buffer_load_dwordx4 %[l16], %[ws], s63 offen offset:2048 lds\n" \
    "buffer_load_dwordx4 %[l16], %[ws], s63 offen offset:3072 lds\n" \
    "s_add_u32 m0, s70, 8192\n" \
    "s_add_u32 s63, s63, 4096\n" \
    "buffer_load_dwordx4 %[l16], %[ws], s63 offen lds\n" \
    "buffer_load_dwordx4 %[l16], %[ws], s63 offen offset:1024 lds\n" \
    "s_mul_i32 s71, %[wave], 3072\n" \
    "v_add_u32 v112, 65536, %[tr0]\n" \
    "v_add_u32 v113, 65536, %[tr1]\n" \
    "v_add_u32 v114, 65536, %[tr2]\n" \
    "v_add_u32 v115, 98304, %[tr0]\n" \
    "v_add_u32 v116, 98304, %[tr1]\n" \
    "v_add_u32 v117, 98304, %[tr2]\n" \
    "s_mov_b32 s61, 0\n" \
    "s_add_u32 m0, s71, 40960\n" \
    "s_nop 0\n" \
    "buffer_load_dwordx4 %[src0], %[w0], s61 offen lds\n" \
    "s_add_u32 m0, s71, 41984\n" \
    "s_nop 0\n" \
    "buffer_load_dwordx4 %[src1], %[w0], s61 offen lds\n" \
    "s_add_u32 m0, s71, 43008\n" \
    "s_nop 0\n" \
    "buffer_load_dwordx4 %[src2], %[w0], s61 offen lds\n" \
    "s_mov_b32 s61, 393216\n" \
    "s_add_u32 m0, s71, 53248\n" \
    "s_nop 0\n" \
    "buffer_load_dwordx4 %[src0], %[w0], s61 offen lds\n" \
    "s_add_u32 m0, s71, 54272\n" \
    "s_nop 0\n" \
    "buffer_load_dwordx4 %[src1], %[w0], s61 offen lds\n" \
    "s_add_u32 m0, s71, 55296\n" \
    "s_nop 0\n" \
    "buffer_load_dwordx4 %[src2], %[w0], s61 offen lds\n" \
    "s_mov_b32 s61, 786432\n" \
    "s_add_u32 m0, s71, 65536\n" \
    "s_nop 0\n" \
    "buffer_load_dwordx4 %[src0], %[w0], s61 offen lds\n" \
    "s_add_u32 m0, s71, 66560\n" \
    "s_nop 0\n" \
    "buffer_load_dwordx4 %[src1], %[w0], s61 offen lds\n" \
    "s_add_u32 m0, s71, 67584\n" \
    "s_nop 0\n" \
    "buffer_load_dwordx4 %[src2], %[w0], s61 offen lds\n" \
    "s_mov_b32 s61, 1179648\n" \
    "s_add_u32 m0, s71, 77824\n" \
    "s_nop 0\n" \
    "buffer_load_dwordx4 %[src0], %[w0], s61 offen lds\n" \
    "s_add_u32 m0, s71, 78848\n" \
    "s_nop 0\n" \
    "buffer_load_dwordx4 %[src1], %[w0], s61 offen lds\n" \
    "s_add_u32 m0, s71, 79872\n" \
    "s_nop 0\n" \
    "buffer_load_dwordx4 %[src2], %[w0], s61 offen lds\n" \
    "s_mov_b32 s61, 1572864\n" \
    "s_add_u32 m0, s71, 90112\n" \
    "s_nop 0\n" \
    "buffer_load_dwordx4 %[src0], %[w0], s61 offen lds\n" \
    "s_add_u32 m0, s71, 91136\n" \
    "s_nop 0\n" \
    "buffer_load_dwordx4 %[src1], %[w0], s61 offen lds\n" \
    "s_add_u32 m0, s71, 92160\n" \
    "s_nop 0\n" \
    "buffer_load_dwordx4 %[src2], %[w0], s61 offen lds\n" \
    "s_mov_b32 s61, 1966080\n" \
    "s_add_u32 m0, s71, 102400\n" \
    "s_nop 0\n" \
    "buffer_load_dwordx4 %[src0], %[w0], s61 offen lds\n" \
    "s_add_u32 m0, s71, 103424\n" \
    "s_nop 0\n" \
    "buffer_load_dwordx4 %[src1], %[w0], s61 offen lds\n" \
    "s_add_u32 m0, s71, 104448\n" \
    "s_nop 0\n" \
    "buffer_load_dwordx4 %[src2], %[w0], s61 offen lds\n" \
    "s_mov_b32 s61, 2359296\n" \
    "s_add_u32 m0, s71, 114688\n" \
    "s_nop 0\n" \
    "buffer_load_dwordx4 %[src0], %[w0], s61 offen lds\n" \
    "s_add_u32 m0, s71, 115712\n" \
    "s_nop 0\n" \
    "buffer_load_dwordx4 %[src1], %[w0], s61 offen lds\n" \
    "s_add_u32 m0, s71, 116736\n" \
    "s_nop 0\n" \
    "buffer_load_dwordx4 %[src2], %[w0], s61 offen lds\n" \
    "s_mov_b32 s61, 2752512\n" \
    "s_add_u32 m0, s71, 126976\n" \
    "s_nop 0\n" \
    "buffer_load_dwordx4 %[src0], %[w0], s61 offen lds\n" \
    "s_add_u32 m0, s71, 128000\n" \
    "s_nop 0\n" \
    "buffer_load_dwordx4 %[src1], %[w0], s61 offen lds\n" \
    "s_add_u32 m0, s71, 129024\n" \
    "s_nop 0\n" \
    "buffer_load_dwordx4 %[src2], %[w0], s61 offen lds\n" \
    "s_mov_b32 s61, 3145728\n" \
    "s_add_u32 m0, s71, 139264\n" \
    "s_nop 0\n" \
    "buffer_load_dwordx4 %[src0], %[w0], s61 offen lds\n" \
    "s_add_u32 m0, s71, 140288\n" \
    "s_nop 0\n" \
    "buffer_load_dwordx4 %[src1], %[w0], s61 offen lds\n" \
    "s_add_u32 m0, s71, 141312\n" \
    "s_nop 0\n" \
    "buffer_load_dwordx4 %[src2], %[w0], s61 offen lds\n" \
    "s_mov_b32 s61, 3538944\n" \
    "s_add_u32 m0, s71, 151552\n" \
    "s_nop 0\n" \
    "buffer_load_dwordx4 %[src0], %[w0], s61 offen lds\n" \
    "s_add_u32 m0, s71, 152576\n" \
    "s_nop 0\n" \
    "buffer_load_dwordx4 %[src1], %[w0], s61 offen lds\n" \
    "s_add_u32 m0, s71, 153600\n" \
    "s_nop 0\n" \
    "buffer_load_dwordx4 %[src2], %[w0], s61 offen lds\n" \
    "s_waitcnt vmcnt(24)\n" \
    "s_barrier\n" \
    "ds_read_b64_tr_b16 a[0:1], %[tr0] offset:40960\n" \
    "ds_read_b64_tr_b16 a[2:3], %[tr0] offset:42496\n" \
    "ds_read_b64_tr_b16 a[80:81], %[tr1] offset:40960\n" \
    "ds_read_b64_tr_b16 a[82:83], %[tr1] offset:42496\n" \
    "ds_read_b64_tr_b16 a[160:161], %[tr2] offset:40960\n" \
    "ds_read_b64_tr_b16 a[162:163], %[tr2] offset:42496\n" \
    "ds_read_b64_tr_b16 a[4:5], %[tr0] offset:53248\n" \
    "ds_read_b64_tr_b16 a[6:7], %[tr0] offset:54784\n" \
    "ds_read_b64_tr_b16 a[84:85], %[tr1] offset:53248\n" \
    "ds_read_b64_tr_b16 a[86:87], %[tr1] offset:54784\n" \
    "ds_read_b64_tr_b16 a[164:165], %[tr2] offset:53248\n" \
    "ds_read_b64_tr_b16 a[166:167], %[tr2] offset:54784\n" \
    "s_waitcnt lgkmcnt(0)\n" \
    "s_waitcnt vmcnt(18)\n" \
    "s_barrier\n" \
    "s_mov_b32 s61, 3932160\n" \
    "s_add_u32 m0, s71, 40960\n" \
    "s_nop 0\n" \
    "buffer_load_dwordx4 %[src0], %[w0], s61 offen lds\n" \
    "s_add_u32 m0, s71, 41984\n" \
    "s_nop 0\n" \
    "buffer_load_dwordx4 %[src1], %[w0], s61 offen lds\n" \
    "s_add_u32 m0, s71, 43008\n" \
    "s_nop 0\n" \
    "buffer_load_dwordx4 %[src2], %[w0], s61 offen lds\n" \
    "s_mov_b32 s61, 4325376\n" \
    "s_add_u32 m0, s71, 53248\n" \
    "s_nop 0\n" \
    "buffer_load_dwordx4 %[src0], %[w0], s61 offen lds\n" \
    "s_add_u32 m0, s71, 54272\n" \
    "s_nop 0\n" \
    "buffer_load_dwordx4 %[src1], %[w0], s61 offen lds\n" \
    "s_add_u32 m0, s71, 55296\n" \
    "s_nop 0\n" \
    "buffer_load_dwordx4 %[src2], %[w0], s61 offen lds\n" \
    "ds_read_b64_tr_b16 a[8:9], v112\n" \
    "ds_read_b64_tr_b16 a[10:11], v112 offset:1536\n" \
    "ds_read_b64_tr_b16 a[88:89], v113\n" \
    "ds_read_b64_tr_b16 a[90:91], v113 offset:1536\n" \
    "ds_read_b64_tr_b16 a[168:169], v114\n" \
    "ds_read_b64_tr_b16 a[170:171], v114 offset:1536\n" \
    "ds_read_b64_tr_b16 a[12:13], v112 offset:12288\n" \
    "ds_read_b64_tr_b16 a[14:15], v112 offset:13824\n" \
    "ds_read_b64_tr_b16 a[92:93], v113 offset:12288\n" \
    "ds_read_b64_tr_b16 a[94:95], v113 offset:13824\n" \
    "ds_read_b64_tr_b16 a[172:173], v114 offset:12288\n" \
    "ds_read_b64_tr_b16 a[174:175], v114 offset:13824\n" \
    "s_waitcnt lgkmcnt(0)\n" \
    "s_waitcnt vmcnt(18)\n" \
    "s_barrier\n" \
    "s_mov_b32 s61, 4718592\n" \
    "s_add_u32 m0, s71, 65536\n" \
    "s_nop 0\n" \
    "buffer_load_dwordx4 %[src0], %[w0], s61 offen lds\n" \
    "s_add_u32 m0, s71, 66560\n" \
    "s_nop 0\n" \
    "buffer_load_dwordx4 %[src1], %[w0], s61 offen lds\n" \
    "s_add_u32 m0, s71, 67584\n" \
    "s_nop 0\n" \
    "buffer_load_dwordx4 %[src2], %[w0], s61 offen lds\n" \
    "s_mov_b32 s61, 5111808\n" \
    "s_add_u32 m0, s71, 77824\n" \
    "s_nop 0\n" \
    "buffer_load_dwordx4 %[src0], %[w0], s61 offen lds\n" \
    "s_add_u32 m0, s71, 78848\n" \
    "s_nop 0\n" \
    "buffer_load_dwordx4 %[src1], %[w0], s61 offen lds\n" \
    "s_add_u32 m0, s71, 79872\n" \
    "s_nop 0\n" \
    "buffer_load_dwordx4 %[src2], %[w0], s61 offen lds\n" \
    "ds_read_b64_tr_b16 a[16:17], v112 offset:24576\n" \
    "ds_read_b64_tr_b16 a[18:19], v112 offset:26112\n" \
    "ds_read_b64_tr_b16 a[96:97], v113 offset:24576\n" \
    "ds_read_b64_tr_b16 a[98:99], v113 offset:26112\n" \
    "ds_read_b64_tr_b16 a[176:177], v114 offset:24576\n" \
    "ds_read_b64_tr_b16 a[178:179], v114 offset:26112\n" \
    "ds_read_b64_tr_b16 a[20:21], v112 offset:36864\n" \
    "ds_read_b64_tr_b16 a[22:23], v112 offset:38400\n" \
    "ds_read_b64_tr_b16 a[100:101], v113 offset:36864\n" \
    "ds_read_b64_tr_b16 a[102:103], v113 offset:38400\n" \
    "ds_read_b64_tr_b16 a[180:181], v114 offset:36864\n" \
    "ds_read_b64_tr_b16 a[182:183], v114 offset:38400\n" \
    "s_waitcnt lgkmcnt(0)\n" \
    "s_waitcnt vmcnt(18)\n" \
    "s_barrier\n" \
    "s_mov_b32 s61, 5505024\n" \
    "s_add_u32 m0, s71, 90112\n" \
    "s_nop 0\n" \
    "buffer_load_dwordx4 %[src0], %[w0], s61 offen lds\n" \
    "s_add_u32 m0, s71, 91136\n" \
    "s_nop 0\n" \
    "buffer_load_dwordx4 %[src1], %[w0], s61 offen lds\n" \
    "s_add_u32 m0, s71, 92160\n" \
    "s_nop 0\n" \
    "buffer_load_dwordx4 %[src2], %[w0], s61 offen lds\n" \
    "s_mov_b32 s61, 5898240\n" \
    "s_add_u32 m0, s71, 102400\n" \
    "s_nop 0\n" \
    "buffer_load_dwordx4 %[src0], %[w0], s61 offen lds\n" \
    "s_add_u32 m0, s71, 103424\n" \
    "s_nop 0\n" \
    "buffer_load_dwordx4 %[src1], %[w0], s61 offen lds\n" \
    "s_add_u32 m0, s71, 104448\n" \
    "s_nop 0\n" \
    "buffer_load_dwordx4 %[src2], %[w0], s61 offen lds\n" \
    "ds_read_b64_tr_b16 a[24:25], v112 offset:49152\n" \
    "ds_read_b64_tr_b16 a[26:27], v112 offset:50688\n" \
    "ds_read_b64_tr_b16 a[104:105], v113 offset:49152\n" \
    "ds_read_b64_tr_b16 a[106:107], v113 offset:50688\n" \
    "ds_read_b64_tr_b16 a[184:185], v114 offset:49152\n" \
    "ds_read_b64_tr_b16 a[186:187], v114 offset:50688\n" \
    "ds_read_b64_tr_b16 a[28:29], v112 offset:61440\n" \
    "ds_read_b64_tr_b16 a[30:31], v112 offset:62976\n" \
    "ds_read_b64_tr_b16 a[108:109], v113 offset:61440\n" \
    "ds_read_b64_tr_b16 a[110:111], v113 offset:62976\n" \
    "ds_read_b64_tr_b16 a[188:189], v114 offset:61440\n" \
    "ds_read_b64_tr_b16 a[190:191], v114 offset:62976\n" \
    "s_waitcnt lgkmcnt(0)\n" \
    "s_waitcnt vmcnt(18)\n" \
    "s_barrier\n" \
    "s_mov_b32 s61, 6291456\n" \
    "s_add_u32 m0, s71, 114688\n" \
    "s_nop 0\n" \
    "buffer_load_dwordx4 %[src0], %[w0], s61 offen lds\n" \
    "s_add_u32 m0, s71, 115712\n" \
    "s_nop 0\n" \
    "buffer_load_dwordx4 %[src1], %[w0], s61 offen lds\n" \
    "s_add_u32 m0, s71, 116736\n" \
    "s_nop 0\n" \
    "buffer_load_dwordx4 %[src2], %[w0], s61 offen lds\n" \
    "s_mov_b32 s61, 6684672\n" \
    "s_add_u32 m0, s71, 126976\n" \
    "s_nop 0\n" \
    "buffer_load_dwordx4 %[src0], %[w0], s61 offen lds\n" \
    "s_add_u32 m0, s71, 128000\n" \
    "s_nop 0\n" \
    "buffer_load_dwordx4 %[src1], %[w0], s61 offen lds\n" \
    "s_add_u32 m0, s71, 129024\n" \
    "s_nop 0\n" \
    "buffer_load_dwordx4 %[src2], %[w0], s61 offen lds\n" \
    "ds_read_b64_tr_b16 a[32:33], v115 offset:40960\n" \
    "ds_read_b64_tr_b16 a[34:35], v115 offset:42496\n" \
    "ds_read_b64_tr_b16 a[112:113], v116 offset:40960\n" \
    "ds_read_b64_tr_b16 a[114:115], v116 offset:42496\n" \
    "ds_read_b64_tr_b16 a[192:193], v117 offset:40960\n" \
    "ds_read_b64_tr_b16 a[194:195], v117 offset:42496\n" \
    "ds_read_b64_tr_b16 a[36:37], v115 offset:53248\n" \
    "ds_read_b64_tr_b16 a[38:39], v115 offset:54784\n" \
    "ds_read_b64_tr_b16 a[116:117], v116 offset:53248\n" \
    "ds_read_b64_tr_b16 a[118:119], v116 offset:54784\n" \
    "ds_read_b64_tr_b16 a[196:197], v117 offset:53248\n" \
    "ds_read_b64_tr_b16 a[198:199], v117 offset:54784\n" \
    "s_waitcnt lgkmcnt(0)\n" \
    "s_waitcnt vmcnt(18)\n" \
    "s_barrier\n" \
    "s_mov_b32 s61, 7077888\n" \
    "s_add_u32 m0, s71, 139264\n" \
    "s_nop 0\n" \
    "buffer_load_dwordx4 %[src0], %[w0], s61 offen lds\n" \
    "s_add_u32 m0, s71, 140288\n" \
    "s_nop 0\n" \
    "buffer_load_dwordx4 %[src1], %[w0], s61 offen lds\n" \
    "s_add_u32 m0, s71, 141312\n" \
    "s_nop 0\n" \
    "buffer_load_dwordx4 %[src2], %[w0], s61 offen lds\n" \
    "s_mov_b32 s61, 7471104\n" \
    "s_add_u32 m0, s71, 151552\n" \
    "s_nop 0\n" \
    "buffer_load_dwordx4 %[src0], %[w0], s61 offen lds\n" \
    "s_add_u32 m0, s71, 152576\n" \
    "s_nop 0\n" \
    "buffer_load_dwordx4 %[src1], %[w0], s61 offen lds\n" \
    "s_add_u32 m0, s71, 153600\n" \
    "s_nop 0\n" \
    "buffer_load_dwordx4 %[src2], %[w0], s61 offen lds\n" \
    "ds_read_b64_tr_b16 a[40:41], %[tr0] offset:40960\n" \
    "ds_read_b64_tr_b16 a[42:43], %[tr0] offset:42496\n" \
    "ds_read_b64_tr_b16 a[120:121], %[tr1] offset:40960\n" \
    "ds_read_b64_tr_b16 a[122:123], %[tr1] offset:42496\n" \
    "ds_read_b64_tr_b16 a[200:201], %[tr2] offset:40960\n" \
    "ds_read_b64_tr_b16 a[202:203], %[tr2] offset:42496\n" \
    "ds_read_b64_tr_b16 a[44:45], %[tr0] offset:53248\n" \
    "ds_read_b64_tr_b16 a[46:47], %[tr0] offset:54784\n" \
    "ds_read_b64_tr_b16 a[124:125], %[tr1] offset:53248\n" \
    "ds_read_b64_tr_b16 a[126:127], %[tr1] offset:54784\n" \
    "ds_read_b64_tr_b16 a[204:205], %[tr2] offset:53248\n" \
    "ds_read_b64_tr_b16 a[206:207], %[tr2] offset:54784\n" \
    "s_waitcnt lgkmcnt(0)\n" \
    "s_waitcnt vmcnt(18)\n" \
    "s_barrier\n" \
    "ds_read_b64_tr_b16 a[48:49], v112\n" \
    "ds_read_b64_tr_b16 a[50:51], v112 offset:1536\n" \
    "ds_read_b64_tr_b16 a[128:129], v113\n" \
    "ds_read_b64_tr_b16 a[130:131], v113 offset:1536\n" \
    "ds_read_b64_tr_b16 a[208:209], v114\n" \
    "ds_read_b64_tr_b16 a[210:211], v114 offset:1536\n" \
    "ds_read_b64_tr_b16 a[52:53], v112 offset:12288\n" \
    "ds_read_b64_tr_b16 a[54:55], v112 offset:13824\n" \
    "ds_read_b64_tr_b16 a[132:133], v113 offset:12288\n" \
    "ds_read_b64_tr_b16 a[134:135], v113 offset:13824\n" \
    "ds_read_b64_tr_b16 a[212:213], v114 offset:12288\n" \
    "ds_read_b64_tr_b16 a[214:215], v114 offset:13824\n" \
    "s_waitcnt lgkmcnt(0)\n" \
    "s_waitcnt vmcnt(12)\n" \
    "s_barrier\n" \
    "ds_read_b64_tr_b16 a[56:57], v112 offset:24576\n" \
    "ds_read_b64_tr_b16 a[58:59], v112 offset:26112\n" \
    "ds_read_b64_tr_b16 a[136:137], v113 offset:24576\n" \
    "ds_read_b64_tr_b16 a[138:139], v113 offset:26112\n" \
    "ds_read_b64_tr_b16 a[216:217], v114 offset:24576\n" \
    "ds_read_b64_tr_b16 a[218:219], v114 offset:26112\n" \
    "ds_read_b64_tr_b16 a[60:61], v112 offset:36864\n" \
    "ds_read_b64_tr_b16 a[62:63], v112 offset:38400\n" \
    "ds_read_b64_tr_b16 a[140:141], v113 offset:36864\n" \
    "ds_read_b64_tr_b16 a[142:143], v113 offset:38400\n" \
    "ds_read_b64_tr_b16 a[220:221], v114 offset:36864\n" \
    "ds_read_b64_tr_b16 a[222:223], v114 offset:38400\n" \
    "s_waitcnt lgkmcnt(0)\n" \
    "s_waitcnt vmcnt(6)\n" \
    "s_barrier\n" \
    "ds_read_b64_tr_b16 a[64:65], v112 offset:49152\n" \
    "ds_read_b64_tr_b16 a[66:67], v112 offset:50688\n" \
    "ds_read_b64_tr_b16 a[144:145], v113 offset:49152\n" \
    "ds_read_b64_tr_b16 a[146:147], v113 offset:50688\n" \
    "ds_read_b64_tr_b16 a[224:225], v114 offset:49152\n" \
    "ds_read_b64_tr_b16 a[226:227], v114 offset:50688\n" \
    "ds_read_b64_tr_b16 a[68:69], v112 offset:61440\n" \
    "ds_read_b64_tr_b16 a[70:71], v112 offset:62976\n" \
    "ds_read_b64_tr_b16 a[148:149], v113 offset:61440\n" \
    "ds_read_b64_tr_b16 a[150:151], v113 offset:62976\n" \
    "ds_read_b64_tr_b16 a[228:229], v114 offset:61440\n" \
    "ds_read_b64_tr_b16 a[230:231], v114 offset:62976\n" \
    "s_waitcnt lgkmcnt(0)\n" \
    "s_waitcnt vmcnt(0)\n" \
    "s_barrier\n" \
    "ds_read_b64_tr_b16 a[72:73], v115 offset:40960\n" \
    "ds_read_b64_tr_b16 a[74:75], v115 offset:42496\n" \
    "ds_read_b64_tr_b16 a[152:153], v116 offset:40960\n" \
    "ds_read_b64_tr_b16 a[154:155], v116 offset:42496\n" \
    "ds_read_b64_tr_b16 a[232:233], v117 offset:40960\n" \
    "ds_read_b64_tr_b16 a[234:235], v117 offset:42496\n" \
    "ds_read_b64_tr_b16 a[76:77], v115 offset:53248\n" \
    "ds_read_b64_tr_b16 a[78:79], v115 offset:54784\n" \
    "ds_read_b64_tr_b16 a[156:157], v116 offset:53248\n" \
    "ds_read_b64_tr_b16 a[158:159], v116 offset:54784\n" \
    "ds_read_b64_tr_b16 a[236:237], v117 offset:53248\n" \
    "ds_read_b64_tr_b16 a[238:239], v117 offset:54784\n" \
    "s_waitcnt lgkmcnt(0)\n" \
    "s_barrier\n" \
    "s_add_u32 s72, s70, 40960\n" \
    "s_add_u32 s63, s70, 250650624\n" \
    "s_add_u32 m0, s72, 0\n" \
    "s_nop 0\n" \
    "buffer_load_dwordx4 %[l16], %[ws], s63 offen lds\n" \
    "buffer_load_dwordx4 %[l16], %[ws], s63 offen offset:1024 lds\n" \
    "buffer_load_dwordx4 %[l16], %[ws], s63 offen offset:2048 lds\n" \
    "buffer_load_dwordx4 %[l16], %[ws], s63 offen offset:3072 lds\n" \
    "s_add_u32 m0, s72, 4096\n" \
    "s_add_u32 s63, s63, 4096\n" \
    "buffer_load_dwordx4 %[l16], %[ws], s63 offen lds\n" \
    "buffer_load_dwordx4 %[l16], %[ws], s63 offen offset:1024 lds\n" \
    "buffer_load_dwordx4 %[l16], %[ws], s63 offen offset:2048 lds\n" \
    "buffer_load_dwordx4 %[l16], %[ws], s63 offen offset:3072 lds\n" \
    "s_add_u32 m0, s72, 8192\n" \
    "s_add_u32 s63, s63, 4096\n" \
    "buffer_load_dwordx4 %[l16], %[ws], s63 offen lds\n" \
    "buffer_load_dwordx4 %[l16], %[ws], s63 offen offset:1024 lds\n" \
    "ds_read_b128 v[16:19], %[l16]\n" \
    "ds_read_b128 v[20:23], %[l16] offset:20480\n" \
    "ds_read_b128 v[24:27], %[l16] offset:1024\n" \
    "ds_read_b128 v[28:31], %[l16] offset:21504\n" \
    "s_mov_b32 s60, 0\n" \
    "s_mov_b32 s64, 0\n" \
    "s_mov_b32 s66, %[ob]\n" \
    "s_lshr_b32 s62, s66, 8\n" \
    "s_xor_b32 s62, s62, %[par]\n" \
    "s_and_b32 s62, s62, %[msk]\n" \
    "s_add_u32 s62, s62, s66\n" \
    "v_mov_b32 v109, %[l16]\n" \
    "v_mov_b32 v108, v109\n" \
    "s_add_u32 s64, s64, 40960\n" \
    "s_cmp_eq_u32 s64, 122880\n" \
    "s_cselect_b32 s64, 0, s64\n" \
    "v_add_u32 v109, s64, %[l16]\n" \
    "s_add_u32 s65, s64, 40960\n" \
    "s_cmp_eq_u32 s65, 122880\n" \
    "s_cselect_b32 s65, 0, s65\n" \
    "s_add_u32 s65, s65, s70\n" \
    "s_min_u32 s63, s60, 61\n" \
    "s_mul_i32 s63, s63, 40960\n" \
    "s_add_u32 s63, s63, s70\n" \
    "s_add_u32 s63, s63, 250691584\n" \
    "s_waitcnt lgkmcnt(3)\n" \
    "v_mfma_f32_16x16x32_bf16 v[48:51], a[0:3], v[16:19], 0\n" \
    "ds_read_b128 v[32:35], v108 offset:2048\n" \
    "v_mfma_f32_16x16x32_bf16 v[52:55], a[80:83], v[16:19], 0\n" \
    "ds_read_b128 v[36:39], v108 offset:22528\n" \
    "v_mfma_f32_16x16x32_bf16 v[56:59], a[160:163], v[16:19], 0\n" \
    "s_waitcnt lgkmcnt(4)\n" \
    "v_mfma_f32_16x16x32_bf16 v[60:63], a[0:3], v[20:23], 0\n" \
    "v_mfma_f32_16x16x32_bf16 v[64:67], a[80:83], v[20:23], 0\n" \
    "v_mfma_f32_16x16x32_bf16 v[68:71], a[160:163], v[20:23], 0\n" \
    "s_waitcnt lgkmcnt(3)\n" \
    "v_mfma_f32_16x16x32_bf16 v[48:51], a[4:7], v[24:27], v[48:51]\n" \
    "ds_read_b128 v[40:43], v108 offset:3072\n" \
    "v_mfma_f32_16x16x32_bf16 v[52:55], a[84:87], v[24:27], v[52:55]\n" \
    "ds_read_b128 v[44:47], v108 offset:23552\n" \
    "v_mfma_f32_16x16x32_bf16 v[56:59], a[164:167], v[24:27], v[56:59]\n" \
    "s_waitcnt lgkmcnt(4)\n" \
    "v_mfma_f32_16x16x32_bf16 v[60:63], a[4:7], v[28:31], v[60:63]\n" \
    "v_mfma_f32_16x16x32_bf16 v[64:67], a[84:87], v[28:31], v[64:67]\n" \
    "v_mfma_f32_16x16x32_bf16 v[68:71], a[164:167], v[28:31], v[68:71]\n" \
    "s_waitcnt lgkmcnt(3)\n" \
    "v_mfma_f32_16x16x32_bf16 v[48:51], a[8:11], v[32:35], v[48:51]\n" \
    "ds_read_b128 v[16:19], v108 offset:4096\n" \
    "v_mfma_f32_16x16x32_bf16 v[52:55], a[88:91], v[32:35], v[52:55]\n" \
    "ds_read_b128 v[20:23], v108 offset:24576\n" \
    "v_mfma_f32_16x16x32_bf16 v[56:59], a[168:171], v[32:35], v[56:59]\n" \
    "s_waitcnt lgkmcnt(4)\n" \
    "v_mfma_f32_16x16x32_bf16 v[60:63], a[8:11], v[36:39], v[60:63]\n" \
    "v_mfma_f32_16x16x32_bf16 v[64:67], a[88:91], v[36:39], v[64:67]\n" \
    "v_mfma_f32_16x16x32_bf16 v[68:71], a[168:171], v[36:39], v[68:71]\n" \
    "s_waitcnt lgkmcnt(3)\n" \
    "v_mfma_f32_16x16x32_bf16 v[48:51], a[12:15], v[40:43], v[48:51]\n" \
    "ds_read_b128 v[24:27], v108 offset:5120\n" \
    "v_mfma_f32_16x16x32_bf16 v[52:55], a[92:95], v[40:43], v[52:55]\n" \
    "ds_read_b128 v[28:31], v108 offset:25600\n" \
    "v_mfma_f32_16x16x32_bf16 v[56:59], a[172:175], v[40:43], v[56:59]\n" \
    "s_waitcnt lgkmcnt(4)\n" \
    "v_mfma_f32_16x16x32_bf16 v[60:63], a[12:15], v[44:47], v[60:63]\n" \
    "v_mfma_f32_16x16x32_bf16 v[64:67], a[92:95], v[44:47], v[64:67]\n" \
    "v_mfma_f32_16x16x32_bf16 v[68:71], a[172:175], v[44:47], v[68:71]\n" \
    "s_waitcnt lgkmcnt(3)\n" \
    "v_mfma_f32_16x16x32_bf16 v[48:51], a[16:19], v[16:19], v[48:51]\n" \
    "ds_read_b128 v[32:35], v108 offset:6144\n" \
    "v_mfma_f32_16x16x32_bf16 v[52:55], a[96:99], v[16:19], v[52:55]\n" \
    "ds_read_b128 v[36:39], v108 offset:26624\n" \
    "v_mfma_f32_16x16x32_bf16 v[56:59], a[176:179], v[16:19], v[56:59]\n" \
    "s_waitcnt lgkmcnt(4)\n" \
    "v_mfma_f32_16x16x32_bf16 v[60:63], a[16:19], v[20:23], v[60:63]\n" \
    "v_mfma_f32_16x16x32_bf16 v[64:67], a[96:99], v[20:23], v[64:67]\n" \
    "v_mfma_f32_16x16x32_bf16 v[68:71], a[176:179], v[20:23], v[68:71]\n" \
    "s_waitcnt lgkmcnt(3)\n" \
    "v_mfma_f32_16x16x32_bf16 v[48:51], a[20:23], v[24:27], v[48:51]\n" \
    "ds_read_b128 v[40:43], v108 offset:7168\n" \
    "v_mfma_f32_16x16x32_bf16 v[52:55], a[100:103], v[24:27], v[52:55]\n" \
    "ds_read_b128 v[44:47], v108 offset:27648\n" \
    "v_mfma_f32_16x16x32_bf16 v[56:59], a[180:183], v[24:27], v[56:59]\n" \
    "s_waitcnt lgkmcnt(4)\n" \
    "v_mfma_f32_16x16x32_bf16 v[60:63], a[20:23], v[28:31], v[60:63]\n" \
    "v_mfma_f32_16x16x32_bf16 v[64:67], a[100:103], v[28:31], v[64:67]\n" \
    "v_mfma_f32_16x16x32_bf16 v[68:71], a[180:183], v[28:31], v[68:71]\n" \
    "s_waitcnt lgkmcnt(3)\n" \
    "v_mfma_f32_16x16x32_bf16 v[48:51], a[24:27], v[32:35], v[48:51]\n" \
    "ds_read_b128 v[16:19], v108 offset:8192\n" \
    "v_mfma_f32_16x16x32_bf16 v[52:55], a[104:107], v[32:35], v[52:55]\n" \
    "ds_read_b128 v[20:23], v108 offset:28672\n" \
    "v_mfma_f32_16x16x32_bf16 v[56:59], a[184:187], v[32:35], v[56:59]\n" \
    "s_waitcnt lgkmcnt(4)\n" \
    "v_mfma_f32_16x16x32_bf16 v[60:63], a[24:27], v[36:39], v[60:63]\n" \
    "v_mfma_f32_16x16x32_bf16 v[64:67], a[104:107], v[36:39], v[64:67]\n" \
    "v_mfma_f32_16x16x32_bf16 v[68:71], a[184:187], v[36:39], v[68:71]\n" \
    "s_waitcnt lgkmcnt(3)\n" \
    "v_mfma_f32_16x16x32_bf16 v[48:51], a[28:31], v[40:43], v[48:51]\n" \
    "ds_read_b128 v[24:27], v108 offset:9216\n" \
    "v_mfma_f32_16x16x32_bf16 v[52:55], a[108:111], v[40:43], v[52:55]\n" \
    "ds_read_b128 v[28:31], v108 offset:29696\n" \
    "v_mfma_f32_16x16x32_bf16 v[56:59], a[188:191], v[40:43], v[56:59]\n" \
    "s_waitcnt lgkmcnt(4)\n" \
    "v_mfma_f32_16x16x32_bf16 v[60:63], a[28:31], v[44:47], v[60:63]\n" \
    "v_mfma_f32_16x16x32_bf16 v[64:67], a[108:111], v[44:47], v[64:67]\n" \
    "v_mfma_f32_16x16x32_bf16 v[68:71], a[188:191], v[44:47], v[68:71]\n" \
    "s_waitcnt lgkmcnt(3)\n" \
    "v_mfma_f32_16x16x32_bf16 v[48:51], a[32:35], v[16:19], v[48:51]\n" \
    "ds_read_b128 v[32:35], v108 offset:10240\n" \
    "v_mfma_f32_16x16x32_bf16 v[52:55], a[112:115], v[16:19], v[52:55]\n" \
    "ds_read_b128 v[36:39], v108 offset:30720\n" \
    "v_mfma_f32_16x16x32_bf16 v[56:59], a[192:195], v[16:19], v[56:59]\n" \
    "s_waitcnt lgkmcnt(4)\n" \
    "v_mfma_f32_16x16x32_bf16 v[60:63], a[32:35], v[20:23], v[60:63]\n" \
    "v_mfma_f32_16x16x32_bf16 v[64:67], a[112:115], v[20:23], v[64:67]\n" \
    "v_mfma_f32_16x16x32_bf16 v[68:71], a[192:195], v[20:23], v[68:71]\n" \
    "s_waitcnt lgkmcnt(3)\n" \
    "v_mfma_f32_16x16x32_bf16 v[48:51], a[36:39], v[24:27], v[48:51]\n" \
    "ds_read_b128 v[40:43], v108 offset:11264\n" \
    "v_mfma_f32_16x16x32_bf16 v[52:55], a[116:119], v[24:27], v[52:55]\n" \
    "ds_read_b128 v[44:47], v108 offset:31744\n" \
    "v_mfma_f32_16x16x32_bf16 v[56:59], a[196:199], v[24:27], v[56:59]\n" \
    "s_waitcnt lgkmcnt(4)\n" \
    "v_mfma_f32_16x16x32_bf16 v[60:63], a[36:39], v[28:31], v[60:63]\n" \
    "v_mfma_f32_16x16x32_bf16 v[64:67], a[116:119], v[28:31], v[64:67]\n" \
    "v_mfma_f32_16x16x32_bf16 v[68:71], a[196:199], v[28:31], v[68:71]\n" \
    "s_waitcnt lgkmcnt(3)\n" \
    "v_mfma_f32_16x16x32_bf16 v[48:51], a[40:43], v[32:35], v[48:51]\n" \
    "ds_read_b128 v[16:19], v108 offset:12288\n" \
    "v_mfma_f32_16x16x32_bf16 v[52:55], a[120:123], v[32:35], v[52:55]\n" \
    "ds_read_b128 v[20:23], v108 offset:32768\n" \
    "v_mfma_f32_16x16x32_bf16 v[56:59], a[200:203], v[32:35], v[56:59]\n" \
    "s_waitcnt lgkmcnt(4)\n" \
    "v_mfma_f32_16x16x32_bf16 v[60:63], a[40:43], v[36:39], v[60:63]\n" \
    "v_mfma_f32_16x16x32_bf16 v[64:67], a[120:123], v[36:39], v[64:67]\n" \
    "v_mfma_f32_16x16x32_bf16 v[68:71], a[200:203], v[36:39], v[68:71]\n" \
    "s_waitcnt lgkmcnt(3)\n" \
    "v_mfma_f32_16x16x32_bf16 v[48:51], a[44:47], v[40:43], v[48:51]\n" \
    "ds_read_b128 v[24:27], v108 offset:13312\n" \
    "v_mfma_f32_16x16x32_bf16 v[52:55], a[124:127], v[40:43], v[52:55]\n" \
    "ds_read_b128 v[28:31], v108 offset:33792\n" \
    "v_mfma_f32_16x16x32_bf16 v[56:59], a[204:207], v[40:43], v[56:59]\n" \
    "s_waitcnt lgkmcnt(4)\n" \
    "v_mfma_f32_16x16x32_bf16 v[60:63], a[44:47], v[44:47], v[60:63]\n" \
    "v_mfma_f32_16x16x32_bf16 v[64:67], a[124:127], v[44:47], v[64:67]\n" \
    "v_mfma_f32_16x16x32_bf16 v[68:71], a[204:207], v[44:47], v[68:71]\n" \
    "s_waitcnt lgkmcnt(3)\n" \
    "v_mfma_f32_16x16x32_bf16 v[48:51], a[48:51], v[16:19], v[48:51]\n" \
    "ds_read_b128 v[32:35], v108 offset:14336\n" \
    "v_mfma_f32_16x16x32_bf16 v[52:55], a[128:131], v[16:19], v[52:55]\n" \
    "ds_read_b128 v[36:39], v108 offset:34816\n" \
    "v_mfma_f32_16x16x32_bf16 v[56:59], a[208:211], v[16:19], v[56:59]\n" \
    "s_waitcnt lgkmcnt(4)\n" \
    "v_mfma_f32_16x16x32_bf16 v[60:63], a[48:51], v[20:23], v[60:63]\n" \
    "v_mfma_f32_16x16x32_bf16 v[64:67], a[128:131], v[20:23], v[64:67]\n" \
    "v_mfma_f32_16x16x32_bf16 v[68:71], a[208:211], v[20:23], v[68:71]\n" \
    "s_waitcnt vmcnt(0)\n" \
    "s_barrier\n" \
    "s_waitcnt lgkmcnt(3)\n" \
    "v_mfma_f32_16x16x32_bf16 v[48:51], a[52:55], v[24:27], v[48:51]\n" \
    "ds_read_b128 v[40:43], v108 offset:15360\n" \
    "v_mfma_f32_16x16x32_bf16 v[52:55], a[132:135], v[24:27], v[52:55]\n" \
    "ds_read_b128 v[44:47], v108 offset:35840\n" \
    "v_mfma_f32_16x16x32_bf16 v[56:59], a[212:215], v[24:27], v[56:59]\n" \
    "s_add_u32 m0, s65, 0\n" \
    "s_nop 0\n" \
    "buffer_load_dwordx4 %[l16], %[ws], s63 offen lds\n" \
    "s_waitcnt lgkmcnt(4)\n" \
    "v_mfma_f32_16x16x32_bf16 v[60:63], a[52:55], v[28:31], v[60:63]\n" \
    "v_mfma_f32_16x16x32_bf16 v[64:67], a[132:135], v[28:31], v[64:67]\n" \
    "buffer_load_dwordx4 %[l16], %[ws], s63 offen offset:1024 lds\n" \
    "v_mfma_f32_16x16x32_bf16 v[68:71], a[212:215], v[28:31], v[68:71]\n" \
    "s_waitcnt lgkmcnt(3)\n" \
    "v_mfma_f32_16x16x32_bf16 v[48:51], a[56:59], v[32:35], v[48:51]\n" \
    "ds_read_b128 v[16:19], v108 offset:16384\n" \
    "v_mfma_f32_16x16x32_bf16 v[52:55], a[136:139], v[32:35], v[52:55]\n" \
    "ds_read_b128 v[20:23], v108 offset:36864\n" \
    "v_mfma_f32_16x16x32_bf16 v[56:59], a[216:219], v[32:35], v[56:59]\n" \
    "buffer_load_dwordx4 %[l16], %[ws], s63 offen offset:2048 lds\n" \
    "s_waitcnt lgkmcnt(4)\n" \
    "v_mfma_f32_16x16x32_bf16 v[60:63], a[56:59], v[36:39], v[60:63]\n" \
    "v_mfma_f32_16x16x32_bf16 v[64:67], a[136:139], v[36:39], v[64:67]\n" \
    "buffer_load_dwordx4 %[l16], %[ws], s63 offen offset:3072 lds\n" \
    "v_mfma_f32_16x16x32_bf16 v[68:71], a[216:219], v[36:39], v[68:71]\n" \
    "s_waitcnt lgkmcnt(3)\n" \
    "v_mfma_f32_16x16x32_bf16 v[48:51], a[60:63], v[40:43], v[48:51]\n" \
    "ds_read_b128 v[24:27], v108 offset:17408\n" \
    "v_mfma_f32_16x16x32_bf16 v[52:55], a[140:143], v[40:43], v[52:55]\n" \
    "ds_read_b128 v[28:31], v108 offset:37888\n" \
    "v_mfma_f32_16x16x32_bf16 v[56:59], a[220:223], v[40:43], v[56:59]\n" \
    "s_add_u32 m0, s65, 4096\n" \
    "s_add_u32 s63, s63, 4096\n" \
    "buffer_load_dwordx4 %[l16], %[ws], s63 offen lds\n" \
    "s_waitcnt lgkmcnt(4)\n" \
    "v_mfma_f32_16x16x32_bf16 v[60:63], a[60:63], v[44:47], v[60:63]\n" \
    "v_mfma_f32_16x16x32_bf16 v[64:67], a[140:143], v[44:47], v[64:67]\n" \
    "buffer_load_dwordx4 %[l16], %[ws], s63 offen offset:1024 lds\n" \
    "v_mfma_f32_16x16x32_bf16 v[68:71], a[220:223], v[44:47], v[68:71]\n" \
    "s_waitcnt lgkmcnt(3)\n" \
    "v_mfma_f32_16x16x32_bf16 v[48:51], a[64:67], v[16:19], v[48:51]\n" \
    "ds_read_b128 v[32:35], v108 offset:18432\n" \
    "v_mfma_f32_16x16x32_bf16 v[52:55], a[144:147], v[16:19], v[52:55]\n" \
    "ds_read_b128 v[36:39], v108 offset:38912\n" \
    "v_mfma_f32_16x16x32_bf16 v[56:59], a[224:227], v[16:19], v[56:59]\n" \
    "buffer_load_dwordx4 %[l16], %[ws], s63 offen offset:2048 lds\n" \
    "s_waitcnt lgkmcnt(4)\n" \
    "v_mfma_f32_16x16x32_bf16 v[60:63], a[64:67], v[20:23], v[60:63]\n" \
    "v_mfma_f32_16x16x32_bf16 v[64:67], a[144:147], v[20:23], v[64:67]\n" \
    "buffer_load_dwordx4 %[l16], %[ws], s63 offen offset:3072 lds\n" \
    "v_mfma_f32_16x16x32_bf16 v[68:71], a[224:227], v[20:23], v[68:71]\n" \
    "s_waitcnt lgkmcnt(3)\n" \
    "v_mfma_f32_16x16x32_bf16 v[48:51], a[68:71], v[24:27], v[48:51]\n" \
    "ds_read_b128 v[40:43], v108 offset:19456\n" \
    "v_mfma_f32_16x16x32_bf16 v[52:55], a[148:151], v[24:27], v[52:55]\n" \
    "ds_read_b128 v[44:47], v108 offset:39936\n" \
    "v_mfma_f32_16x16x32_bf16 v[56:59], a[228:231], v[24:27], v[56:59]\n" \
    "s_add_u32 m0, s65, 8192\n" \
    "s_add_u32 s63, s63, 4096\n" \
    "buffer_load_dwordx4 %[l16], %[ws], s63 offen lds\n" \
    "s_waitcnt lgkmcnt(4)\n" \
    "v_mfma_f32_16x16x32_bf16 v[60:63], a[68:71], v[28:31], v[60:63]\n" \
    "v_mfma_f32_16x16x32_bf16 v[64:67], a[148:151], v[28:31], v[64:67]\n" \
    "buffer_load_dwordx4 %[l16], %[ws], s63 offen offset:1024 lds\n" \
    "v_mfma_f32_16x16x32_bf16 v[68:71], a[228:231], v[28:31], v[68:71]\n" \
    "s_waitcnt lgkmcnt(3)\n" \
    "v_mfma_f32_16x16x32_bf16 v[48:51], a[72:75], v[32:35], v[48:51]\n" \
    "ds_read_b128 v[16:19], v109\n" \
    "v_mfma_f32_16x16x32_bf16 v[52:55], a[152:155], v[32:35], v[52:55]\n" \
    "ds_read_b128 v[20:23], v109 offset:20480\n" \
    "v_mfma_f32_16x16x32_bf16 v[56:59], a[232:235], v[32:35], v[56:59]\n" \
    "s_waitcnt lgkmcnt(4)\n" \
    "v_mfma_f32_16x16x32_bf16 v[60:63], a[72:75], v[36:39], v[60:63]\n" \
    "v_mfma_f32_16x16x32_bf16 v[64:67], a[152:155], v[36:39], v[64:67]\n" \
    "v_mfma_f32_16x16x32_bf16 v[68:71], a[232:235], v[36:39], v[68:71]\n" \
    "s_waitcnt lgkmcnt(3)\n" \
    "v_mfma_f32_16x16x32_bf16 v[48:51], a[76:79], v[40:43], v[48:51]\n" \
    "ds_read_b128 v[24:27], v109 offset:1024\n" \
    "v_mfma_f32_16x16x32_bf16 v[52:55], a[156:159], v[40:43], v[52:55]\n" \
    "ds_read_b128 v[28:31], v109 offset:21504\n" \
    "v_mfma_f32_16x16x32_bf16 v[56:59], a[236:239], v[40:43], v[56:59]\n" \
    "s_waitcnt lgkmcnt(4)\n" \
    "v_mfma_f32_16x16x32_bf16 v[60:63], a[76:79], v[44:47], v[60:63]\n" \
    "v_mfma_f32_16x16x32_bf16 v[64:67], a[156:159], v[44:47], v[64:67]\n" \
    "v_mfma_f32_16x16x32_bf16 v[68:71], a[236:239], v[44:47], v[68:71]\n" \
    "s_add_u32 s60, s60, 1\n" \
    "v_cvt_pk_bf16_f32 v96, v48, v49\n" \
    "v_cvt_pk_bf16_f32 v97, v50, v51\n" \
    "s_nop 2\n" \
    "v_cvt_pk_bf16_f32 v98, v60, v61\n" \
    "v_cvt_pk_bf16_f32 v99, v62, v63\n" \
    "buffer_store_dwordx4 v[96:99], %[l16], %[ws], s62 offen sc1\n" \
    "v_cvt_pk_bf16_f32 v100, v52, v53\n" \
    "v_cvt_pk_bf16_f32 v101, v54, v55\n" \
    "v_cvt_pk_bf16_f32 v102, v64, v65\n" \
    "v_cvt_pk_bf16_f32 v103, v66, v67\n" \
    "buffer_store_dwordx4 v[100:103], %[l16], %[ws], s62 offen offset:1024 sc1\n" \
    "v_cvt_pk_bf16_f32 v104, v56, v57\n" \
    "v_cvt_pk_bf16_f32 v105, v58, v59\n" \
    "v_cvt_pk_bf16_f32 v106, v68, v69\n" \
    "v_cvt_pk_bf16_f32 v107, v70, v71\n" \
    "buffer_store_dwordx4 v[104:107], %[l16], %[ws], s62 offen offset:2048 sc1\n" \
    "s_waitcnt vmcnt(0)\n" \
    "s_barrier\n" \
    "s_cmp_lg_u32 %[wave], 0\n" \
    "s_cbranch_scc1 Lp_fla\n" \
    "v_mov_b32 v110, s60\n" \
    "buffer_store_dword v110, %[one], %[ws], %[flag] offen sc1\n" \
    "Lp_fla:\n" \
    "s_add_u32 s66, s66, %[ost]\n" \
    "s_lshr_b32 s62, s66, 8\n" \
    "s_xor_b32 s62, s62, %[par]\n" \
    "s_and_b32 s62, s62, %[msk]\n" \
    "s_add_u32 s62, s62, s66\n" \
    "v_mov_b32 v108, v109\n" \
    "s_add_u32 s64, s64, 40960\n" \
    "s_cmp_eq_u32 s64, 122880\n" \
    "s_cselect_b32 s64, 0, s64\n" \
    "v_add_u32 v109, s64, %[l16]\n" \
    "s_add_u32 s65, s64, 40960\n" \
    "s_cmp_eq_u32 s65, 122880\n" \
    "s_cselect_b32 s65, 0, s65\n" \
    "s_add_u32 s65, s65, s70\n" \
    "s_min_u32 s63, s60, 61\n" \
    "s_mul_i32 s63, s63, 40960\n" \
    "s_add_u32 s63, s63, s70\n" \
    "s_add_u32 s63, s63, 250691584\n" \
    "s_waitcnt lgkmcnt(3)\n" \
    "v_mfma_f32_16x16x32_bf16 v[72:75], a[0:3], v[16:19], 0\n" \
    "ds_read_b128 v[32:35], v108 offset:2048\n" \
    "v_mfma_f32_16x16x32_bf16 v[76:79], a[80:83], v[16:19], 0\n" \
    "ds_read_b128 v[36:39], v108 offset:22528\n" \
    "v_mfma_f32_16x16x32_bf16 v[80:83], a[160:163], v[16:19], 0\n" \
    "s_waitcnt lgkmcnt(4)\n" \
    "v_mfma_f32_16x16x32_bf16 v[84:87], a[0:3], v[20:23], 0\n" \
    "v_mfma_f32_16x16x32_bf16 v[88:91], a[80:83], v[20:23], 0\n" \
    "v_mfma_f32_16x16x32_bf16 v[92:95], a[160:163], v[20:23], 0\n" \
    "s_waitcnt lgkmcnt(3)\n" \
    "v_mfma_f32_16x16x32_bf16 v[72:75], a[4:7], v[24:27], v[72:75]\n" \
    "ds_read_b128 v[40:43], v108 offset:3072\n" \
    "v_mfma_f32_16x16x32_bf16 v[76:79], a[84:87], v[24:27], v[76:79]\n" \
    "ds_read_b128 v[44:47], v108 offset:23552\n" \
    "v_mfma_f32_16x16x32_bf16 v[80:83], a[164:167], v[24:27], v[80:83]\n" \
    "s_waitcnt lgkmcnt(4)\n" \
    "v_mfma_f32_16x16x32_bf16 v[84:87], a[4:7], v[28:31], v[84:87]\n" \
    "v_mfma_f32_16x16x32_bf16 v[88:91], a[84:87], v[28:31], v[88:91]\n" \
    "v_mfma_f32_16x16x32_bf16 v[92:95], a[164:167], v[28:31], v[92:95]\n" \
    "s_waitcnt lgkmcnt(3)\n" \
    "v_mfma_f32_16x16x32_bf16 v[72:75], a[8:11], v[32:35], v[72:75]\n" \
    "ds_read_b128 v[16:19], v108 offset:4096\n" \
    "v_mfma_f32_16x16x32_bf16 v[76:79], a[88:91], v[32:35], v[76:79]\n" \
    "ds_read_b128 v[20:23], v108 offset:24576\n" \
    "v_mfma_f32_16x16x32_bf16 v[80:83], a[168:171], v[32:35], v[80:83]\n" \
    "s_waitcnt lgkmcnt(4)\n" \
    "v_mfma_f32_16x16x32_bf16 v[84:87], a[8:11], v[36:39], v[84:87]\n" \
    "v_mfma_f32_16x16x32_bf16 v[88:91], a[88:91], v[36:39], v[88:91]\n" \
    "v_mfma_f32_16x16x32_bf16 v[92:95], a[168:171], v[36:39], v[92:95]\n" \
    "s_waitcnt lgkmcnt(3)\n" \
    "v_mfma_f32_16x16x32_bf16 v[72:75], a[12:15], v[40:43], v[72:75]\n" \
    "ds_read_b128 v[24:27], v108 offset:5120\n" \
    "v_mfma_f32_16x16x32_bf16 v[76:79], a[92:95], v[40:43], v[76:79]\n" \
    "ds_read_b128 v[28:31], v108 offset:25600\n" \
    "v_mfma_f32_16x16x32_bf16 v[80:83], a[172:175], v[40:43], v[80:83]\n" \
    "s_waitcnt lgkmcnt(4)\n" \
    "v_mfma_f32_16x16x32_bf16 v[84:87], a[12:15], v[44:47], v[84:87]\n" \
    "v_mfma_f32_16x16x32_bf16 v[88:91], a[92:95], v[44:47], v[88:91]\n" \
    "v_mfma_f32_16x16x32_bf16 v[92:95], a[172:175], v[44:47], v[92:95]\n" \
    "s_waitcnt lgkmcnt(3)\n" \
    "v_mfma_f32_16x16x32_bf16 v[72:75], a[16:19], v[16:19], v[72:75]\n" \
    "ds_read_b128 v[32:35], v108 offset:6144\n" \
    "v_mfma_f32_16x16x32_bf16 v[76:79], a[96:99], v[16:19], v[76:79]\n" \
    "ds_read_b128 v[36:39], v108 offset:26624\n" \
    "v_mfma_f32_16x16x32_bf16 v[80:83], a[176:179], v[16:19], v[80:83]\n" \
    "s_waitcnt lgkmcnt(4)\n" \
    "v_mfma_f32_16x16x32_bf16 v[84:87], a[16:19], v[20:23], v[84:87]\n" \
    "v_mfma_f32_16x16x32_bf16 v[88:91], a[96:99], v[20:23], v[88:91]\n" \
    "v_mfma_f32_16x16x32_bf16 v[92:95], a[176:179], v[20:23], v[92:95]\n" \
    "s_waitcnt lgkmcnt(3)\n" \
    "v_mfma_f32_16x16x32_bf16 v[72:75], a[20:23], v[24:27], v[72:75]\n" \
    "ds_read_b128 v[40:43], v108 offset:7168\n" \
    "v_mfma_f32_16x16x32_bf16 v[76:79], a[100:103], v[24:27], v[76:79]\n" \
    "ds_read_b128 v[44:47], v108 offset:27648\n" \
    "v_mfma_f32_16x16x32_bf16 v[80:83], a[180:183], v[24:27], v[80:83]\n" \
    "s_waitcnt lgkmcnt(4)\n" \
    "v_mfma_f32_16x16x32_bf16 v[84:87], a[20:23], v[28:31], v[84:87]\n" \
    "v_mfma_f32_16x16x32_bf16 v[88:91], a[100:103], v[28:31], v[88:91]\n" \
    "v_mfma_f32_16x16x32_bf16 v[92:95], a[180:183], v[28:31], v[92:95]\n" \
    "s_waitcnt lgkmcnt(3)\n" \
    "v_mfma_f32_16x16x32_bf16 v[72:75], a[24:27], v[32:35], v[72:75]\n" \
    "ds_read_b128 v[16:19], v108 offset:8192\n" \
    "v_mfma_f32_16x16x32_bf16 v[76:79], a[104:107], v[32:35], v[76:79]\n" \
    "ds_read_b128 v[20:23], v108 offset:28672\n" \
    "v_mfma_f32_16x16x32_bf16 v[80:83], a[184:187], v[32:35], v[80:83]\n" \
    "s_waitcnt lgkmcnt(4)\n" \
    "v_mfma_f32_16x16x32_bf16 v[84:87], a[24:27], v[36:39], v[84:87]\n" \
    "v_mfma_f32_16x16x32_bf16 v[88:91], a[104:107], v[36:39], v[88:91]\n" \
    "v_mfma_f32_16x16x32_bf16 v[92:95], a[184:187], v[36:39], v[92:95]\n" \
    "s_waitcnt lgkmcnt(3)\n" \
    "v_mfma_f32_16x16x32_bf16 v[72:75], a[28:31], v[40:43], v[72:75]\n" \
    "ds_read_b128 v[24:27], v108 offset:9216\n" \
    "v_mfma_f32_16x16x32_bf16 v[76:79], a[108:111], v[40:43], v[76:79]\n" \
    "ds_read_b128 v[28:31], v108 offset:29696\n" \
    "v_mfma_f32_16x16x32_bf16 v[80:83], a[188:191], v[40:43], v[80:83]\n" \
    "s_waitcnt lgkmcnt(4)\n" \
    "v_mfma_f32_16x16x32_bf16 v[84:87], a[28:31], v[44:47], v[84:87]\n" \
    "v_mfma_f32_16x16x32_bf16 v[88:91], a[108:111], v[44:47], v[88:91]\n" \
    "v_mfma_f32_16x16x32_bf16 v[92:95], a[188:191], v[44:47], v[92:95]\n" \
    "s_waitcnt lgkmcnt(3)\n" \
    "v_mfma_f32_16x16x32_bf16 v[72:75], a[32:35], v[16:19], v[72:75]\n" \
    "ds_read_b128 v[32:35], v108 offset:10240\n" \
    "v_mfma_f32_16x16x32_bf16 v[76:79], a[112:115], v[16:19], v[76:79]\n" \
    "ds_read_b128 v[36:39], v108 offset:30720\n" \
    "v_mfma_f32_16x16x32_bf16 v[80:83], a[192:195], v[16:19], v[80:83]\n" \
    "s_waitcnt lgkmcnt(4)\n" \
    "v_mfma_f32_16x16x32_bf16 v[84:87], a[32:35], v[20:23], v[84:87]\n" \
    "v_mfma_f32_16x16x32_bf16 v[88:91], a[112:115], v[20:23], v[88:91]\n" \
    "v_mfma_f32_16x16x32_bf16 v[92:95], a[192:195], v[20:23], v[92:95]\n" \
    "s_waitcnt lgkmcnt(3)\n" \
    "v_mfma_f32_16x16x32_bf16 v[72:75], a[36:39], v[24:27], v[72:75]\n" \
    "ds_read_b128 v[40:43], v108 offset:11264\n" \
    "v_mfma_f32_16x16x32_bf16 v[76:79], a[116:119], v[24:27], v[76:79]\n" \
    "ds_read_b128 v[44:47], v108 offset:31744\n" \
    "v_mfma_f32_16x16x32_bf16 v[80:83], a[196:199], v[24:27], v[80:83]\n" \
    "s_waitcnt lgkmcnt(4)\n" \
    "v_mfma_f32_16x16x32_bf16 v[84:87], a[36:39], v[28:31], v[84:87]\n" \
    "v_mfma_f32_16x16x32_bf16 v[88:91], a[116:119], v[28:31], v[88:91]\n" \
    "v_mfma_f32_16x16x32_bf16 v[92:95], a[196:199], v[28:31], v[92:95]\n" \
    "s_waitcnt lgkmcnt(3)\n" \
    "v_mfma_f32_16x16x32_bf16 v[72:75], a[40:43], v[32:35], v[72:75]\n" \
    "ds_read_b128 v[16:19], v108 offset:12288\n" \
    "v_mfma_f32_16x16x32_bf16 v[76:79], a[120:123], v[32:35], v[76:79]\n" \
    "ds_read_b128 v[20:23], v108 offset:32768\n" \
    "v_mfma_f32_16x16x32_bf16 v[80:83], a[200:203], v[32:35], v[80:83]\n" \
    "s_waitcnt lgkmcnt(4)\n" \
    "v_mfma_f32_16x16x32_bf16 v[84:87], a[40:43], v[36:39], v[84:87]\n" \
    "v_mfma_f32_16x16x32_bf16 v[88:91], a[120:123], v[36:39], v[88:91]\n" \
    "v_mfma_f32_16x16x32_bf16 v[92:95], a[200:203], v[36:39], v[92:95]\n" \
    "s_waitcnt lgkmcnt(3)\n" \
    "v_mfma_f32_16x16x32_bf16 v[72:75], a[44:47], v[40:43], v[72:75]\n" \
    "ds_read_b128 v[24:27], v108 offset:13312\n" \
    "v_mfma_f32_16x16x32_bf16 v[76:79], a[124:127], v[40:43], v[76:79]\n" \
    "ds_read_b128 v[28:31], v108 offset:33792\n" \
    "v_mfma_f32_16x16x32_bf16 v[80:83], a[204:207], v[40:43], v[80:83]\n" \
    "s_waitcnt lgkmcnt(4)\n" \
    "v_mfma_f32_16x16x32_bf16 v[84:87], a[44:47], v[44:47], v[84:87]\n" \
    "v_mfma_f32_16x16x32_bf16 v[88:91], a[124:127], v[44:47], v[88:91]\n" \
    "v_mfma_f32_16x16x32_bf16 v[92:95], a[204:207], v[44:47], v[92:95]\n" \
    "s_waitcnt vmcnt(0)\n" \
    "s_barrier\n" \
    "s_waitcnt lgkmcnt(3)\n" \
    "v_mfma_f32_16x16x32_bf16 v[72:75], a[48:51], v[16:19], v[72:75]\n" \
    "ds_read_b128 v[32:35], v108 offset:14336\n" \
    "v_mfma_f32_16x16x32_bf16 v[76:79], a[128:131], v[16:19], v[76:79]\n" \
    "ds_read_b128 v[36:39], v108 offset:34816\n" \
    "v_mfma_f32_16x16x32_bf16 v[80:83], a[208:211], v[16:19], v[80:83]\n" \
    "s_add_u32 m0, s65, 0\n" \
    "s_nop 0\n" \
    "buffer_load_dwordx4 %[l16], %[ws], s63 offen lds\n" \
    "s_waitcnt lgkmcnt(4)\n" \
    "v_mfma_f32_16x16x32_bf16 v[84:87], a[48:51], v[20:23], v[84:87]\n" \
    "v_mfma_f32_16x16x32_bf16 v[88:91], a[128:131], v[20:23], v[88:91]\n" \
    "buffer_load_dwordx4 %[l16], %[ws], s63 offen offset:1024 lds\n" \
    "v_mfma_f32_16x16x32_bf16 v[92:95], a[208:211], v[20:23], v[92:95]\n" \
    "s_waitcnt lgkmcnt(3)\n" \
    "v_mfma_f32_16x16x32_bf16 v[72:75], a[52:55], v[24:27], v[72:75]\n" \
    "ds_read_b128 v[40:43], v108 offset:15360\n" \
    "v_mfma_f32_16x16x32_bf16 v[76:79], a[132:135], v[24:27], v[76:79]\n" \
    "ds_read_b128 v[44:47], v108 offset:35840\n" \
    "v_mfma_f32_16x16x32_bf16 v[80:83], a[212:215], v[24:27], v[80:83]\n" \
    "buffer_load_dwordx4 %[l16], %[ws], s63 offen offset:2048 lds\n" \
    "s_waitcnt lgkmcnt(4)\n" \
    "v_mfma_f32_16x16x32_bf16 v[84:87], a[52:55], v[28:31], v[84:87]\n" \
    "v_mfma_f32_16x16x32_bf16 v[88:91], a[132:135], v[28:31], v[88:91]\n" \
    "buffer_load_dwordx4 %[l16], %[ws], s63 offen offset:3072 lds\n" \
    "v_mfma_f32_16x16x32_bf16 v[92:95], a[212:215], v[28:31], v[92:95]\n" \
    "s_waitcnt lgkmcnt(3)\n" \
    "v_mfma_f32_16x16x32_bf16 v[72:75], a[56:59], v[32:35], v[72:75]\n" \
    "ds_read_b128 v[16:19], v108 offset:16384\n" \
    "v_mfma_f32_16x16x32_bf16 v[76:79], a[136:139], v[32:35], v[76:79]\n" \
    "ds_read_b128 v[20:23], v108 offset:36864\n" \
    "v_mfma_f32_16x16x32_bf16 v[80:83], a[216:219], v[32:35], v[80:83]\n" \
    "s_add_u32 m0, s65, 4096\n" \
    "s_add_u32 s63, s63, 4096\n" \
    "buffer_load_dwordx4 %[l16], %[ws], s63 offen lds\n" \
    "s_waitcnt lgkmcnt(4)\n" \
    "v_mfma_f32_16x16x32_bf16 v[84:87], a[56:59], v[36:39], v[84:87]\n" \
    "v_mfma_f32_16x16x32_bf16 v[88:91], a[136:139], v[36:39], v[88:91]\n" \
    "buffer_load_dwordx4 %[l16], %[ws], s63 offen offset:1024 lds\n" \
    "v_mfma_f32_16x16x32_bf16 v[92:95], a[216:219], v[36:39], v[92:95]\n" \
    "s_waitcnt lgkmcnt(3)\n" \
    "v_mfma_f32_16x16x32_bf16 v[72:75], a[60:63], v[40:43], v[72:75]\n" \
    "ds_read_b128 v[24:27], v108 offset:17408\n" \
    "v_mfma_f32_16x16x32_bf16 v[76:79], a[140:143], v[40:43], v[76:79]\n" \
    "ds_read_b128 v[28:31], v108 offset:37888\n" \
    "v_mfma_f32_16x16x32_bf16 v[80:83], a[220:223], v[40:43], v[80:83]\n" \
    "buffer_load_dwordx4 %[l16], %[ws], s63 offen offset:2048 lds\n" \
    "s_waitcnt lgkmcnt(4)\n" \
    "v_mfma_f32_16x16x32_bf16 v[84:87], a[60:63], v[44:47], v[84:87]\n" \
    "v_mfma_f32_16x16x32_bf16 v[88:91], a[140:143], v[44:47], v[88:91]\n" \
    "buffer_load_dwordx4 %[l16], %[ws], s63 offen offset:3072 lds\n" \
    "v_mfma_f32_16x16x32_bf16 v[92:95], a[220:223], v[44:47], v[92:95]\n" \
    "s_waitcnt lgkmcnt(3)\n" \
    "v_mfma_f32_16x16x32_bf16 v[72:75], a[64:67], v[16:19], v[72:75]\n" \
    "ds_read_b128 v[32:35], v108 offset:18432\n" \
    "v_mfma_f32_16x16x32_bf16 v[76:79], a[144:147], v[16:19], v[76:79]\n" \
    "ds_read_b128 v[36:39], v108 offset:38912\n" \
    "v_mfma_f32_16x16x32_bf16 v[80:83], a[224:227], v[16:19], v[80:83]\n" \
    "s_add_u32 m0, s65, 8192\n" \
    "s_add_u32 s63, s63, 4096\n" \
    "buffer_load_dwordx4 %[l16], %[ws], s63 offen lds\n" \
    "s_waitcnt lgkmcnt(4)\n" \
    "v_mfma_f32_16x16x32_bf16 v[84:87], a[64:67], v[20:23], v[84:87]\n" \
    "v_mfma_f32_16x16x32_bf16 v[88:91], a[144:147], v[20:23], v[88:91]\n" \
    "buffer_load_dwordx4 %[l16], %[ws], s63 offen offset:1024 lds\n" \
    "v_mfma_f32_16x16x32_bf16 v[92:95], a[224:227], v[20:23], v[92:95]\n" \
    "s_waitcnt lgkmcnt(3)\n" \
    "v_mfma_f32_16x16x32_bf16 v[72:75], a[68:71], v[24:27], v[72:75]\n" \
    "ds_read_b128 v[40:43], v108 offset:19456\n" \
    "v_mfma_f32_16x16x32_bf16 v[76:79], a[148:151], v[24:27], v[76:79]\n" \
    "ds_read_b128 v[44:47], v108 offset:39936\n" \
    "v_mfma_f32_16x16x32_bf16 v[80:83], a[228:231], v[24:27], v[80:83]\n" \
    "s_waitcnt lgkmcnt(4)\n" \
    "v_mfma_f32_16x16x32_bf16 v[84:87], a[68:71], v[28:31], v[84:87]\n" \
    "v_mfma_f32_16x16x32_bf16 v[88:91], a[148:151], v[28:31], v[88:91]\n" \
    "v_mfma_f32_16x16x32_bf16 v[92:95], a[228:231], v[28:31], v[92:95]\n" \
    "s_waitcnt lgkmcnt(3)\n" \
    "v_mfma_f32_16x16x32_bf16 v[72:75], a[72:75], v[32:35], v[72:75]\n" \
    "ds_read_b128 v[16:19], v109\n" \
    "v_mfma_f32_16x16x32_bf16 v[76:79], a[152:155], v[32:35], v[76:79]\n" \
    "ds_read_b128 v[20:23], v109 offset:20480\n" \
    "v_mfma_f32_16x16x32_bf16 v[80:83], a[232:235], v[32:35], v[80:83]\n" \
    "s_waitcnt lgkmcnt(4)\n" \
    "v_mfma_f32_16x16x32_bf16 v[84:87], a[72:75], v[36:39], v[84:87]\n" \
    "v_mfma_f32_16x16x32_bf16 v[88:91], a[152:155], v[36:39], v[88:91]\n" \
    "v_mfma_f32_16x16x32_bf16 v[92:95], a[232:235], v[36:39], v[92:95]\n" \
    "s_waitcnt lgkmcnt(3)\n" \
    "v_mfma_f32_16x16x32_bf16 v[72:75], a[76:79], v[40:43], v[72:75]\n" \
    "ds_read_b128 v[24:27], v109 offset:1024\n" \
    "v_mfma_f32_16x16x32_bf16 v[76:79], a[156:159], v[40:43], v[76:79]\n" \
    "ds_read_b128 v[28:31], v109 offset:21504\n" \
    "v_mfma_f32_16x16x32_bf16 v[80:83], a[236:239], v[40:43], v[80:83]\n" \
    "s_waitcnt lgkmcnt(4)\n" \
    "v_mfma_f32_16x16x32_bf16 v[84:87], a[76:79], v[44:47], v[84:87]\n" \
    "v_mfma_f32_16x16x32_bf16 v[88:91], a[156:159], v[44:47], v[88:91]\n" \
    "v_mfma_f32_16x16x32_bf16 v[92:95], a[236:239], v[44:47], v[92:95]\n" \
    "s_add_u32 s60, s60, 1\n" \
    "Lp_loop:\n" \
    "v_mov_b32 v108, v109\n" \
    "s_add_u32 s64, s64, 40960\n" \
    "s_cmp_eq_u32 s64, 122880\n" \
    "s_cselect_b32 s64, 0, s64\n" \
    "v_add_u32 v109, s64, %[l16]\n" \
    "s_add_u32 s65, s64, 40960\n" \
    "s_cmp_eq_u32 s65, 122880\n" \
    "s_cselect_b32 s65, 0, s65\n" \
    "s_add_u32 s65, s65, s70\n" \
    "s_min_u32 s63, s60, 61\n" \
    "s_mul_i32 s63, s63, 40960\n" \
    "s_add_u32 s63, s63, s70\n" \
    "s_add_u32 s63, s63, 250691584\n" \
    "s_waitcnt lgkmcnt(3)\n" \
    "v_mfma_f32_16x16x32_bf16 v[48:51], a[0:3], v[16:19], 0\n" \
    "ds_read_b128 v[32:35], v108 offset:2048\n" \
    "v_mfma_f32_16x16x32_bf16 v[52:55], a[80:83], v[16:19], 0\n" \
    "ds_read_b128 v[36:39], v108 offset:22528\n" \
    "v_mfma_f32_16x16x32_bf16 v[56:59], a[160:163], v[16:19], 0\n" \
    "s_waitcnt lgkmcnt(4)\n" \
    "v_mfma_f32_16x16x32_bf16 v[60:63], a[0:3], v[20:23], 0\n" \
    "v_mfma_f32_16x16x32_bf16 v[64:67], a[80:83], v[20:23], 0\n" \
    "v_mfma_f32_16x16x32_bf16 v[68:71], a[160:163], v[20:23], 0\n" \
    "s_waitcnt lgkmcnt(3)\n" \
    "v_mfma_f32_16x16x32_bf16 v[48:51], a[4:7], v[24:27], v[48:51]\n" \
    "ds_read_b128 v[40:43], v108 offset:3072\n" \
    "v_mfma_f32_16x16x32_bf16 v[52:55], a[84:87], v[24:27], v[52:55]\n" \
    "ds_read_b128 v[44:47], v108 offset:23552\n" \
    "v_mfma_f32_16x16x32_bf16 v[56:59], a[164:167], v[24:27], v[56:59]\n" \
    "v_cvt_pk_bf16_f32 v96, v72, v73\n" \
    "s_waitcnt lgkmcnt(4)\n" \
    "v_mfma_f32_16x16x32_bf16 v[60:63], a[4:7], v[28:31], v[60:63]\n" \
    "v_cvt_pk_bf16_f32 v97, v74, v75\n" \
    "v_mfma_f32_16x16x32_bf16 v[64:67], a[84:87], v[28:31], v[64:67]\n" \
    "v_cvt_pk_bf16_f32 v98, v84, v85\n" \
    "v_mfma_f32_16x16x32_bf16 v[68:71], a[164:167], v[28:31], v[68:71]\n" \
    "v_cvt_pk_bf16_f32 v99, v86, v87\n" \
    "s_waitcnt lgkmcnt(3)\n" \
    "v_mfma_f32_16x16x32_bf16 v[48:51], a[8:11], v[32:35], v[48:51]\n" \
    "ds_read_b128 v[16:19], v108 offset:4096\n" \
    "v_mfma_f32_16x16x32_bf16 v[52:55], a[88:91], v[32:35], v[52:55]\n" \
    "ds_read_b128 v[20:23], v108 offset:24576\n" \
    "v_mfma_f32_16x16x32_bf16 v[56:59], a[168:171], v[32:35], v[56:59]\n" \
    "buffer_store_dwordx4 v[96:99], %[l16], %[ws], s62 offen sc1\n" \
    "s_waitcnt lgkmcnt(4)\n" \
    "v_mfma_f32_16x16x32_bf16 v[60:63], a[8:11], v[36:39], v[60:63]\n" \
    "v_cvt_pk_bf16_f32 v100, v76, v77\n" \
    "v_mfma_f32_16x16x32_bf16 v[64:67], a[88:91], v[36:39], v[64:67]\n" \
    "v_cvt_pk_bf16_f32 v101, v78, v79\n" \
    "v_mfma_f32_16x16x32_bf16 v[68:71], a[168:171], v[36:39], v[68:71]\n" \
    "v_cvt_pk_bf16_f32 v102, v88, v89\n" \
    "s_waitcnt lgkmcnt(3)\n" \
    "v_mfma_f32_16x16x32_bf16 v[48:51], a[12:15], v[40:43], v[48:51]\n" \
    "ds_read_b128 v[24:27], v108 offset:5120\n" \
    "v_mfma_f32_16x16x32_bf16 v[52:55], a[92:95], v[40:43], v[52:55]\n" \
    "ds_read_b128 v[28:31], v108 offset:25600\n" \
    "v_mfma_f32_16x16x32_bf16 v[56:59], a[172:175], v[40:43], v[56:59]\n" \
    "v_cvt_pk_bf16_f32 v103, v90, v91\n" \
    "s_waitcnt lgkmcnt(4)\n" \
    "v_mfma_f32_16x16x32_bf16 v[60:63], a[12:15], v[44:47], v[60:63]\n" \
    "buffer_store_dwordx4 v[100:103], %[l16], %[ws], s62 offen offset:1024 sc1\n" \
    "v_mfma_f32_16x16x32_bf16 v[64:67], a[92:95], v[44:47], v[64:67]\n" \
    "v_cvt_pk_bf16_f32 v104, v80, v81\n" \
    "v_mfma_f32_16x16x32_bf16 v[68:71], a[172:175], v[44:47], v[68:71]\n" \
    "v_cvt_pk_bf16_f32 v105, v82, v83\n" \
    "s_waitcnt lgkmcnt(3)\n" \
    "v_mfma_f32_16x16x32_bf16 v[48:51], a[16:19], v[16:19], v[48:51]\n" \
    "ds_read_b128 v[32:35], v108 offset:6144\n" \
    "v_mfma_f32_16x16x32_bf16 v[52:55], a[96:99], v[16:19], v[52:55]\n" \
    "ds_read_b128 v[36:39], v108 offset:26624\n" \
    "v_mfma_f32_16x16x32_bf16 v[56:59], a[176:179], v[16:19], v[56:59]\n" \
    "v_cvt_pk_bf16_f32 v106, v92, v93\n" \
    "s_waitcnt lgkmcnt(4)\n" \
    "v_mfma_f32_16x16x32_bf16 v[60:63], a[16:19], v[20:23], v[60:63]\n" \
    "v_cvt_pk_bf16_f32 v107, v94, v95\n" \
    "v_mfma_f32_16x16x32_bf16 v[64:67], a[96:99], v[20:23], v[64:67]\n" \
    "buffer_store_dwordx4 v[104:107], %[l16], %[ws], s62 offen offset:2048 sc1\n" \
    "v_mfma_f32_16x16x32_bf16 v[68:71], a[176:179], v[20:23], v[68:71]\n" \
    "s_waitcnt lgkmcnt(3)\n" \
    "v_mfma_f32_16x16x32_bf16 v[48:51], a[20:23], v[24:27], v[48:51]\n" \
    "ds_read_b128 v[40:43], v108 offset:7168\n" \
    "v_mfma_f32_16x16x32_bf16 v[52:55], a[100:103], v[24:27], v[52:55]\n" \
    "ds_read_b128 v[44:47], v108 offset:27648\n" \
    "v_mfma_f32_16x16x32_bf16 v[56:59], a[180:183], v[24:27], v[56:59]\n" \
    "s_waitcnt lgkmcnt(4)\n" \
    "v_mfma_f32_16x16x32_bf16 v[60:63], a[20:23], v[28:31], v[60:63]\n" \
    "v_mfma_f32_16x16x32_bf16 v[64:67], a[100:103], v[28:31], v[64:67]\n" \
    "v_mfma_f32_16x16x32_bf16 v[68:71], a[180:183], v[28:31], v[68:71]\n" \
    "s_waitcnt lgkmcnt(3)\n" \
    "v_mfma_f32_16x16x32_bf16 v[48:51], a[24:27], v[32:35], v[48:51]\n" \
    "ds_read_b128 v[16:19], v108 offset:8192\n" \
    "v_mfma_f32_16x16x32_bf16 v[52:55], a[104:107], v[32:35], v[52:55]\n" \
    "ds_read_b128 v[20:23], v108 offset:28672\n" \
    "v_mfma_f32_16x16x32_bf16 v[56:59], a[184:187], v[32:35], v[56:59]\n" \
    "s_waitcnt lgkmcnt(4)\n" \
    "v_mfma_f32_16x16x32_bf16 v[60:63], a[24:27], v[36:39], v[60:63]\n" \
    "v_mfma_f32_16x16x32_bf16 v[64:67], a[104:107], v[36:39], v[64:67]\n" \
    "v_mfma_f32_16x16x32_bf16 v[68:71], a[184:187], v[36:39], v[68:71]\n" \
    "s_waitcnt lgkmcnt(3)\n" \
    "v_mfma_f32_16x16x32_bf16 v[48:51], a[28:31], v[40:43], v[48:51]\n" \
    "ds_read_b128 v[24:27], v108 offset:9216\n" \
    "v_mfma_f32_16x16x32_bf16 v[52:55], a[108:111], v[40:43], v[52:55]\n" \
    "ds_read_b128 v[28:31], v108 offset:29696\n" \
    "v_mfma_f32_16x16x32_bf16 v[56:59], a[188:191], v[40:43], v[56:59]\n" \
    "s_waitcnt lgkmcnt(4)\n" \
    "v_mfma_f32_16x16x32_bf16 v[60:63], a[28:31], v[44:47], v[60:63]\n" \
    "v_mfma_f32_16x16x32_bf16 v[64:67], a[108:111], v[44:47], v[64:67]\n" \
    "v_mfma_f32_16x16x32_bf16 v[68:71], a[188:191], v[44:47], v[68:71]\n" \
    "s_waitcnt lgkmcnt(3)\n" \
    "v_mfma_f32_16x16x32_bf16 v[48:51], a[32:35], v[16:19], v[48:51]\n" \
    "ds_read_b128 v[32:35], v108 offset:10240\n" \
    "v_mfma_f32_16x16x32_bf16 v[52:55], a[112:115], v[16:19], v[52:55]\n" \
    "ds_read_b128 v[36:39], v108 offset:30720\n" \
    "v_mfma_f32_16x16x32_bf16 v[56:59], a[192:195], v[16:19], v[56:59]\n" \
    "s_waitcnt lgkmcnt(4)\n" \
    "v_mfma_f32_16x16x32_bf16 v[60:63], a[32:35], v[20:23], v[60:63]\n" \
    "v_mfma_f32_16x16x32_bf16 v[64:67], a[112:115], v[20:23], v[64:67]\n" \
    "v_mfma_f32_16x16x32_bf16 v[68:71], a[192:195], v[20:23], v[68:71]\n" \
    "s_waitcnt lgkmcnt(3)\n" \
    "v_mfma_f32_16x16x32_bf16 v[48:51], a[36:39], v[24:27], v[48:51]\n" \
    "ds_read_b128 v[40:43], v108 offset:11264\n" \
    "v_mfma_f32_16x16x32_bf16 v[52:55], a[116:119], v[24:27], v[52:55]\n" \
    "ds_read_b128 v[44:47], v108 offset:31744\n" \
    "v_mfma_f32_16x16x32_bf16 v[56:59], a[196:199], v[24:27], v[56:59]\n" \
    "s_waitcnt lgkmcnt(4)\n" \
    "v_mfma_f32_16x16x32_bf16 v[60:63], a[36:39], v[28:31], v[60:63]\n" \
    "v_mfma_f32_16x16x32_bf16 v[64:67], a[116:119], v[28:31], v[64:67]\n" \
    "v_mfma_f32_16x16x32_bf16 v[68:71], a[196:199], v[28:31], v[68:71]\n" \
    "s_waitcnt lgkmcnt(3)\n" \
    "v_mfma_f32_16x16x32_bf16 v[48:51], a[40:43], v[32:35], v[48:51]\n" \
    "ds_read_b128 v[16:19], v108 offset:12288\n" \
    "v_mfma_f32_16x16x32_bf16 v[52:55], a[120:123], v[32:35], v[52:55]\n" \
    "ds_read_b128 v[20:23], v108 offset:32768\n" \
    "v_mfma_f32_16x16x32_bf16 v[56:59], a[200:203], v[32:35], v[56:59]\n" \
    "s_waitcnt lgkmcnt(4)\n" \
    "v_mfma_f32_16x16x32_bf16 v[60:63], a[40:43], v[36:39], v[60:63]\n" \
    "v_mfma_f32_16x16x32_bf16 v[64:67], a[120:123], v[36:39], v[64:67]\n" \
    "v_mfma_f32_16x16x32_bf16 v[68:71], a[200:203], v[36:39], v[68:71]\n" \
    "s_waitcnt lgkmcnt(3)\n" \
    "v_mfma_f32_16x16x32_bf16 v[48:51], a[44:47], v[40:43], v[48:51]\n" \
    "ds_read_b128 v[24:27], v108 offset:13312\n" \
    "v_mfma_f32_16x16x32_bf16 v[52:55], a[124:127], v[40:43], v[52:55]\n" \
    "ds_read_b128 v[28:31], v108 offset:33792\n" \
    "v_mfma_f32_16x16x32_bf16 v[56:59], a[204:207], v[40:43], v[56:59]\n" \
    "s_waitcnt lgkmcnt(4)\n" \
    "v_mfma_f32_16x16x32_bf16 v[60:63], a[44:47], v[44:47], v[60:63]\n" \
    "v_mfma_f32_16x16x32_bf16 v[64:67], a[124:127], v[44:47], v[64:67]\n" \
    "v_mfma_f32_16x16x32_bf16 v[68:71], a[204:207], v[44:47], v[68:71]\n" \
    "s_waitcnt vmcnt(0)\n" \
    "s_barrier\n" \
    "s_sub_u32 s73, s60, 0\n" \
    "s_cmp_lg_u32 %[wave], 0\n" \
    "s_cbranch_scc1 Lp_pubc\n" \
    "v_mov_b32 v110, s73\n" \
    "buffer_store_dword v110, %[one], %[ws], %[flag] offen sc1\n" \
    "Lp_pubc:\n" \
    "s_waitcnt lgkmcnt(3)\n" \
    "v_mfma_f32_16x16x32_bf16 v[48:51], a[48:51], v[16:19], v[48:51]\n" \
    "ds_read_b128 v[32:35], v108 offset:14336\n" \
    "v_mfma_f32_16x16x32_bf16 v[52:55], a[128:131], v[16:19], v[52:55]\n" \
    "ds_read_b128 v[36:39], v108 offset:34816\n" \
    "v_mfma_f32_16x16x32_bf16 v[56:59], a[208:211], v[16:19], v[56:59]\n" \
    "s_add_u32 m0, s65, 0\n" \
    "s_nop 0\n" \
    "buffer_load_dwordx4 %[l16], %[ws], s63 offen lds\n" \
    "s_waitcnt lgkmcnt(4)\n" \
    "v_mfma_f32_16x16x32_bf16 v[60:63], a[48:51], v[20:23], v[60:63]\n" \
    "v_mfma_f32_16x16x32_bf16 v[64:67], a[128:131], v[20:23], v[64:67]\n" \
    "buffer_load_dwordx4 %[l16], %[ws], s63 offen offset:1024 lds\n" \
    "v_mfma_f32_16x16x32_bf16 v[68:71], a[208:211], v[20:23], v[68:71]\n" \
    "s_waitcnt lgkmcnt(3)\n" \
    "v_mfma_f32_16x16x32_bf16 v[48:51], a[52:55], v[24:27], v[48:51]\n" \
    "ds_read_b128 v[40:43], v108 offset:15360\n" \
    "v_mfma_f32_16x16x32_bf16 v[52:55], a[132:135], v[24:27], v[52:55]\n" \
    "ds_read_b128 v[44:47], v108 offset:35840\n" \
    "v_mfma_f32_16x16x32_bf16 v[56:59], a[212:215], v[24:27], v[56:59]\n" \
    "buffer_load_dwordx4 %[l16], %[ws], s63 offen offset:2048 lds\n" \
    "s_waitcnt lgkmcnt(4)\n" \
    "v_mfma_f32_16x16x32_bf16 v[60:63], a[52:55], v[28:31], v[60:63]\n" \
    "v_mfma_f32_16x16x32_bf16 v[64:67], a[132:135], v[28:31], v[64:67]\n" \
    "buffer_load_dwordx4 %[l16], %[ws], s63 offen offset:3072 lds\n" \
    "v_mfma_f32_16x16x32_bf16 v[68:71], a[212:215], v[28:31], v[68:71]\n" \
    "s_waitcnt lgkmcnt(3)\n" \
    "v_mfma_f32_16x16x32_bf16 v[48:51], a[56:59], v[32:35], v[48:51]\n" \
    "ds_read_b128 v[16:19], v108 offset:16384\n" \
    "v_mfma_f32_16x16x32_bf16 v[52:55], a[136:139], v[32:35], v[52:55]\n" \
    "ds_read_b128 v[20:23], v108 offset:36864\n" \
    "v_mfma_f32_16x16x32_bf16 v[56:59], a[216:219], v[32:35], v[56:59]\n" \
    "s_add_u32 m0, s65, 4096\n" \
    "s_add_u32 s63, s63, 4096\n" \
    "buffer_load_dwordx4 %[l16], %[ws], s63 offen lds\n" \
    "s_waitcnt lgkmcnt(4)\n" \
    "v_mfma_f32_16x16x32_bf16 v[60:63], a[56:59], v[36:39], v[60:63]\n" \
    "v_mfma_f32_16x16x32_bf16 v[64:67], a[136:139], v[36:39], v[64:67]\n" \
    "buffer_load_dwordx4 %[l16], %[ws], s63 offen offset:1024 lds\n" \
    "v_mfma_f32_16x16x32_bf16 v[68:71], a[216:219], v[36:39], v[68:71]\n" \
    "s_waitcnt lgkmcnt(3)\n" \
    "v_mfma_f32_16x16x32_bf16 v[48:51], a[60:63], v[40:43], v[48:51]\n" \
    "ds_read_b128 v[24:27], v108 offset:17408\n" \
    "v_mfma_f32_16x16x32_bf16 v[52:55], a[140:143], v[40:43], v[52:55]\n" \
    "ds_read_b128 v[28:31], v108 offset:37888\n" \
    "v_mfma_f32_16x16x32_bf16 v[56:59], a[220:223], v[40:43], v[56:59]\n" \
    "buffer_load_dwordx4 %[l16], %[ws], s63 offen offset:2048 lds\n" \
    "s_waitcnt lgkmcnt(4)\n" \
    "v_mfma_f32_16x16x32_bf16 v[60:63], a[60:63], v[44:47], v[60:63]\n" \
    "v_mfma_f32_16x16x32_bf16 v[64:67], a[140:143], v[44:47], v[64:67]\n" \
    "buffer_load_dwordx4 %[l16], %[ws], s63 offen offset:3072 lds\n" \
    "v_mfma_f32_16x16x32_bf16 v[68:71], a[220:223], v[44:47], v[68:71]\n" \
    "s_waitcnt lgkmcnt(3)\n" \
    "v_mfma_f32_16x16x32_bf16 v[48:51], a[64:67], v[16:19], v[48:51]\n" \
    "ds_read_b128 v[32:35], v108 offset:18432\n" \
    "v_mfma_f32_16x16x32_bf16 v[52:55], a[144:147], v[16:19], v[52:55]\n" \
    "ds_read_b128 v[36:39], v108 offset:38912\n" \
    "v_mfma_f32_16x16x32_bf16 v[56:59], a[224:227], v[16:19], v[56:59]\n" \
    "s_add_u32 m0, s65, 8192\n" \
    "s_add_u32 s63, s63, 4096\n" \
    "buffer_load_dwordx4 %[l16], %[ws], s63 offen lds\n" \
    "s_waitcnt lgkmcnt(4)\n" \
    "v_mfma_f32_16x16x32_bf16 v[60:63], a[64:67], v[20:23], v[60:63]\n" \
    "v_mfma_f32_16x16x32_bf16 v[64:67], a[144:147], v[20:23], v[64:67]\n" \
    "buffer_load_dwordx4 %[l16], %[ws], s63 offen offset:1024 lds\n" \
    "v_mfma_f32_16x16x32_bf16 v[68:71], a[224:227], v[20:23], v[68:71]\n" \
    "s_waitcnt lgkmcnt(3)\n" \
    "v_mfma_f32_16x16x32_bf16 v[48:51], a[68:71], v[24:27], v[48:51]\n" \
    "ds_read_b128 v[40:43], v108 offset:19456\n" \
    "v_mfma_f32_16x16x32_bf16 v[52:55], a[148:151], v[24:27], v[52:55]\n" \
    "ds_read_b128 v[44:47], v108 offset:39936\n" \
    "v_mfma_f32_16x16x32_bf16 v[56:59], a[228:231], v[24:27], v[56:59]\n" \
    "s_waitcnt lgkmcnt(4)\n" \
    "v_mfma_f32_16x16x32_bf16 v[60:63], a[68:71], v[28:31], v[60:63]\n" \
    "v_mfma_f32_16x16x32_bf16 v[64:67], a[148:151], v[28:31], v[64:67]\n" \
    "v_mfma_f32_16x16x32_bf16 v[68:71], a[228:231], v[28:31], v[68:71]\n" \
    "s_waitcnt lgkmcnt(3)\n" \
    "v_mfma_f32_16x16x32_bf16 v[48:51], a[72:75], v[32:35], v[48:51]\n" \
    "ds_read_b128 v[16:19], v109\n" \
    "v_mfma_f32_16x16x32_bf16 v[52:55], a[152:155], v[32:35], v[52:55]\n" \
    "ds_read_b128 v[20:23], v109 offset:20480\n" \
    "v_mfma_f32_16x16x32_bf16 v[56:59], a[232:235], v[32:35], v[56:59]\n" \
    "s_waitcnt lgkmcnt(4)\n" \
    "v_mfma_f32_16x16x32_bf16 v[60:63], a[72:75], v[36:39], v[60:63]\n" \
    "v_mfma_f32_16x16x32_bf16 v[64:67], a[152:155], v[36:39], v[64:67]\n" \
    "v_mfma_f32_16x16x32_bf16 v[68:71], a[232:235], v[36:39], v[68:71]\n" \
    "s_waitcnt lgkmcnt(3)\n" \
    "v_mfma_f32_16x16x32_bf16 v[48:51], a[76:79], v[40:43], v[48:51]\n" \
    "ds_read_b128 v[24:27], v109 offset:1024\n" \
    "v_mfma_f32_16x16x32_bf16 v[52:55], a[156:159], v[40:43], v[52:55]\n" \
    "ds_read_b128 v[28:31], v109 offset:21504\n" \
    "v_mfma_f32_16x16x32_bf16 v[56:59], a[236:239], v[40:43], v[56:59]\n" \
    "s_waitcnt lgkmcnt(4)\n" \
    "v_mfma_f32_16x16x32_bf16 v[60:63], a[76:79], v[44:47], v[60:63]\n" \
    "v_mfma_f32_16x16x32_bf16 v[64:67], a[156:159], v[44:47], v[64:67]\n" \
    "v_mfma_f32_16x16x32_bf16 v[68:71], a[236:239], v[44:47], v[68:71]\n" \
    "s_add_u32 s66, s66, %[ost]\n" \
    "s_lshr_b32 s62, s66, 8\n" \
    "s_xor_b32 s62, s62, %[par]\n" \
    "s_and_b32 s62, s62, %[msk]\n" \
    "s_add_u32 s62, s62, s66\n" \
    "s_add_u32 s60, s60, 1\n" \
    "v_mov_b32 v108, v109\n" \
    "s_add_u32 s64, s64, 40960\n" \
    "s_cmp_eq_u32 s64, 122880\n" \
    "s_cselect_b32 s64, 0, s64\n" \
    "v_add_u32 v109, s64, %[l16]\n" \
    "s_add_u32 s65, s64, 40960\n" \
    "s_cmp_eq_u32 s65, 122880\n" \
    "s_cselect_b32 s65, 0, s65\n" \
    "s_add_u32 s65, s65, s70\n" \
    "s_min_u32 s63, s60, 61\n" \
    "s_mul_i32 s63, s63, 40960\n" \
    "s_add_u32 s63, s63, s70\n" \
    "s_add_u32 s63, s63, 250691584\n" \
    "s_waitcnt lgkmcnt(3)\n" \
    "v_mfma_f32_16x16x32_bf16 v[72:75], a[0:3], v[16:19], 0\n" \
    "ds_read_b128 v[32:35], v108 offset:2048\n" \
    "v_mfma_f32_16x16x32_bf16 v[76:79], a[80:83], v[16:19], 0\n" \
    "ds_read_b128 v[36:39], v108 offset:22528\n" \
    "v_mfma_f32_16x16x32_bf16 v[80:83], a[160:163], v[16:19], 0\n" \
    "s_waitcnt lgkmcnt(4)\n" \
    "v_mfma_f32_16x16x32_bf16 v[84:87], a[0:3], v[20:23], 0\n" \
    "v_mfma_f32_16x16x32_bf16 v[88:91], a[80:83], v[20:23], 0\n" \
    "v_mfma_f32_16x16x32_bf16 v[92:95], a[160:163], v[20:23], 0\n" \
    "s_waitcnt lgkmcnt(3)\n" \
    "v_mfma_f32_16x16x32_bf16 v[72:75], a[4:7], v[24:27], v[72:75]\n" \
    "ds_read_b128 v[40:43], v108 offset:3072\n" \
    "v_mfma_f32_16x16x32_bf16 v[76:79], a[84:87], v[24:27], v[76:79]\n" \
    "ds_read_b128 v[44:47], v108 offset:23552\n" \
    "v_mfma_f32_16x16x32_bf16 v[80:83], a[164:167], v[24:27], v[80:83]\n" \
    "v_cvt_pk_bf16_f32 v96, v48, v49\n" \
    "s_waitcnt lgkmcnt(4)\n" \
    "v_mfma_f32_16x16x32_bf16 v[84:87], a[4:7], v[28:31], v[84:87]\n" \
    "v_cvt_pk_bf16_f32 v97, v50, v51\n" \
    "v_mfma_f32_16x16x32_bf16 v[88:91], a[84:87], v[28:31], v[88:91]\n" \
    "v_cvt_pk_bf16_f32 v98, v60, v61\n" \
    "v_mfma_f32_16x16x32_bf16 v[92:95], a[164:167], v[28:31], v[92:95]\n" \
    "v_cvt_pk_bf16_f32 v99, v62, v63\n" \
    "s_waitcnt lgkmcnt(3)\n" \
    "v_mfma_f32_16x16x32_bf16 v[72:75], a[8:11], v[32:35], v[72:75]\n" \
    "ds_read_b128 v[16:19], v108 offset:4096\n" \
    "v_mfma_f32_16x16x32_bf16 v[76:79], a[88:91], v[32:35], v[76:79]\n" \
    "ds_read_b128 v[20:23], v108 offset:24576\n" \
    "v_mfma_f32_16x16x32_bf16 v[80:83], a[168:171], v[32:35], v[80:83]\n" \
    "buffer_store_dwordx4 v[96:99], %[l16], %[ws], s62 offen sc1\n" \
    "s_waitcnt lgkmcnt(4)\n" \
    "v_mfma_f32_16x16x32_bf16 v[84:87], a[8:11], v[36:39], v[84:87]\n" \
    "v_cvt_pk_bf16_f32 v100, v52, v53\n" \
    "v_mfma_f32_16x16x32_bf16 v[88:91], a[88:91], v[36:39], v[88:91]\n" \
    "v_cvt_pk_bf16_f32 v101, v54, v55\n" \
    "v_mfma_f32_16x16x32_bf16 v[92:95], a[168:171], v[36:39], v[92:95]\n" \
    "v_cvt_pk_bf16_f32 v102, v64, v65\n" \
    "s_waitcnt lgkmcnt(3)\n" \
    "v_mfma_f32_16x16x32_bf16 v[72:75], a[12:15], v[40:43], v[72:75]\n" \
    "ds_read_b128 v[24:27], v108 offset:5120\n" \
    "v_mfma_f32_16x16x32_bf16 v[76:79], a[92:95], v[40:43], v[76:79]\n" \
    "ds_read_b128 v[28:31], v108 offset:25600\n" \
    "v_mfma_f32_16x16x32_bf16 v[80:83], a[172:175], v[40:43], v[80:83]\n" \
    "v_cvt_pk_bf16_f32 v103, v66, v67\n" \
    "s_waitcnt lgkmcnt(4)\n" \
    "v_mfma_f32_16x16x32_bf16 v[84:87], a[12:15], v[44:47], v[84:87]\n" \
    "buffer_store_dwordx4 v[100:103], %[l16], %[ws], s62 offen offset:1024 sc1\n" \
    "v_mfma_f32_16x16x32_bf16 v[88:91], a[92:95], v[44:47], v[88:91]\n" \
    "v_cvt_pk_bf16_f32 v104, v56, v57\n" \
    "v_mfma_f32_16x16x32_bf16 v[92:95], a[172:175], v[44:47], v[92:95]\n" \
    "v_cvt_pk_bf16_f32 v105, v58, v59\n" \
    "s_waitcnt lgkmcnt(3)\n" \
    "v_mfma_f32_16x16x32_bf16 v[72:75], a[16:19], v[16:19], v[72:75]\n" \
    "ds_read_b128 v[32:35], v108 offset:6144\n" \
    "v_mfma_f32_16x16x32_bf16 v[76:79], a[96:99], v[16:19], v[76:79]\n" \
    "ds_read_b128 v[36:39], v108 offset:26624\n" \
    "v_mfma_f32_16x16x32_bf16 v[80:83], a[176:179], v[16:19], v[80:83]\n" \
    "v_cvt_pk_bf16_f32 v106, v68, v69\n" \
    "s_waitcnt lgkmcnt(4)\n" \
    "v_mfma_f32_16x16x32_bf16 v[84:87], a[16:19], v[20:23], v[84:87]\n" \
    "v_cvt_pk_bf16_f32 v107, v70, v71\n" \
    "v_mfma_f32_16x16x32_bf16 v[88:91], a[96:99], v[20:23], v[88:91]\n" \
    "buffer_store_dwordx4 v[104:107], %[l16], %[ws], s62 offen offset:2048 sc1\n" \
    "v_mfma_f32_16x16x32_bf16 v[92:95], a[176:179], v[20:23], v[92:95]\n" \
    "s_waitcnt lgkmcnt(3)\n" \
    "v_mfma_f32_16x16x32_bf16 v[72:75], a[20:23], v[24:27], v[72:75]\n" \
    "ds_read_b128 v[40:43], v108 offset:7168\n" \
    "v_mfma_f32_16x16x32_bf16 v[76:79], a[100:103], v[24:27], v[76:79]\n" \
    "ds_read_b128 v[44:47], v108 offset:27648\n" \
    "v_mfma_f32_16x16x32_bf16 v[80:83], a[180:183], v[24:27], v[80:83]\n" \
    "s_waitcnt lgkmcnt(4)\n" \
    "v_mfma_f32_16x16x32_bf16 v[84:87], a[20:23], v[28:31], v[84:87]\n" \
    "v_mfma_f32_16x16x32_bf16 v[88:91], a[100:103], v[28:31], v[88:91]\n" \
    "v_mfma_f32_16x16x32_bf16 v[92:95], a[180:183], v[28:31], v[92:95]\n" \
    "s_waitcnt lgkmcnt(3)\n" \
    "v_mfma_f32_16x16x32_bf16 v[72:75], a[24:27], v[32:35], v[72:75]\n" \
    "ds_read_b128 v[16:19], v108 offset:8192\n" \
    "v_mfma_f32_16x16x32_bf16 v[76:79], a[104:107], v[32:35], v[76:79]\n" \
    "ds_read_b128 v[20:23], v108 offset:28672\n" \
    "v_mfma_f32_16x16x32_bf16 v[80:83], a[184:187], v[32:35], v[80:83]\n" \
    "s_waitcnt lgkmcnt(4)\n" \
    "v_mfma_f32_16x16x32_bf16 v[84:87], a[24:27], v[36:39], v[84:87]\n" \
    "v_mfma_f32_16x16x32_bf16 v[88:91], a[104:107], v[36:39], v[88:91]\n" \
    "v_mfma_f32_16x16x32_bf16 v[92:95], a[184:187], v[36:39], v[92:95]\n" \
    "s_waitcnt lgkmcnt(3)\n" \
    "v_mfma_f32_16x16x32_bf16 v[72:75], a[28:31], v[40:43], v[72:75]\n" \
    "ds_read_b128 v[24:27], v108 offset:9216\n" \
    "v_mfma_f32_16x16x32_bf16 v[76:79], a[108:111], v[40:43], v[76:79]\n" \
    "ds_read_b128 v[28:31], v108 offset:29696\n" \
    "v_mfma_f32_16x16x32_bf16 v[80:83], a[188:191], v[40:43], v[80:83]\n" \
    "s_waitcnt lgkmcnt(4)\n" \
    "v_mfma_f32_16x16x32_bf16 v[84:87], a[28:31], v[44:47], v[84:87]\n" \
    "v_mfma_f32_16x16x32_bf16 v[88:91], a[108:111], v[44:47], v[88:91]\n" \
    "v_mfma_f32_16x16x32_bf16 v[92:95], a[188:191], v[44:47], v[92:95]\n" \
    "s_waitcnt lgkmcnt(3)\n" \
    "v_mfma_f32_16x16x32_bf16 v[72:75], a[32:35], v[16:19], v[72:75]\n" \
    "ds_read_b128 v[32:35], v108 offset:10240\n" \
    "v_mfma_f32_16x16x32_bf16 v[76:79], a[112:115], v[16:19], v[76:79]\n" \
    "ds_read_b128 v[36:39], v108 offset:30720\n" \
    "v_mfma_f32_16x16x32_bf16 v[80:83], a[192:195], v[16:19], v[80:83]\n" \
    "s_waitcnt lgkmcnt(4)\n" \
    "v_mfma_f32_16x16x32_bf16 v[84:87], a[32:35], v[20:23], v[84:87]\n" \
    "v_mfma_f32_16x16x32_bf16 v[88:91], a[112:115], v[20:23], v[88:91]\n" \
    "v_mfma_f32_16x16x32_bf16 v[92:95], a[192:195], v[20:23], v[92:95]\n" \
    "s_waitcnt lgkmcnt(3)\n" \
    "v_mfma_f32_16x16x32_bf16 v[72:75], a[36:39], v[24:27], v[72:75]\n" \
    "ds_read_b128 v[40:43], v108 offset:11264\n" \
    "v_mfma_f32_16x16x32_bf16 v[76:79], a[116:119], v[24:27], v[76:79]\n" \
    "ds_read_b128 v[44:47], v108 offset:31744\n" \
    "v_mfma_f32_16x16x32_bf16 v[80:83], a[196:199], v[24:27], v[80:83]\n" \
    "s_waitcnt lgkmcnt(4)\n" \
    "v_mfma_f32_16x16x32_bf16 v[84:87], a[36:39], v[28:31], v[84:87]\n" \
    "v_mfma_f32_16x16x32_bf16 v[88:91], a[116:119], v[28:31], v[88:91]\n" \
    "v_mfma_f32_16x16x32_bf16 v[92:95], a[196:199], v[28:31], v[92:95]\n" \
    "s_waitcnt lgkmcnt(3)\n" \
    "v_mfma_f32_16x16x32_bf16 v[72:75], a[40:43], v[32:35], v[72:75]\n" \
    "ds_read_b128 v[16:19], v108 offset:12288\n" \
    "v_mfma_f32_16x16x32_bf16 v[76:79], a[120:123], v[32:35], v[76:79]\n" \
    "ds_read_b128 v[20:23], v108 offset:32768\n" \
    "v_mfma_f32_16x16x32_bf16 v[80:83], a[200:203], v[32:35], v[80:83]\n" \
    "s_waitcnt lgkmcnt(4)\n" \
    "v_mfma_f32_16x16x32_bf16 v[84:87], a[40:43], v[36:39], v[84:87]\n" \
    "v_mfma_f32_16x16x32_bf16 v[88:91], a[120:123], v[36:39], v[88:91]\n" \
    "v_mfma_f32_16x16x32_bf16 v[92:95], a[200:203], v[36:39], v[92:95]\n" \
    "s_waitcnt lgkmcnt(3)\n" \
    "v_mfma_f32_16x16x32_bf16 v[72:75], a[44:47], v[40:43], v[72:75]\n" \
    "ds_read_b128 v[24:27], v108 offset:13312\n" \
    "v_mfma_f32_16x16x32_bf16 v[76:79], a[124:127], v[40:43], v[76:79]\n" \
    "ds_read_b128 v[28:31], v108 offset:33792\n" \
    "v_mfma_f32_16x16x32_bf16 v[80:83], a[204:207], v[40:43], v[80:83]\n" \
    "s_waitcnt lgkmcnt(4)\n" \
    "v_mfma_f32_16x16x32_bf16 v[84:87], a[44:47], v[44:47], v[84:87]\n" \
    "v_mfma_f32_16x16x32_bf16 v[88:91], a[124:127], v[44:47], v[88:91]\n" \
    "v_mfma_f32_16x16x32_bf16 v[92:95], a[204:207], v[44:47], v[92:95]\n" \
    "s_waitcnt vmcnt(0)\n" \
    "s_barrier\n" \
    "s_sub_u32 s73, s60, 0\n" \
    "s_cmp_lg_u32 %[wave], 0\n" \
    "s_cbranch_scc1 Lp_pubd\n" \
    "v_mov_b32 v110, s73\n" \
    "buffer_store_dword v110, %[one], %[ws], %[flag] offen sc1\n" \
    "Lp_pubd:\n" \
    "s_waitcnt lgkmcnt(3)\n" \
    "v_mfma_f32_16x16x32_bf16 v[72:75], a[48:51], v[16:19], v[72:75]\n" \
    "ds_read_b128 v[32:35], v108 offset:14336\n" \
    "v_mfma_f32_16x16x32_bf16 v[76:79], a[128:131], v[16:19], v[76:79]\n" \
    "ds_read_b128 v[36:39], v108 offset:34816\n" \
    "v_mfma_f32_16x16x32_bf16 v[80:83], a[208:211], v[16:19], v[80:83]\n" \
    "s_add_u32 m0, s65, 0\n" \
    "s_nop 0\n" \
    "buffer_load_dwordx4 %[l16], %[ws], s63 offen lds\n" \
    "s_waitcnt lgkmcnt(4)\n" \
    "v_mfma_f32_16x16x32_bf16 v[84:87], a[48:51], v[20:23], v[84:87]\n" \
    "v_mfma_f32_16x16x32_bf16 v[88:91], a[128:131], v[20:23], v[88:91]\n" \
    "buffer_load_dwordx4 %[l16], %[ws], s63 offen offset:1024 lds\n" \
    "v_mfma_f32_16x16x32_bf16 v[92:95], a[208:211], v[20:23], v[92:95]\n" \
    "s_waitcnt lgkmcnt(3)\n" \
    "v_mfma_f32_16x16x32_bf16 v[72:75], a[52:55], v[24:27], v[72:75]\n" \
    "ds_read_b128 v[40:43], v108 offset:15360\n" \
    "v_mfma_f32_16x16x32_bf16 v[76:79], a[132:135], v[24:27], v[76:79]\n" \
    "ds_read_b128 v[44:47], v108 offset:35840\n" \
    "v_mfma_f32_16x16x32_bf16 v[80:83], a[212:215], v[24:27], v[80:83]\n" \
    "buffer_load_dwordx4 %[l16], %[ws], s63 offen offset:2048 lds\n" \
    "s_waitcnt lgkmcnt(4)\n" \
    "v_mfma_f32_16x16x32_bf16 v[84:87], a[52:55], v[28:31], v[84:87]\n" \
    "v_mfma_f32_16x16x32_bf16 v[88:91], a[132:135], v[28:31], v[88:91]\n" \
    "buffer_load_dwordx4 %[l16], %[ws], s63 offen offset:3072 lds\n" \
    "v_mfma_f32_16x16x32_bf16 v[92:95], a[212:215], v[28:31], v[92:95]\n" \
    "s_waitcnt lgkmcnt(3)\n" \
    "v_mfma_f32_16x16x32_bf16 v[72:75], a[56:59], v[32:35], v[72:75]\n" \
    "ds_read_b128 v[16:19], v108 offset:16384\n" \
    "v_mfma_f32_16x16x32_bf16 v[76:79], a[136:139], v[32:35], v[76:79]\n" \
    "ds_read_b128 v[20:23], v108 offset:36864\n" \
    "v_mfma_f32_16x16x32_bf16 v[80:83], a[216:219], v[32:35], v[80:83]\n" \
    "s_add_u32 m0, s65, 4096\n" \
    "s_add_u32 s63, s63, 4096\n" \
    "buffer_load_dwordx4 %[l16], %[ws], s63 offen lds\n" \
    "s_waitcnt lgkmcnt(4)\n" \
    "v_mfma_f32_16x16x32_bf16 v[84:87], a[56:59], v[36:39], v[84:87]\n" \
    "v_mfma_f32_16x16x32_bf16 v[88:91], a[136:139], v[36:39], v[88:91]\n" \
    "buffer_load_dwordx4 %[l16], %[ws], s63 offen offset:1024 lds\n" \
    "v_mfma_f32_16x16x32_bf16 v[92:95], a[216:219], v[36:39], v[92:95]\n" \
    "s_waitcnt lgkmcnt(3)\n" \
    "v_mfma_f32_16x16x32_bf16 v[72:75], a[60:63], v[40:43], v[72:75]\n" \
    "ds_read_b128 v[24:27], v108 offset:17408\n" \
    "v_mfma_f32_16x16x32_bf16 v[76:79], a[140:143], v[40:43], v[76:79]\n" \
    "ds_read_b128 v[28:31], v108 offset:37888\n" \
    "v_mfma_f32_16x16x32_bf16 v[80:83], a[220:223], v[40:43], v[80:83]\n" \
    "buffer_load_dwordx4 %[l16], %[ws], s63 offen offset:2048 lds\n" \
    "s_waitcnt lgkmcnt(4)\n" \
    "v_mfma_f32_16x16x32_bf16 v[84:87], a[60:63], v[44:47], v[84:87]\n" \
    "v_mfma_f32_16x16x32_bf16 v[88:91], a[140:143], v[44:47], v[88:91]\n" \
    "buffer_load_dwordx4 %[l16], %[ws], s63 offen offset:3072 lds\n" \
    "v_mfma_f32_16x16x32_bf16 v[92:95], a[220:223], v[44:47], v[92:95]\n" \
    "s_waitcnt lgkmcnt(3)\n" \
    "v_mfma_f32_16x16x32_bf16 v[72:75], a[64:67], v[16:19], v[72:75]\n" \
    "ds_read_b128 v[32:35], v108 offset:18432\n" \
    "v_mfma_f32_16x16x32_bf16 v[76:79], a[144:147], v[16:19], v[76:79]\n" \
    "ds_read_b128 v[36:39], v108 offset:38912\n" \
    "v_mfma_f32_16x16x32_bf16 v[80:83], a[224:227], v[16:19], v[80:83]\n" \
    "s_add_u32 m0, s65, 8192\n" \
    "s_add_u32 s63, s63, 4096\n" \
    "buffer_load_dwordx4 %[l16], %[ws], s63 offen lds\n" \
    "s_waitcnt lgkmcnt(4)\n" \
    "v_mfma_f32_16x16x32_bf16 v[84:87], a[64:67], v[20:23], v[84:87]\n" \
    "v_mfma_f32_16x16x32_bf16 v[88:91], a[144:147], v[20:23], v[88:91]\n" \
    "buffer_load_dwordx4 %[l16], %[ws], s63 offen offset:1024 lds\n" \
    "v_mfma_f32_16x16x32_bf16 v[92:95], a[224:227], v[20:23], v[92:95]\n" \
    "s_waitcnt lgkmcnt(3)\n" \
    "v_mfma_f32_16x16x32_bf16 v[72:75], a[68:71], v[24:27], v[72:75]\n" \
    "ds_read_b128 v[40:43], v108 offset:19456\n" \
    "v_mfma_f32_16x16x32_bf16 v[76:79], a[148:151], v[24:27], v[76:79]\n" \
    "ds_read_b128 v[44:47], v108 offset:39936\n" \
    "v_mfma_f32_16x16x32_bf16 v[80:83], a[228:231], v[24:27], v[80:83]\n" \
    "s_waitcnt lgkmcnt(4)\n" \
    "v_mfma_f32_16x16x32_bf16 v[84:87], a[68:71], v[28:31], v[84:87]\n" \
    "v_mfma_f32_16x16x32_bf16 v[88:91], a[148:151], v[28:31], v[88:91]\n" \
    "v_mfma_f32_16x16x32_bf16 v[92:95], a[228:231], v[28:31], v[92:95]\n" \
    "s_waitcnt lgkmcnt(3)\n" \
    "v_mfma_f32_16x16x32_bf16 v[72:75], a[72:75], v[32:35], v[72:75]\n" \
    "ds_read_b128 v[16:19], v109\n" \
    "v_mfma_f32_16x16x32_bf16 v[76:79], a[152:155], v[32:35], v[76:79]\n" \
    "ds_read_b128 v[20:23], v109 offset:20480\n" \
    "v_mfma_f32_16x16x32_bf16 v[80:83], a[232:235], v[32:35], v[80:83]\n" \
    "s_waitcnt lgkmcnt(4)\n" \
    "v_mfma_f32_16x16x32_bf16 v[84:87], a[72:75], v[36:39], v[84:87]\n" \
    "v_mfma_f32_16x16x32_bf16 v[88:91], a[152:155], v[36:39], v[88:91]\n" \
    "v_mfma_f32_16x16x32_bf16 v[92:95], a[232:235], v[36:39], v[92:95]\n" \
    "s_waitcnt lgkmcnt(3)\n" \
    "v_mfma_f32_16x16x32_bf16 v[72:75], a[76:79], v[40:43], v[72:75]\n" \
    "ds_read_b128 v[24:27], v109 offset:1024\n" \
    "v_mfma_f32_16x16x32_bf16 v[76:79], a[156:159], v[40:43], v[76:79]\n" \
    "ds_read_b128 v[28:31], v109 offset:21504\n" \
    "v_mfma_f32_16x16x32_bf16 v[80:83], a[236:239], v[40:43], v[80:83]\n" \
    "s_waitcnt lgkmcnt(4)\n" \
    "v_mfma_f32_16x16x32_bf16 v[84:87], a[76:79], v[44:47], v[84:87]\n" \
    "v_mfma_f32_16x16x32_bf16 v[88:91], a[156:159], v[44:47], v[88:91]\n" \
    "v_mfma_f32_16x16x32_bf16 v[92:95], a[236:239], v[44:47], v[92:95]\n" \
    "s_add_u32 s66, s66, %[ost]\n" \
    "s_lshr_b32 s62, s66, 8\n" \
    "s_xor_b32 s62, s62, %[par]\n" \
    "s_and_b32 s62, s62, %[msk]\n" \
    "s_add_u32 s62, s62, s66\n" \
    "s_add_u32 s60, s60, 1\n" \
    "s_cmp_lt_u32 s60, 64\n" \
    "s_cbranch_scc1 Lp_loop\n" \
    "v_cvt_pk_bf16_f32 v96, v72, v73\n" \
    "v_cvt_pk_bf16_f32 v97, v74, v75\n" \
    "v_cvt_pk_bf16_f32 v98, v84, v85\n" \
    "v_cvt_pk_bf16_f32 v99, v86, v87\n" \
    "buffer_store_dwordx4 v[96:99], %[l16], %[ws], s62 offen sc1\n" \
    "v_cvt_pk_bf16_f32 v100, v76, v77\n" \
    "v_cvt_pk_bf16_f32 v101, v78, v79\n" \
    "v_cvt_pk_bf16_f32 v102, v88, v89\n" \
    "v_cvt_pk_bf16_f32 v103, v90, v91\n" \
    "buffer_store_dwordx4 v[100:103], %[l16], %[ws], s62 offen offset:1024 sc1\n" \
    "v_cvt_pk_bf16_f32 v104, v80, v81\n" \
    "v_cvt_pk_bf16_f32 v105, v82, v83\n" \
    "v_cvt_pk_bf16_f32 v106, v92, v93\n" \
    "v_cvt_pk_bf16_f32 v107, v94, v95\n" \
    "buffer_store_dwordx4 v[104:107], %[l16], %[ws], s62 offen offset:2048 sc1\n" \
    "s_waitcnt vmcnt(0)\n" \
    "s_barrier\n" \
    "s_cmp_lg_u32 %[wave], 0\n" \
    "s_cbranch_scc1 Lp_fle\n" \
    "v_mov_b32 v110, s60\n" \
    "buffer_store_dword v110, %[one], %[ws], %[flag] offen sc1\n" \
    "Lp_fle:\n" \
    "s_add_u32 s66, s66, %[ost]\n" \
    "s_lshr_b32 s62, s66, 8\n" \
    "s_xor_b32 s62, s62, %[par]\n" \
    "s_and_b32 s62, s62, %[msk]\n" \
    "s_add_u32 s62, s62, s66\n" \
    "s_waitcnt lgkmcnt(0)\n" \
    "s_waitcnt vmcnt(0)\n"
#define ASM_O \
    "ds_read_b128 v[112:115], %[tab]\n" \
    "ds_read_b128 v[116:119], %[tab] offset:1024\n" \
    "ds_read_b128 v[120:123], %[tab] offset:2048\n" \
    "ds_read_b128 v[128:131], %[tab] offset:3072\n" \
    "ds_read_b128 v[132:135], %[tab] offset:4096\n" \
    "ds_read_b128 v[136:139], %[tab] offset:5120\n" \
    "s_waitcnt lgkmcnt(0)\n" \
    "s_barrier\n" \
    "v_add_u32 v140, 81920, v128\n" \
    "v_add_u32 v141, 81920, v129\n" \
    "v_add_u32 v142, 81920, v130\n" \
    "v_add_u32 v143, 81920, v131\n" \
    "v_add_u32 v144, 81920, v132\n" \
    "v_add_u32 v145, 81920, v133\n" \
    "v_add_u32 v146, 81920, v134\n" \
    "v_add_u32 v147, 81920, v135\n" \
    "v_add_u32 v148, 81920, v136\n" \
    "v_add_u32 v149, 81920, v137\n" \
    "s_mul_i32 s70, %[wave], 10240\n" \
    "s_mov_b32 s61, 0\n" \
    "s_add_u32 m0, s70, 0\n" \
    "s_nop 0\n" \
    "buffer_load_dwordx4 v112, %[w0], s61 offen lds\n" \
    "s_add_u32 m0, s70, 1024\n" \
    "s_nop 0\n" \
    "buffer_load_dwordx4 v113, %[w0], s61 offen lds\n" \
    "s_add_u32 m0, s70, 2048\n" \
    "s_nop 0\n" \
    "buffer_load_dwordx4 v114, %[w0], s61 offen lds\n" \
    "s_add_u32 m0, s70, 3072\n" \
    "s_nop 0\n" \
    "buffer_load_dwordx4 v115, %[w0], s61 offen lds\n" \
    "s_add_u32 m0, s70, 4096\n" \
    "s_nop 0\n" \
    "buffer_load_dwordx4 v116, %[w0], s61 offen lds\n" \
    "s_add_u32 m0, s70, 5120\n" \
    "s_nop 0\n" \
    "buffer_load_dwordx4 v117, %[w0], s61 offen lds\n" \
    "s_add_u32 m0, s70, 6144\n" \
    "s_nop 0\n" \
    "buffer_load_dwordx4 v118, %[w0], s61 offen lds\n" \
    "s_add_u32 m0, s70, 7168\n" \
    "s_nop 0\n" \
    "buffer_load_dwordx4 v119, %[w0], s61 offen lds\n" \
    "s_add_u32 m0, s70, 8192\n" \
    "s_nop 0\n" \
    "buffer_load_dwordx4 v120, %[w0], s61 offen lds\n" \
    "s_add_u32 m0, s70, 9216\n" \
    "s_nop 0\n" \
    "buffer_load_dwordx4 v121, %[w0], s61 offen lds\n" \
    "s_mov_b32 s61, 40960\n" \
    "s_add_u32 m0, s70, 40960\n" \
    "s_nop 0\n" \
    "buffer_load_dwordx4 v112, %[w0], s61 offen lds\n" \
    "s_add_u32 m0, s70, 41984\n" \
    "s_nop 0\n" \
    "buffer_load_dwordx4 v113, %[w0], s61 offen lds\n" \
    "s_add_u32 m0, s70, 43008\n" \
    "s_nop 0\n" \
    "buffer_load_dwordx4 v114, %[w0], s61 offen lds\n" \
    "s_add_u32 m0, s70, 44032\n" \
    "s_nop 0\n" \
    "buffer_load_dwordx4 v115, %[w0], s61 offen lds\n" \
    "s_add_u32 m0, s70, 45056\n" \
    "s_nop 0\n" \
    "buffer_load_dwordx4 v116, %[w0], s61 offen lds\n" \
    "s_add_u32 m0, s70, 46080\n" \
    "s_nop 0\n" \
    "buffer_load_dwordx4 v117, %[w0], s61 offen lds\n" \
    "s_add_u32 m0, s70, 47104\n" \
    "s_nop 0\n" \
    "buffer_load_dwordx4 v118, %[w0], s61 offen lds\n" \
    "s_add_u32 m0, s70, 48128\n" \
    "s_nop 0\n" \
    "buffer_load_dwordx4 v119, %[w0], s61 offen lds\n" \
    "s_add_u32 m0, s70, 49152\n" \
    "s_nop 0\n" \
    "buffer_load_dwordx4 v120, %[w0], s61 offen lds\n" \
    "s_add_u32 m0, s70, 50176\n" \
    "s_nop 0\n" \
    "buffer_load_dwordx4 v121, %[w0], s61 offen lds\n" \
    "s_mov_b32 s61, 81920\n" \
    "s_add_u32 m0, s70, 81920\n" \
    "s_nop 0\n" \
    "buffer_load_dwordx4 v112, %[w0], s61 offen lds\n" \
    "s_add_u32 m0, s70, 82944\n" \
    "s_nop 0\n" \
    "buffer_load_dwordx4 v113, %[w0], s61 offen lds\n" \
    "s_add_u32 m0, s70, 83968\n" \
    "s_nop 0\n" \
    "buffer_load_dwordx4 v114, %[w0], s61 offen lds\n" \
    "s_add_u32 m0, s70, 84992\n" \
    "s_nop 0\n" \
    "buffer_load_dwordx4 v115, %[w0], s61 offen lds\n" \
    "s_add_u32 m0, s70, 86016\n" \
    "s_nop 0\n" \
    "buffer_load_dwordx4 v116, %[w0], s61 offen lds\n" \
    "s_add_u32 m0, s70, 87040\n" \
    "s_nop 0\n" \
    "buffer_load_dwordx4 v117, %[w0], s61 offen lds\n" \
    "s_add_u32 m0, s70, 88064\n" \
    "s_nop 0\n" \
    "buffer_load_dwordx4 v118, %[w0], s61 offen lds\n" \
    "s_add_u32 m0, s70, 89088\n" \
    "s_nop 0\n" \
    "buffer_load_dwordx4 v119, %[w0], s61 offen lds\n" \
    "s_add_u32 m0, s70, 90112\n" \
    "s_nop 0\n" \
    "buffer_load_dwordx4 v120, %[w0], s61 offen lds\n" \
    "s_add_u32 m0, s70, 91136\n" \
    "s_nop 0\n" \
    "buffer_load_dwordx4 v121, %[w0], s61 offen lds\n" \
    "s_mov_b32 s61, 122880\n" \
    "s_add_u32 m0, s70, 122880\n" \
    "s_nop 0\n" \
    "buffer_load_dwordx4 v112, %[w0], s61 offen lds\n" \
    "s_add_u32 m0, s70, 123904\n" \
    "s_nop 0\n" \
    "buffer_load_dwordx4 v113, %[w0], s61 offen lds\n" \
    "s_add_u32 m0, s70, 124928\n" \
    "s_nop 0\n" \
    "buffer_load_dwordx4 v114, %[w0], s61 offen lds\n" \
    "s_add_u32 m0, s70, 125952\n" \
    "s_nop 0\n" \
    "buffer_load_dwordx4 v115, %[w0], s61 offen lds\n" \
    "s_add_u32 m0, s70, 126976\n" \
    "s_nop 0\n" \
    "buffer_load_dwordx4 v116, %[w0], s61 offen lds\n" \
    "s_add_u32 m0, s70, 128000\n" \
    "s_nop 0\n" \
    "buffer_load_dwordx4 v117, %[w0], s61 offen lds\n" \
    "s_add_u32 m0, s70, 129024\n" \
    "s_nop 0\n" \
    "buffer_load_dwordx4 v118, %[w0], s61 offen lds\n" \
    "s_add_u32 m0, s70, 130048\n" \
    "s_nop 0\n" \
    "buffer_load_dwordx4 v119, %[w0], s61 offen lds\n" \
    "s_add_u32 m0, s70, 131072\n" \
    "s_nop 0\n" \
    "buffer_load_dwordx4 v120, %[w0], s61 offen lds\n" \
    "s_add_u32 m0, s70, 132096\n" \
    "s_nop 0\n" \
    "buffer_load_dwordx4 v121, %[w0], s61 offen lds\n" \
    "s_waitcnt vmcnt(30)\n" \
    "s_barrier\n" \
    "ds_read_b64_tr_b16 a[0:1], v128\n" \
    "ds_read_b64_tr_b16 a[2:3], v128 offset:20480\n" \
    "ds_read_b64_tr_b16 a[24:25], v129\n" \
    "ds_read_b64_tr_b16 a[26:27], v129 offset:20480\n" \
    "ds_read_b64_tr_b16 a[48:49], v130\n" \
    "ds_read_b64_tr_b16 a[50:51], v130 offset:20480\n" \
    "ds_read_b64_tr_b16 a[72:73], v131\n" \
    "ds_read_b64_tr_b16 a[74:75], v131 offset:20480\n" \
    "ds_read_b64_tr_b16 a[96:97], v132\n" \
    "ds_read_b64_tr_b16 a[98:99], v132 offset:20480\n" \
    "ds_read_b64_tr_b16 a[120:121], v133\n" \
    "ds_read_b64_tr_b16 a[122:123], v133 offset:20480\n" \
    "s_waitcnt lgkmcnt(8)\n" \
    "ds_read_b64_tr_b16 a[144:145], v134\n" \
    "ds_read_b64_tr_b16 a[146:147], v134 offset:20480\n" \
    "ds_read_b64_tr_b16 a[168:169], v135\n" \
    "ds_read_b64_tr_b16 a[170:171], v135 offset:20480\n" \
    "s_waitcnt lgkmcnt(8)\n" \
    "ds_read_b64_tr_b16 a[192:193], v136\n" \
    "ds_read_b64_tr_b16 a[194:195], v136 offset:20480\n" \
    "ds_read_b64_tr_b16 a[216:217], v137\n" \
    "ds_read_b64_tr_b16 a[218:219], v137 offset:20480\n" \
    "s_waitcnt lgkmcnt(0)\n" \
    "s_waitcnt vmcnt(20)\n" \
    "s_barrier\n" \
    "s_mov_b32 s61, 163840\n" \
    "s_add_u32 m0, s70, 0\n" \
    "s_nop 0\n" \
    "buffer_load_dwordx4 v112, %[w0], s61 offen lds\n" \
    "s_add_u32 m0, s70, 1024\n" \
    "s_nop 0\n" \
    "buffer_load_dwordx4 v113, %[w0], s61 offen lds\n" \
    "s_add_u32 m0, s70, 2048\n" \
    "s_nop 0\n" \
    "buffer_load_dwordx4 v114, %[w0], s61 offen lds\n" \
    "s_add_u32 m0, s70, 3072\n" \
    "s_nop 0\n" \
    "buffer_load_dwordx4 v115, %[w0], s61 offen lds\n" \
    "s_add_u32 m0, s70, 4096\n" \
    "s_nop 0\n" \
    "buffer_load_dwordx4 v116, %[w0], s61 offen lds\n" \
    "s_add_u32 m0, s70, 5120\n" \
    "s_nop 0\n" \
    "buffer_load_dwordx4 v117, %[w0], s61 offen lds\n" \
    "s_add_u32 m0, s70, 6144\n" \
    "s_nop 0\n" \
    "buffer_load_dwordx4 v118, %[w0], s61 offen lds\n" \
    "s_add_u32 m0, s70, 7168\n" \
    "s_nop 0\n" \
    "buffer_load_dwordx4 v119, %[w0], s61 offen lds\n" \
    "s_add_u32 m0, s70, 8192\n" \
    "s_nop 0\n" \
    "buffer_load_dwordx4 v120, %[w0], s61 offen lds\n" \
    "s_add_u32 m0, s70, 9216\n" \
    "s_nop 0\n" \
    "buffer_load_dwordx4 v121, %[w0], s61 offen lds\n" \
    "ds_read_b64_tr_b16 a[4:5], v128 offset:40960\n" \
    "ds_read_b64_tr_b16 a[6:7], v128 offset:61440\n" \
    "ds_read_b64_tr_b16 a[28:29], v129 offset:40960\n" \
    "ds_read_b64_tr_b16 a[30:31], v129 offset:61440\n" \
    "ds_read_b64_tr_b16 a[52:53], v130 offset:40960\n" \
    "ds_read_b64_tr_b16 a[54:55], v130 offset:61440\n" \
    "ds_read_b64_tr_b16 a[76:77], v131 offset:40960\n" \
    "ds_read_b64_tr_b16 a[78:79], v131 offset:61440\n" \
    "ds_read_b64_tr_b16 a[100:101], v132 offset:40960\n" \
    "ds_read_b64_tr_b16 a[102:103], v132 offset:61440\n" \
    "ds_read_b64_tr_b16 a[124:125], v133 offset:40960\n" \
    "ds_read_b64_tr_b16 a[126:127], v133 offset:61440\n" \
    "s_waitcnt lgkmcnt(8)\n" \
    "ds_read_b64_tr_b16 a[148:149], v134 offset:40960\n" \
    "ds_read_b64_tr_b16 a[150:151], v134 offset:61440\n" \
    "ds_read_b64_tr_b16 a[172:173], v135 offset:40960\n" \
    "ds_read_b64_tr_b16 a[174:175], v135 offset:61440\n" \
    "s_waitcnt lgkmcnt(8)\n" \
    "ds_read_b64_tr_b16 a[196:197], v136 offset:40960\n" \
    "ds_read_b64_tr_b16 a[198:199], v136 offset:61440\n" \
    "ds_read_b64_tr_b16 a[220:221], v137 offset:40960\n" \
    "ds_read_b64_tr_b16 a[222:223], v137 offset:61440\n" \
    "s_waitcnt lgkmcnt(0)\n" \
    "s_waitcnt vmcnt(20)\n" \
    "s_barrier\n" \
    "s_mov_b32 s61, 204800\n" \
    "s_add_u32 m0, s70, 40960\n" \
    "s_nop 0\n" \
    "buffer_load_dwordx4 v112, %[w0], s61 offen lds\n" \
    "s_add_u32 m0, s70, 41984\n" \
    "s_nop 0\n" \
    "buffer_load_dwordx4 v113, %[w0], s61 offen lds\n" \
    "s_add_u32 m0, s70, 43008\n" \
    "s_nop 0\n" \
    "buffer_load_dwordx4 v114, %[w0], s61 offen lds\n" \
    "s_add_u32 m0, s70, 44032\n" \
    "s_nop 0\n" \
    "buffer_load_dwordx4 v115, %[w0], s61 offen lds\n" \
    "s_add_u32 m0, s70, 45056\n" \
    "s_nop 0\n" \
    "buffer_load_dwordx4 v116, %[w0], s61 offen lds\n" \
    "s_add_u32 m0, s70, 46080\n" \
    "s_nop 0\n" \
    "buffer_load_dwordx4 v117, %[w0], s61 offen lds\n" \
    "s_add_u32 m0, s70, 47104\n" \
    "s_nop 0\n" \
    "buffer_load_dwordx4 v118, %[w0], s61 offen lds\n" \
    "s_add_u32 m0, s70, 48128\n" \
    "s_nop 0\n" \
    "buffer_load_dwordx4 v119, %[w0], s61 offen lds\n" \
    "s_add_u32 m0, s70, 49152\n" \
    "s_nop 0\n" \
    "buffer_load_dwordx4 v120, %[w0], s61 offen lds\n" \
    "s_add_u32 m0, s70, 50176\n" \
    "s_nop 0\n" \
    "buffer_load_dwordx4 v121, %[w0], s61 offen lds\n" \
    "ds_read_b64_tr_b16 a[8:9], v140\n" \
    "ds_read_b64_tr_b16 a[10:11], v140 offset:20480\n" \
    "ds_read_b64_tr_b16 a[32:33], v141\n" \
    "ds_read_b64_tr_b16 a[34:35], v141 offset:20480\n" \
    "ds_read_b64_tr_b16 a[56:57], v142\n" \
    "ds_read_b64_tr_b16 a[58:59], v142 offset:20480\n" \
    "ds_read_b64_tr_b16 a[80:81], v143\n" \
    "ds_read_b64_tr_b16 a[82:83], v143 offset:20480\n" \
    "ds_read_b64_tr_b16 a[104:105], v144\n" \
    "ds_read_b64_tr_b16 a[106:107], v144 offset:20480\n" \
    "ds_read_b64_tr_b16 a[128:129], v145\n" \
    "ds_read_b64_tr_b16 a[130:131], v145 offset:20480\n" \
    "s_waitcnt lgkmcnt(8)\n" \
    "ds_read_b64_tr_b16 a[152:153], v146\n" \
    "ds_read_b64_tr_b16 a[154:155], v146 offset:20480\n" \
    "ds_read_b64_tr_b16 a[176:177], v147\n" \
    "ds_read_b64_tr_b16 a[178:179], v147 offset:20480\n" \
    "s_waitcnt lgkmcnt(8)\n" \
    "ds_read_b64_tr_b16 a[200:201], v148\n" \
    "ds_read_b64_tr_b16 a[202:203], v148 offset:20480\n" \
    "ds_read_b64_tr_b16 a[224:225], v149\n" \
    "ds_read_b64_tr_b16 a[226:227], v149 offset:20480\n" \
    "s_waitcnt lgkmcnt(0)\n" \
    "s_waitcnt vmcnt(20)\n" \
    "s_barrier\n" \
    "ds_read_b64_tr_b16 a[12:13], v140 offset:40960\n" \
    "ds_read_b64_tr_b16 a[14:15], v140 offset:61440\n" \
    "ds_read_b64_tr_b16 a[36:37], v141 offset:40960\n" \
    "ds_read_b64_tr_b16 a[38:39], v141 offset:61440\n" \
    "ds_read_b64_tr_b16 a[60:61], v142 offset:40960\n" \
    "ds_read_b64_tr_b16 a[62:63], v142 offset:61440\n" \
    "ds_read_b64_tr_b16 a[84:85], v143 offset:40960\n" \
    "ds_read_b64_tr_b16 a[86:87], v143 offset:61440\n" \
    "ds_read_b64_tr_b16 a[108:109], v144 offset:40960\n" \
    "ds_read_b64_tr_b16 a[110:111], v144 offset:61440\n" \
    "ds_read_b64_tr_b16 a[132:133], v145 offset:40960\n" \
    "ds_read_b64_tr_b16 a[134:135], v145 offset:61440\n" \
    "s_waitcnt lgkmcnt(8)\n" \
    "ds_read_b64_tr_b16 a[156:157], v146 offset:40960\n" \
    "ds_read_b64_tr_b16 a[158:159], v146 offset:61440\n" \
    "ds_read_b64_tr_b16 a[180:181], v147 offset:40960\n" \
    "ds_read_b64_tr_b16 a[182:183], v147 offset:61440\n" \
    "s_waitcnt lgkmcnt(8)\n" \
    "ds_read_b64_tr_b16 a[204:205], v148 offset:40960\n" \
    "ds_read_b64_tr_b16 a[206:207], v148 offset:61440\n" \
    "ds_read_b64_tr_b16 a[228:229], v149 offset:40960\n" \
    "ds_read_b64_tr_b16 a[230:231], v149 offset:61440\n" \
    "s_waitcnt lgkmcnt(0)\n" \
    "s_waitcnt vmcnt(10)\n" \
    "s_barrier\n" \
    "ds_read_b64_tr_b16 a[16:17], v128\n" \
    "ds_read_b64_tr_b16 a[18:19], v128 offset:20480\n" \
    "ds_read_b64_tr_b16 a[40:41], v129\n" \
    "ds_read_b64_tr_b16 a[42:43], v129 offset:20480\n" \
    "ds_read_b64_tr_b16 a[64:65], v130\n" \
    "ds_read_b64_tr_b16 a[66:67], v130 offset:20480\n" \
    "ds_read_b64_tr_b16 a[88:89], v131\n" \
    "ds_read_b64_tr_b16 a[90:91], v131 offset:20480\n" \
    "ds_read_b64_tr_b16 a[112:113], v132\n" \
    "ds_read_b64_tr_b16 a[114:115], v132 offset:20480\n" \
    "ds_read_b64_tr_b16 a[136:137], v133\n" \
    "ds_read_b64_tr_b16 a[138:139], v133 offset:20480\n" \
    "s_waitcnt lgkmcnt(8)\n" \
    "ds_read_b64_tr_b16 a[160:161], v134\n" \
    "ds_read_b64_tr_b16 a[162:163], v134 offset:20480\n" \
    "ds_read_b64_tr_b16 a[184:185], v135\n" \
    "ds_read_b64_tr_b16 a[186:187], v135 offset:20480\n" \
    "s_waitcnt lgkmcnt(8)\n" \
    "ds_read_b64_tr_b16 a[208:209], v136\n" \
    "ds_read_b64_tr_b16 a[210:211], v136 offset:20480\n" \
    "ds_read_b64_tr_b16 a[232:233], v137\n" \
    "ds_read_b64_tr_b16 a[234:235], v137 offset:20480\n" \
    "s_waitcnt lgkmcnt(0)\n" \
    "s_waitcnt vmcnt(0)\n" \
    "s_barrier\n" \
    "ds_read_b64_tr_b16 a[20:21], v128 offset:40960\n" \
    "ds_read_b64_tr_b16 a[22:23], v128 offset:61440\n" \
    "ds_read_b64_tr_b16 a[44:45], v129 offset:40960\n" \
    "ds_read_b64_tr_b16 a[46:47], v129 offset:61440\n" \
    "ds_read_b64_tr_b16 a[68:69], v130 offset:40960\n" \
    "ds_read_b64_tr_b16 a[70:71], v130 offset:61440\n" \
    "ds_read_b64_tr_b16 a[92:93], v131 offset:40960\n" \
    "ds_read_b64_tr_b16 a[94:95], v131 offset:61440\n" \
    "ds_read_b64_tr_b16 a[116:117], v132 offset:40960\n" \
    "ds_read_b64_tr_b16 a[118:119], v132 offset:61440\n" \
    "ds_read_b64_tr_b16 a[140:141], v133 offset:40960\n" \
    "ds_read_b64_tr_b16 a[142:143], v133 offset:61440\n" \
    "s_waitcnt lgkmcnt(8)\n" \
    "ds_read_b64_tr_b16 a[164:165], v134 offset:40960\n" \
    "ds_read_b64_tr_b16 a[166:167], v134 offset:61440\n" \
    "ds_read_b64_tr_b16 a[188:189], v135 offset:40960\n" \
    "ds_read_b64_tr_b16 a[190:191], v135 offset:61440\n" \
    "s_waitcnt lgkmcnt(8)\n" \
    "ds_read_b64_tr_b16 a[212:213], v136 offset:40960\n" \
    "ds_read_b64_tr_b16 a[214:215], v136 offset:61440\n" \
    "ds_read_b64_tr_b16 a[236:237], v137 offset:40960\n" \
    "ds_read_b64_tr_b16 a[238:239], v137 offset:61440\n" \
    "s_waitcnt lgkmcnt(0)\n" \
    "s_barrier\n" \
    "s_mov_b32 s60, 0\n" \
    "s_mov_b32 s65, 0\n" \
    "s_mov_b32 s62, %[ob]\n" \
    "s_add_u32 s72, s60, 0\n" \
    "s_min_u32 s72, s72, 63\n" \
    "s_cmp_lt_u32 s72, s65\n" \
    "s_cbranch_scc1 Lo_oka\n" \
    "s_mov_b32 s64, 1048576\n" \
    "Lo_wa:\n" \
    "s_sub_u32 s64, s64, 1\n" \
    "s_cbranch_scc1 Lo_tmo\n" \
    "buffer_load_dword v184, %[one], %[ws], %[vflag] offen sc1\n" \
    "s_waitcnt vmcnt(0)\n" \
    "v_readfirstlane_b32 s65, v184\n" \
    "s_nop 3\n" \
    "s_cmp_lt_u32 s72, s65\n" \
    "s_cbranch_scc1 Lo_oka\n" \
    "s_sleep 2\n" \
    "s_branch Lo_wa\n" \
    "Lo_oka:\n" \
    "s_mul_i32 s63, s72, 393216\n" \
    "s_add_u32 s63, s63, %[vb]\n" \
    "buffer_load_dwordx4 v[168:171], %[l16], %[ws], s63 offen sc1\n" \
    "buffer_load_dwordx4 v[172:175], %[l16], %[ws], s63 offen offset:1024 sc1\n" \
    "buffer_load_dwordx4 v[176:179], %[l16], %[ws], s63 offen offset:2048 sc1\n" \
    "s_waitcnt vmcnt(2)\n" \
    "ds_write_b128 %[pk], v[168:171]\n" \
    "s_waitcnt vmcnt(1)\n" \
    "ds_write_b128 %[pk], v[172:175] offset:1024\n" \
    "s_waitcnt vmcnt(0)\n" \
    "ds_write_b128 %[pk], v[176:179] offset:2048\n" \
    "s_waitcnt lgkmcnt(0)\n" \
    "s_barrier\n" \
    "ds_read_b64 v[16:17], %[l16]\n" \
    "ds_read_b64 v[18:19], %[l16] offset:1024\n" \
    "ds_read_b64 v[20:21], %[l16] offset:8\n" \
    "ds_read_b64 v[22:23], %[l16] offset:1032\n" \
    "ds_read_b64 v[24:25], %[l16] offset:2048\n" \
    "ds_read_b64 v[26:27], %[l16] offset:3072\n" \
    "ds_read_b64 v[28:29], %[l16] offset:2056\n" \
    "ds_read_b64 v[30:31], %[l16] offset:3080\n" \
    "ds_read_b64 v[32:33], %[l16] offset:4096\n" \
    "ds_read_b64 v[34:35], %[l16] offset:5120\n" \
    "ds_read_b64 v[36:37], %[l16] offset:4104\n" \
    "ds_read_b64 v[38:39], %[l16] offset:5128\n" \
    "s_waitcnt lgkmcnt(8)\n" \
    "ds_read_b64 v[40:41], %[l16] offset:6144\n" \
    "ds_read_b64 v[42:43], %[l16] offset:7168\n" \
    "ds_read_b64 v[44:45], %[l16] offset:6152\n" \
    "ds_read_b64 v[46:47], %[l16] offset:7176\n" \
    "s_waitcnt lgkmcnt(8)\n" \
    "ds_read_b64 v[48:49], %[l16] offset:8192\n" \
    "ds_read_b64 v[50:51], %[l16] offset:9216\n" \
    "ds_read_b64 v[52:53], %[l16] offset:8200\n" \
    "ds_read_b64 v[54:55], %[l16] offset:9224\n" \
    "s_waitcnt lgkmcnt(8)\n" \
    "ds_read_b64 v[56:57], %[l16] offset:10240\n" \
    "ds_read_b64 v[58:59], %[l16] offset:11264\n" \
    "ds_read_b64 v[60:61], %[l16] offset:10248\n" \
    "ds_read_b64 v[62:63], %[l16] offset:11272\n" \
    "v_mfma_f32_16x16x32_bf16 v[112:115], v[16:19], a[0:3], 0\n" \
    "v_mfma_f32_16x16x32_bf16 v[116:119], v[20:23], a[0:3], 0\n" \
    "v_mfma_f32_16x16x32_bf16 v[112:115], v[24:27], a[4:7], v[112:115]\n" \
    "v_mfma_f32_16x16x32_bf16 v[116:119], v[28:31], a[4:7], v[116:119]\n" \
    "v_mfma_f32_16x16x32_bf16 v[112:115], v[32:35], a[8:11], v[112:115]\n" \
    "v_mfma_f32_16x16x32_bf16 v[116:119], v[36:39], a[8:11], v[116:119]\n" \
    "s_waitcnt lgkmcnt(10)\n" \
    "v_mfma_f32_16x16x32_bf16 v[112:115], v[40:43], a[12:15], v[112:115]\n" \
    "s_waitcnt lgkmcnt(8)\n" \
    "v_mfma_f32_16x16x32_bf16 v[116:119], v[44:47], a[12:15], v[116:119]\n" \
    "s_waitcnt lgkmcnt(6)\n" \
    "v_mfma_f32_16x16x32_bf16 v[112:115], v[48:51], a[16:19], v[112:115]\n" \
    "s_waitcnt lgkmcnt(4)\n" \
    "v_mfma_f32_16x16x32_bf16 v[116:119], v[52:55], a[16:19], v[116:119]\n" \
    "s_waitcnt lgkmcnt(2)\n" \
    "v_mfma_f32_16x16x32_bf16 v[112:115], v[56:59], a[20:23], v[112:115]\n" \
    "s_waitcnt lgkmcnt(0)\n" \
    "v_mfma_f32_16x16x32_bf16 v[116:119], v[60:63], a[20:23], v[116:119]\n" \
    "v_mfma_f32_16x16x32_bf16 v[120:123], v[16:19], a[24:27], 0\n" \
    "s_nop 1\n" \
    "v_mfma_f32_16x16x32_bf16 v[124:127], v[20:23], a[24:27], 0\n" \
    "s_nop 1\n" \
    "v_mfma_f32_16x16x32_bf16 v[120:123], v[24:27], a[28:31], v[120:123]\n" \
    "v_cvt_pk_bf16_f32 v128, v112, v113\n" \
    "v_cvt_pk_bf16_f32 v129, v116, v117\n" \
    "v_mfma_f32_16x16x32_bf16 v[124:127], v[28:31], a[28:31], v[124:127]\n" \
    "v_cvt_pk_bf16_f32 v130, v114, v115\n" \
    "v_cvt_pk_bf16_f32 v131, v118, v119\n" \
    "v_mfma_f32_16x16x32_bf16 v[120:123], v[32:35], a[32:35], v[120:123]\n" \
    "s_add_u32 s61, s62, 0\n" \
    "s_lshr_b32 s79, s61, 8\n" \
    "v_mfma_f32_16x16x32_bf16 v[124:127], v[36:39], a[32:35], v[124:127]\n" \
    "s_andn2_b32 s79, 0x2000, s79\n" \
    "s_xor_b32 s79, s79, %[par]\n" \
    "v_mfma_f32_16x16x32_bf16 v[120:123], v[40:43], a[36:39], v[120:123]\n" \
    "s_add_u32 s61, s61, s79\n" \
    "buffer_store_dwordx4 v[128:131], %[l16], %[ws], s61 offen sc1\n" \
    "v_mfma_f32_16x16x32_bf16 v[124:127], v[44:47], a[36:39], v[124:127]\n" \
    "v_mfma_f32_16x16x32_bf16 v[120:123], v[48:51], a[40:43], v[120:123]\n" \
    "v_mfma_f32_16x16x32_bf16 v[124:127], v[52:55], a[40:43], v[124:127]\n" \
    "v_mfma_f32_16x16x32_bf16 v[120:123], v[56:59], a[44:47], v[120:123]\n" \
    "v_mfma_f32_16x16x32_bf16 v[124:127], v[60:63], a[44:47], v[124:127]\n" \
    "v_mfma_f32_16x16x32_bf16 v[112:115], v[16:19], a[48:51], 0\n" \
    "s_nop 1\n" \
    "v_mfma_f32_16x16x32_bf16 v[116:119], v[20:23], a[48:51], 0\n" \
    "s_nop 1\n" \
    "v_mfma_f32_16x16x32_bf16 v[112:115], v[24:27], a[52:55], v[112:115]\n" \
    "v_cvt_pk_bf16_f32 v132, v120, v121\n" \
    "v_cvt_pk_bf16_f32 v133, v124, v125\n" \
    "v_mfma_f32_16x16x32_bf16 v[116:119], v[28:31], a[52:55], v[116:119]\n" \
    "v_cvt_pk_bf16_f32 v134, v122, v123\n" \
    "v_cvt_pk_bf16_f32 v135, v126, v127\n" \
    "v_mfma_f32_16x16x32_bf16 v[112:115], v[32:35], a[56:59], v[112:115]\n" \
    "s_add_u32 s61, s62, 65536\n" \
    "s_lshr_b32 s79, s61, 8\n" \
    "v_mfma_f32_16x16x32_bf16 v[116:119], v[36:39], a[56:59], v[116:119]\n" \
    "s_andn2_b32 s79, 0x2000, s79\n" \
    "s_xor_b32 s79, s79, %[par]\n" \
    "v_mfma_f32_16x16x32_bf16 v[112:115], v[40:43], a[60:63], v[112:115]\n" \
    "s_add_u32 s61, s61, s79\n" \
    "buffer_store_dwordx4 v[132:135], %[l16], %[ws], s61 offen sc1\n" \
    "v_mfma_f32_16x16x32_bf16 v[116:119], v[44:47], a[60:63], v[116:119]\n" \
    "v_mfma_f32_16x16x32_bf16 v[112:115], v[48:51], a[64:67], v[112:115]\n" \
    "v_mfma_f32_16x16x32_bf16 v[116:119], v[52:55], a[64:67], v[116:119]\n" \
    "v_mfma_f32_16x16x32_bf16 v[112:115], v[56:59], a[68:71], v[112:115]\n" \
    "v_mfma_f32_16x16x32_bf16 v[116:119], v[60:63], a[68:71], v[116:119]\n" \
    "v_mfma_f32_16x16x32_bf16 v[120:123], v[16:19], a[72:75], 0\n" \
    "s_nop 1\n" \
    "v_mfma_f32_16x16x32_bf16 v[124:127], v[20:23], a[72:75], 0\n" \
    "s_nop 1\n" \
    "v_mfma_f32_16x16x32_bf16 v[120:123], v[24:27], a[76:79], v[120:123]\n" \
    "v_cvt_pk_bf16_f32 v136, v112, v113\n" \
    "v_cvt_pk_bf16_f32 v137, v116, v117\n" \
    "v_mfma_f32_16x16x32_bf16 v[124:127], v[28:31], a[76:79], v[124:127]\n" \
    "v_cvt_pk_bf16_f32 v138, v114, v115\n" \
    "v_cvt_pk_bf16_f32 v139, v118, v119\n" \
    "v_mfma_f32_16x16x32_bf16 v[120:123], v[32:35], a[80:83], v[120:123]\n" \
    "s_add_u32 s61, s62, 131072\n" \
    "s_lshr_b32 s79, s61, 8\n" \
    "v_mfma_f32_16x16x32_bf16 v[124:127], v[36:39], a[80:83], v[124:127]\n" \
    "s_andn2_b32 s79, 0x2000, s79\n" \
    "s_xor_b32 s79, s79, %[par]\n" \
    "v_mfma_f32_16x16x32_bf16 v[120:123], v[40:43], a[84:87], v[120:123]\n" \
    "s_add_u32 s61, s61, s79\n" \
    "buffer_store_dwordx4 v[136:139], %[l16], %[ws], s61 offen sc1\n" \
    "v_mfma_f32_16x16x32_bf16 v[124:127], v[44:47], a[84:87], v[124:127]\n" \
    "v_mfma_f32_16x16x32_bf16 v[120:123], v[48:51], a[88:91], v[120:123]\n" \
    "v_mfma_f32_16x16x32_bf16 v[124:127], v[52:55], a[88:91], v[124:127]\n" \
    "v_mfma_f32_16x16x32_bf16 v[120:123], v[56:59], a[92:95], v[120:123]\n" \
    "v_mfma_f32_16x16x32_bf16 v[124:127], v[60:63], a[92:95], v[124:127]\n" \
    "v_mfma_f32_16x16x32_bf16 v[112:115], v[16:19], a[96:99], 0\n" \
    "s_nop 1\n" \
    "v_mfma_f32_16x16x32_bf16 v[116:119], v[20:23], a[96:99], 0\n" \
    "s_nop 1\n" \
    "v_mfma_f32_16x16x32_bf16 v[112:115], v[24:27], a[100:103], v[112:115]\n" \
    "v_cvt_pk_bf16_f32 v140, v120, v121\n" \
    "v_cvt_pk_bf16_f32 v141, v124, v125\n" \
    "v_mfma_f32_16x16x32_bf16 v[116:119], v[28:31], a[100:103], v[116:119]\n" \
    "v_cvt_pk_bf16_f32 v142, v122, v123\n" \
    "v_cvt_pk_bf16_f32 v143, v126, v127\n" \
    "v_mfma_f32_16x16x32_bf16 v[112:115], v[32:35], a[104:107], v[112:115]\n" \
    "s_add_u32 s61, s62, 196608\n" \
    "s_lshr_b32 s79, s61, 8\n" \
    "v_mfma_f32_16x16x32_bf16 v[116:119], v[36:39], a[104:107], v[116:119]\n" \
    "s_andn2_b32 s79, 0x2000, s79\n" \
    "s_xor_b32 s79, s79, %[par]\n" \
    "v_mfma_f32_16x16x32_bf16 v[112:115], v[40:43], a[108:111], v[112:115]\n" \
    "s_add_u32 s61, s61, s79\n" \
    "buffer_store_dwordx4 v[140:143], %[l16], %[ws], s61 offen sc1\n" \
    "v_mfma_f32_16x16x32_bf16 v[116:119], v[44:47], a[108:111], v[116:119]\n" \
    "v_mfma_f32_16x16x32_bf16 v[112:115], v[48:51], a[112:115], v[112:115]\n" \
    "v_mfma_f32_16x16x32_bf16 v[116:119], v[52:55], a[112:115], v[116:119]\n" \
    "v_mfma_f32_16x16x32_bf16 v[112:115], v[56:59], a[116:119], v[112:115]\n" \
    "v_mfma_f32_16x16x32_bf16 v[116:119], v[60:63], a[116:119], v[116:119]\n" \
    "v_mfma_f32_16x16x32_bf16 v[120:123], v[16:19], a[120:123], 0\n" \
    "s_nop 1\n" \
    "v_mfma_f32_16x16x32_bf16 v[124:127], v[20:23], a[120:123], 0\n" \
    "s_nop 1\n" \
    "v_mfma_f32_16x16x32_bf16 v[120:123], v[24:27], a[124:127], v[120:123]\n" \
    "v_cvt_pk_bf16_f32 v144, v112, v113\n" \
    "v_cvt_pk_bf16_f32 v145, v116, v117\n" \
    "v_mfma_f32_16x16x32_bf16 v[124:127], v[28:31], a[124:127], v[124:127]\n" \
    "v_cvt_pk_bf16_f32 v146, v114, v115\n" \
    "v_cvt_pk_bf16_f32 v147, v118, v119\n" \
    "v_mfma_f32_16x16x32_bf16 v[120:123], v[32:35], a[128:131], v[120:123]\n" \
    "s_add_u32 s61, s62, 262144\n" \
    "s_lshr_b32 s79, s61, 8\n" \
    "v_mfma_f32_16x16x32_bf16 v[124:127], v[36:39], a[128:131], v[124:127]\n" \
    "s_andn2_b32 s79, 0x2000, s79\n" \
    "s_xor_b32 s79, s79, %[par]\n" \
    "v_mfma_f32_16x16x32_bf16 v[120:123], v[40:43], a[132:135], v[120:123]\n" \
    "s_add_u32 s61, s61, s79\n" \
    "buffer_store_dwordx4 v[144:147], %[l16], %[ws], s61 offen sc1\n" \
    "v_mfma_f32_16x16x32_bf16 v[124:127], v[44:47], a[132:135], v[124:127]\n" \
    "v_mfma_f32_16x16x32_bf16 v[120:123], v[48:51], a[136:139], v[120:123]\n" \
    "v_mfma_f32_16x16x32_bf16 v[124:127], v[52:55], a[136:139], v[124:127]\n" \
    "v_mfma_f32_16x16x32_bf16 v[120:123], v[56:59], a[140:143], v[120:123]\n" \
    "v_mfma_f32_16x16x32_bf16 v[124:127], v[60:63], a[140:143], v[124:127]\n" \
    "v_mfma_f32_16x16x32_bf16 v[112:115], v[16:19], a[144:147], 0\n" \
    "buffer_load_dword v184, %[one], %[ws], %[vflag] offen sc1\n" \
    "s_nop 0\n" \
    "v_mfma_f32_16x16x32_bf16 v[116:119], v[20:23], a[144:147], 0\n" \
    "s_nop 1\n" \
    "v_mfma_f32_16x16x32_bf16 v[112:115], v[24:27], a[148:151], v[112:115]\n" \
    "v_cvt_pk_bf16_f32 v148, v120, v121\n" \
    "v_cvt_pk_bf16_f32 v149, v124, v125\n" \
    "v_mfma_f32_16x16x32_bf16 v[116:119], v[28:31], a[148:151], v[116:119]\n" \
    "v_cvt_pk_bf16_f32 v150, v122, v123\n" \
    "v_cvt_pk_bf16_f32 v151, v126, v127\n" \
    "v_mfma_f32_16x16x32_bf16 v[112:115], v[32:35], a[152:155], v[112:115]\n" \
    "s_add_u32 s61, s62, 327680\n" \
    "s_lshr_b32 s79, s61, 8\n" \
    "v_mfma_f32_16x16x32_bf16 v[116:119], v[36:39], a[152:155], v[116:119]\n" \
    "s_andn2_b32 s79, 0x2000, s79\n" \
    "s_xor_b32 s79, s79, %[par]\n" \
    "v_mfma_f32_16x16x32_bf16 v[112:115], v[40:43], a[156:159], v[112:115]\n" \
    "s_add_u32 s61, s61, s79\n" \
    "buffer_store_dwordx4 v[148:151], %[l16], %[ws], s61 offen sc1\n" \
    "v_mfma_f32_16x16x32_bf16 v[116:119], v[44:47], a[156:159], v[116:119]\n" \
    "v_mfma_f32_16x16x32_bf16 v[112:115], v[48:51], a[160:163], v[112:115]\n" \
    "v_mfma_f32_16x16x32_bf16 v[116:119], v[52:55], a[160:163], v[116:119]\n" \
    "v_mfma_f32_16x16x32_bf16 v[112:115], v[56:59], a[164:167], v[112:115]\n" \
    "v_mfma_f32_16x16x32_bf16 v[116:119], v[60:63], a[164:167], v[116:119]\n" \
    "v_mfma_f32_16x16x32_bf16 v[120:123], v[16:19], a[168:171], 0\n" \
    "s_nop 1\n" \
    "v_mfma_f32_16x16x32_bf16 v[124:127], v[20:23], a[168:171], 0\n" \
    "s_nop 1\n" \
    "v_mfma_f32_16x16x32_bf16 v[120:123], v[24:27], a[172:175], v[120:123]\n" \
    "v_cvt_pk_bf16_f32 v152, v112, v113\n" \
    "v_cvt_pk_bf16_f32 v153, v116, v117\n" \
    "v_mfma_f32_16x16x32_bf16 v[124:127], v[28:31], a[172:175], v[124:127]\n" \
    "v_cvt_pk_bf16_f32 v154, v114, v115\n" \
    "v_cvt_pk_bf16_f32 v155, v118, v119\n" \
    "v_mfma_f32_16x16x32_bf16 v[120:123], v[32:35], a[176:179], v[120:123]\n" \
    "s_add_u32 s61, s62, 393216\n" \
    "s_lshr_b32 s79, s61, 8\n" \
    "v_mfma_f32_16x16x32_bf16 v[124:127], v[36:39], a[176:179], v[124:127]\n" \
    "s_andn2_b32 s79, 0x2000, s79\n" \
    "s_xor_b32 s79, s79, %[par]\n" \
    "v_mfma_f32_16x16x32_bf16 v[120:123], v[40:43], a[180:183], v[120:123]\n" \
    "s_add_u32 s61, s61, s79\n" \
    "buffer_store_dwordx4 v[152:155], %[l16], %[ws], s61 offen sc1\n" \
    "v_mfma_f32_16x16x32_bf16 v[124:127], v[44:47], a[180:183], v[124:127]\n" \
    "v_mfma_f32_16x16x32_bf16 v[120:123], v[48:51], a[184:187], v[120:123]\n" \
    "v_mfma_f32_16x16x32_bf16 v[124:127], v[52:55], a[184:187], v[124:127]\n" \
    "v_mfma_f32_16x16x32_bf16 v[120:123], v[56:59], a[188:191], v[120:123]\n" \
    "v_mfma_f32_16x16x32_bf16 v[124:127], v[60:63], a[188:191], v[124:127]\n" \
    "v_mfma_f32_16x16x32_bf16 v[112:115], v[16:19], a[192:195], 0\n" \
    "s_nop 1\n" \
    "v_mfma_f32_16x16x32_bf16 v[116:119], v[20:23], a[192:195], 0\n" \
    "s_nop 1\n" \
    "v_mfma_f32_16x16x32_bf16 v[112:115], v[24:27], a[196:199], v[112:115]\n" \
    "v_cvt_pk_bf16_f32 v156, v120, v121\n" \
    "v_cvt_pk_bf16_f32 v157, v124, v125\n" \
    "v_mfma_f32_16x16x32_bf16 v[116:119], v[28:31], a[196:199], v[116:119]\n" \
    "v_cvt_pk_bf16_f32 v158, v122, v123\n" \
    "v_cvt_pk_bf16_f32 v159, v126, v127\n" \
    "v_mfma_f32_16x16x32_bf16 v[112:115], v[32:35], a[200:203], v[112:115]\n" \
    "s_add_u32 s61, s62, 458752\n" \
    "s_lshr_b32 s79, s61, 8\n" \
    "v_mfma_f32_16x16x32_bf16 v[116:119], v[36:39], a[200:203], v[116:119]\n" \
    "s_andn2_b32 s79, 0x2000, s79\n" \
    "s_xor_b32 s79, s79, %[par]\n" \
    "v_mfma_f32_16x16x32_bf16 v[112:115], v[40:43], a[204:207], v[112:115]\n" \
    "s_add_u32 s61, s61, s79\n" \
    "buffer_store_dwordx4 v[156:159], %[l16], %[ws], s61 offen sc1\n" \
    "v_mfma_f32_16x16x32_bf16 v[116:119], v[44:47], a[204:207], v[116:119]\n" \
    "v_mfma_f32_16x16x32_bf16 v[112:115], v[48:51], a[208:211], v[112:115]\n" \
    "v_mfma_f32_16x16x32_bf16 v[116:119], v[52:55], a[208:211], v[116:119]\n" \
    "v_mfma_f32_16x16x32_bf16 v[112:115], v[56:59], a[212:215], v[112:115]\n" \
    "v_mfma_f32_16x16x32_bf16 v[116:119], v[60:63], a[212:215], v[116:119]\n" \
    "v_mfma_f32_16x16x32_bf16 v[120:123], v[16:19], a[216:219], 0\n" \
    "s_nop 1\n" \
    "v_mfma_f32_16x16x32_bf16 v[124:127], v[20:23], a[216:219], 0\n" \
    "s_nop 1\n" \
    "v_mfma_f32_16x16x32_bf16 v[120:123], v[24:27], a[220:223], v[120:123]\n" \
    "v_cvt_pk_bf16_f32 v160, v112, v113\n" \
    "v_cvt_pk_bf16_f32 v161, v116, v117\n" \
    "v_mfma_f32_16x16x32_bf16 v[124:127], v[28:31], a[220:223], v[124:127]\n" \
    "v_cvt_pk_bf16_f32 v162, v114, v115\n" \
    "v_cvt_pk_bf16_f32 v163, v118, v119\n" \
    "v_mfma_f32_16x16x32_bf16 v[120:123], v[32:35], a[224:227], v[120:123]\n" \
    "s_add_u32 s61, s62, 524288\n" \
    "s_lshr_b32 s79, s61, 8\n" \
    "v_mfma_f32_16x16x32_bf16 v[124:127], v[36:39], a[224:227], v[124:127]\n" \
    "s_andn2_b32 s79, 0x2000, s79\n" \
    "s_xor_b32 s79, s79, %[par]\n" \
    "v_mfma_f32_16x16x32_bf16 v[120:123], v[40:43], a[228:231], v[120:123]\n" \
    "s_add_u32 s61, s61, s79\n" \
    "buffer_store_dwordx4 v[160:163], %[l16], %[ws], s61 offen sc1\n" \
    "v_mfma_f32_16x16x32_bf16 v[124:127], v[44:47], a[228:231], v[124:127]\n" \
    "v_mfma_f32_16x16x32_bf16 v[120:123], v[48:51], a[232:235], v[120:123]\n" \
    "v_mfma_f32_16x16x32_bf16 v[124:127], v[52:55], a[232:235], v[124:127]\n" \
    "v_mfma_f32_16x16x32_bf16 v[120:123], v[56:59], a[236:239], v[120:123]\n" \
    "v_mfma_f32_16x16x32_bf16 v[124:127], v[60:63], a[236:239], v[124:127]\n" \
    "s_waitcnt vmcnt(4)\n" \
    "v_readfirstlane_b32 s65, v184\n" \
    "s_add_u32 s61, s62, 589824\n" \
    "s_lshr_b32 s79, s61, 8\n" \
    "s_andn2_b32 s79, 0x2000, s79\n" \
    "s_xor_b32 s79, s79, %[par]\n" \
    "s_add_u32 s61, s61, s79\n" \
    "v_cvt_pk_bf16_f32 v164, v120, v121\n" \
    "v_cvt_pk_bf16_f32 v165, v124, v125\n" \
    "v_cvt_pk_bf16_f32 v166, v122, v123\n" \
    "v_cvt_pk_bf16_f32 v167, v126, v127\n" \
    "buffer_store_dwordx4 v[164:167], %[l16], %[ws], s61 offen sc1\n" \
    "s_add_u32 s72, s60, 1\n" \
    "s_min_u32 s72, s72, 63\n" \
    "s_cmp_lt_u32 s72, s65\n" \
    "s_cbranch_scc1 Lo_okb\n" \
    "s_mov_b32 s64, 1048576\n" \
    "Lo_wb:\n" \
    "s_sub_u32 s64, s64, 1\n" \
    "s_cbranch_scc1 Lo_tmo\n" \
    "buffer_load_dword v184, %[one], %[ws], %[vflag] offen sc1\n" \
    "s_waitcnt vmcnt(0)\n" \
    "v_readfirstlane_b32 s65, v184\n" \
    "s_nop 3\n" \
    "s_cmp_lt_u32 s72, s65\n" \
    "s_cbranch_scc1 Lo_okb\n" \
    "s_sleep 2\n" \
    "s_branch Lo_wb\n" \
    "Lo_okb:\n" \
    "s_mul_i32 s63, s72, 393216\n" \
    "s_add_u32 s63, s63, %[vb]\n" \
    "buffer_load_dwordx4 v[168:171], %[l16], %[ws], s63 offen sc1\n" \
    "buffer_load_dwordx4 v[172:175], %[l16], %[ws], s63 offen offset:1024 sc1\n" \
    "buffer_load_dwordx4 v[176:179], %[l16], %[ws], s63 offen offset:2048 sc1\n" \
    "buffer_load_dword v184, %[one], %[ws], %[vflag] offen sc1\n" \
    "s_waitcnt vmcnt(0)\n" \
    "v_readfirstlane_b32 s65, v184\n" \
    "ds_write_b128 %[pk], v[168:171] offset:12288\n" \
    "ds_write_b128 %[pk], v[172:175] offset:13312\n" \
    "ds_write_b128 %[pk], v[176:179] offset:14336\n" \
    "s_waitcnt lgkmcnt(0)\n" \
    "s_barrier\n" \
    "s_mov_b32 s73, 1\n" \
    "s_cmp_lg_u32 %[wave], 0\n" \
    "s_cbranch_scc1 Lo_pubb\n" \
    "v_mov_b32 v180, s73\n" \
    "buffer_store_dword v180, %[one], %[ws], %[flag] offen sc1\n" \
    "Lo_pubb:\n" \
    "ds_read_b64 v[64:65], %[l16] offset:12288\n" \
    "ds_read_b64 v[66:67], %[l16] offset:13312\n" \
    "ds_read_b64 v[68:69], %[l16] offset:12296\n" \
    "ds_read_b64 v[70:71], %[l16] offset:13320\n" \
    "ds_read_b64 v[72:73], %[l16] offset:14336\n" \
    "ds_read_b64 v[74:75], %[l16] offset:15360\n" \
    "ds_read_b64 v[76:77], %[l16] offset:14344\n" \
    "ds_read_b64 v[78:79], %[l16] offset:15368\n" \
    "ds_read_b64 v[80:81], %[l16] offset:16384\n" \
    "ds_read_b64 v[82:83], %[l16] offset:17408\n" \
    "ds_read_b64 v[84:85], %[l16] offset:16392\n" \
    "ds_read_b64 v[86:87], %[l16] offset:17416\n" \
    "s_waitcnt lgkmcnt(8)\n" \
    "ds_read_b64 v[88:89], %[l16] offset:18432\n" \
    "ds_read_b64 v[90:91], %[l16] offset:19456\n" \
    "ds_read_b64 v[92:93], %[l16] offset:18440\n" \
    "ds_read_b64 v[94:95], %[l16] offset:19464\n" \
    "s_waitcnt lgkmcnt(8)\n" \
    "ds_read_b64 v[96:97], %[l16] offset:20480\n" \
    "ds_read_b64 v[98:99], %[l16] offset:21504\n" \
    "ds_read_b64 v[100:101], %[l16] offset:20488\n" \
    "ds_read_b64 v[102:103], %[l16] offset:21512\n" \
    "s_waitcnt lgkmcnt(8)\n" \
    "ds_read_b64 v[104:105], %[l16] offset:22528\n" \
    "ds_read_b64 v[106:107], %[l16] offset:23552\n" \
    "ds_read_b64 v[108:109], %[l16] offset:22536\n" \
    "ds_read_b64 v[110:111], %[l16] offset:23560\n" \
    "s_add_u32 s72, s60, 2\n" \
    "s_min_u32 s72, s72, 63\n" \
    "s_cmp_lt_u32 s72, s65\n" \
    "s_cbranch_scc1 Lo_okbx\n" \
    "s_mov_b32 s64, 1048576\n" \
    "Lo_wbx:\n" \
    "s_sub_u32 s64, s64, 1\n" \
    "s_cbranch_scc1 Lo_tmo\n" \
    "buffer_load_dword v184, %[one], %[ws], %[vflag] offen sc1\n" \
    "s_waitcnt vmcnt(0)\n" \
    "v_readfirstlane_b32 s65, v184\n" \
    "s_nop 3\n" \
    "s_cmp_lt_u32 s72, s65\n" \
    "s_cbranch_scc1 Lo_okbx\n" \
    "s_sleep 2\n" \
    "s_branch Lo_wbx\n" \
    "Lo_okbx:\n" \
    "s_mul_i32 s63, s72, 393216\n" \
    "s_add_u32 s63, s63, %[vb]\n" \
    "buffer_load_dwordx4 v[168:171], %[l16], %[ws], s63 offen sc1\n" \
    "buffer_load_dwordx4 v[172:175], %[l16], %[ws], s63 offen offset:1024 sc1\n" \
    "buffer_load_dwordx4 v[176:179], %[l16], %[ws], s63 offen offset:2048 sc1\n" \
    "buffer_load_dword v184, %[one], %[ws], %[vflag] offen sc1\n" \
    "s_add_u32 s60, s60, 1\n" \
    "s_waitcnt lgkmcnt(0)\n" \
    "s_branch Lo_odd\n" \
    "Lo_loop:\n" \
    "s_nop 1\n" \
    "v_mfma_f32_16x16x32_bf16 v[112:115], v[16:19], a[0:3], 0\n" \
    "s_add_u32 s61, s62, 589824\n" \
    "s_lshr_b32 s79, s61, 8\n" \
    "v_mfma_f32_16x16x32_bf16 v[116:119], v[20:23], a[0:3], 0\n" \
    "s_andn2_b32 s79, 0x2000, s79\n" \
    "s_xor_b32 s79, s79, %[par]\n" \
    "v_mfma_f32_16x16x32_bf16 v[112:115], v[24:27], a[4:7], v[112:115]\n" \
    "s_add_u32 s61, s61, s79\n" \
    "v_cvt_pk_bf16_f32 v164, v120, v121\n" \
    "v_mfma_f32_16x16x32_bf16 v[116:119], v[28:31], a[4:7], v[116:119]\n" \
    "v_cvt_pk_bf16_f32 v165, v124, v125\n" \
    "v_cvt_pk_bf16_f32 v166, v122, v123\n" \
    "v_mfma_f32_16x16x32_bf16 v[112:115], v[32:35], a[8:11], v[112:115]\n" \
    "v_cvt_pk_bf16_f32 v167, v126, v127\n" \
    "buffer_store_dwordx4 v[164:167], %[l16], %[ws], s61 offen sc1\n" \
    "v_mfma_f32_16x16x32_bf16 v[116:119], v[36:39], a[8:11], v[116:119]\n" \
    "s_add_u32 s62, s62, 2621440\n" \
    "v_mfma_f32_16x16x32_bf16 v[112:115], v[40:43], a[12:15], v[112:115]\n" \
    "v_mfma_f32_16x16x32_bf16 v[116:119], v[44:47], a[12:15], v[116:119]\n" \
    "v_mfma_f32_16x16x32_bf16 v[112:115], v[48:51], a[16:19], v[112:115]\n" \
    "v_mfma_f32_16x16x32_bf16 v[116:119], v[52:55], a[16:19], v[116:119]\n" \
    "v_mfma_f32_16x16x32_bf16 v[112:115], v[56:59], a[20:23], v[112:115]\n" \
    "v_mfma_f32_16x16x32_bf16 v[116:119], v[60:63], a[20:23], v[116:119]\n" \
    "v_mfma_f32_16x16x32_bf16 v[120:123], v[16:19], a[24:27], 0\n" \
    "s_nop 1\n" \
    "v_mfma_f32_16x16x32_bf16 v[124:127], v[20:23], a[24:27], 0\n" \
    "s_nop 1\n" \
    "v_mfma_f32_16x16x32_bf16 v[120:123], v[24:27], a[28:31], v[120:123]\n" \
    "v_cvt_pk_bf16_f32 v128, v112, v113\n" \
    "v_cvt_pk_bf16_f32 v129, v116, v117\n" \
    "v_mfma_f32_16x16x32_bf16 v[124:127], v[28:31], a[28:31], v[124:127]\n" \
    "v_cvt_pk_bf16_f32 v130, v114, v115\n" \
    "v_cvt_pk_bf16_f32 v131, v118, v119\n" \
    "v_mfma_f32_16x16x32_bf16 v[120:123], v[32:35], a[32:35], v[120:123]\n" \
    "v_mfma_f32_16x16x32_bf16 v[124:127], v[36:39], a[32:35], v[124:127]\n" \
    "v_mfma_f32_16x16x32_bf16 v[120:123], v[40:43], a[36:39], v[120:123]\n" \
    "v_mfma_f32_16x16x32_bf16 v[124:127], v[44:47], a[36:39], v[124:127]\n" \
    "v_mfma_f32_16x16x32_bf16 v[120:123], v[48:51], a[40:43], v[120:123]\n" \
    "v_mfma_f32_16x16x32_bf16 v[124:127], v[52:55], a[40:43], v[124:127]\n" \
    "v_mfma_f32_16x16x32_bf16 v[120:123], v[56:59], a[44:47], v[120:123]\n" \
    "v_mfma_f32_16x16x32_bf16 v[124:127], v[60:63], a[44:47], v[124:127]\n" \
    "v_mfma_f32_16x16x32_bf16 v[112:115], v[16:19], a[48:51], 0\n" \
    "s_nop 1\n" \
    "v_mfma_f32_16x16x32_bf16 v[116:119], v[20:23], a[48:51], 0\n" \
    "s_nop 1\n" \
    "v_mfma_f32_16x16x32_bf16 v[112:115], v[24:27], a[52:55], v[112:115]\n" \
    "v_cvt_pk_bf16_f32 v132, v120, v121\n" \
    "v_cvt_pk_bf16_f32 v133, v124, v125\n" \
    "v_mfma_f32_16x16x32_bf16 v[116:119], v[28:31], a[52:55], v[116:119]\n" \
    "v_cvt_pk_bf16_f32 v134, v122, v123\n" \
    "v_cvt_pk_bf16_f32 v135, v126, v127\n" \
    "v_mfma_f32_16x16x32_bf16 v[112:115], v[32:35], a[56:59], v[112:115]\n" \
    "v_mfma_f32_16x16x32_bf16 v[116:119], v[36:39], a[56:59], v[116:119]\n" \
    "v_mfma_f32_16x16x32_bf16 v[112:115], v[40:43], a[60:63], v[112:115]\n" \
    "v_mfma_f32_16x16x32_bf16 v[116:119], v[44:47], a[60:63], v[116:119]\n" \
    "v_mfma_f32_16x16x32_bf16 v[112:115], v[48:51], a[64:67], v[112:115]\n" \
    "v_mfma_f32_16x16x32_bf16 v[116:119], v[52:55], a[64:67], v[116:119]\n" \
    "v_mfma_f32_16x16x32_bf16 v[112:115], v[56:59], a[68:71], v[112:115]\n" \
    "v_mfma_f32_16x16x32_bf16 v[116:119], v[60:63], a[68:71], v[116:119]\n" \
    "v_mfma_f32_16x16x32_bf16 v[120:123], v[16:19], a[72:75], 0\n" \
    "s_nop 1\n" \
    "v_mfma_f32_16x16x32_bf16 v[124:127], v[20:23], a[72:75], 0\n" \
    "s_nop 1\n" \
    "v_mfma_f32_16x16x32_bf16 v[120:123], v[24:27], a[76:79], v[120:123]\n" \
    "v_cvt_pk_bf16_f32 v136, v112, v113\n" \
    "v_cvt_pk_bf16_f32 v137, v116, v117\n" \
    "v_mfma_f32_16x16x32_bf16 v[124:127], v[28:31], a[76:79], v[124:127]\n" \
    "v_cvt_pk_bf16_f32 v138, v114, v115\n" \
    "v_cvt_pk_bf16_f32 v139, v118, v119\n" \
    "v_mfma_f32_16x16x32_bf16 v[120:123], v[32:35], a[80:83], v[120:123]\n" \
    "v_mfma_f32_16x16x32_bf16 v[124:127], v[36:39], a[80:83], v[124:127]\n" \
    "v_mfma_f32_16x16x32_bf16 v[120:123], v[40:43], a[84:87], v[120:123]\n" \
    "v_mfma_f32_16x16x32_bf16 v[124:127], v[44:47], a[84:87], v[124:127]\n" \
    "v_mfma_f32_16x16x32_bf16 v[120:123], v[48:51], a[88:91], v[120:123]\n" \
    "v_mfma_f32_16x16x32_bf16 v[124:127], v[52:55], a[88:91], v[124:127]\n" \
    "v_mfma_f32_16x16x32_bf16 v[120:123], v[56:59], a[92:95], v[120:123]\n" \
    "v_mfma_f32_16x16x32_bf16 v[124:127], v[60:63], a[92:95], v[124:127]\n" \
    "s_waitcnt vmcnt(0)\n" \
    "v_readfirstlane_b32 s65, v184\n" \
    "ds_write_b128 %[pk], v[168:171] offset:12288\n" \
    "ds_write_b128 %[pk], v[172:175] offset:13312\n" \
    "ds_write_b128 %[pk], v[176:179] offset:14336\n" \
    "v_mfma_f32_16x16x32_bf16 v[112:115], v[16:19], a[96:99], 0\n" \
    "s_add_u32 s61, s62, 0\n" \
    "s_lshr_b32 s79, s61, 8\n" \
    "v_mfma_f32_16x16x32_bf16 v[116:119], v[20:23], a[96:99], 0\n" \
    "s_andn2_b32 s79, 0x2000, s79\n" \
    "s_xor_b32 s79, s79, %[par]\n" \
    "v_mfma_f32_16x16x32_bf16 v[112:115], v[24:27], a[100:103], v[112:115]\n" \
    "s_add_u32 s61, s61, s79\n" \
    "buffer_store_dwordx4 v[128:131], %[l16], %[ws], s61 offen sc1\n" \
    "v_mfma_f32_16x16x32_bf16 v[116:119], v[28:31], a[100:103], v[116:119]\n" \
    "s_add_u32 s61, s62, 65536\n" \
    "s_lshr_b32 s79, s61, 8\n" \
    "v_mfma_f32_16x16x32_bf16 v[112:115], v[32:35], a[104:107], v[112:115]\n" \
    "s_andn2_b32 s79, 0x2000, s79\n" \
    "s_xor_b32 s79, s79, %[par]\n" \
    "v_mfma_f32_16x16x32_bf16 v[116:119], v[36:39], a[104:107], v[116:119]\n" \
    "s_add_u32 s61, s61, s79\n" \
    "buffer_store_dwordx4 v[132:135], %[l16], %[ws], s61 offen sc1\n" \
    "v_mfma_f32_16x16x32_bf16 v[112:115], v[40:43], a[108:111], v[112:115]\n" \
    "v_cvt_pk_bf16_f32 v140, v120, v121\n" \
    "v_cvt_pk_bf16_f32 v141, v124, v125\n" \
    "v_mfma_f32_16x16x32_bf16 v[116:119], v[44:47], a[108:111], v[116:119]\n" \
    "v_cvt_pk_bf16_f32 v142, v122, v123\n" \
    "v_cvt_pk_bf16_f32 v143, v126, v127\n" \
    "v_mfma_f32_16x16x32_bf16 v[112:115], v[48:51], a[112:115], v[112:115]\n" \
    "v_mfma_f32_16x16x32_bf16 v[116:119], v[52:55], a[112:115], v[116:119]\n" \
    "v_mfma_f32_16x16x32_bf16 v[112:115], v[56:59], a[116:119], v[112:115]\n" \
    "v_mfma_f32_16x16x32_bf16 v[116:119], v[60:63], a[116:119], v[116:119]\n" \
    "s_waitcnt lgkmcnt(0)\n" \
    "s_barrier\n" \
    "s_cmp_lg_u32 %[wave], 0\n" \
    "s_cbranch_scc1 Lo_pubd\n" \
    "v_mov_b32 v180, s60\n" \
    "buffer_store_dword v180, %[one], %[ws], %[flag] offen sc1\n" \
    "Lo_pubd:\n" \
    "s_add_u32 s72, s60, 2\n" \
    "s_min_u32 s72, s72, 63\n" \
    "s_cmp_lt_u32 s72, s65\n" \
    "s_cbranch_scc1 Lo_okd\n" \
    "s_mov_b32 s64, 1048576\n" \
    "Lo_wd:\n" \
    "s_sub_u32 s64, s64, 1\n" \
    "s_cbranch_scc1 Lo_tmo\n" \
    "buffer_load_dword v184, %[one], %[ws], %[vflag] offen sc1\n" \
    "s_waitcnt vmcnt(0)\n" \
    "v_readfirstlane_b32 s65, v184\n" \
    "s_nop 3\n" \
    "s_cmp_lt_u32 s72, s65\n" \
    "s_cbranch_scc1 Lo_okd\n" \
    "s_sleep 2\n" \
    "s_branch Lo_wd\n" \
    "Lo_okd:\n" \
    "s_mul_i32 s63, s72, 393216\n" \
    "s_add_u32 s63, s63, %[vb]\n" \
    "buffer_load_dwordx4 v[168:171], %[l16], %[ws], s63 offen sc1\n" \
    "buffer_load_dwordx4 v[172:175], %[l16], %[ws], s63 offen offset:1024 sc1\n" \
    "buffer_load_dwordx4 v[176:179], %[l16], %[ws], s63 offen offset:2048 sc1\n" \
    "v_mfma_f32_16x16x32_bf16 v[120:123], v[16:19], a[120:123], 0\n" \
    "s_add_u32 s61, s62, 131072\n" \
    "s_lshr_b32 s79, s61, 8\n" \
    "v_mfma_f32_16x16x32_bf16 v[124:127], v[20:23], a[120:123], 0\n" \
    "s_andn2_b32 s79, 0x2000, s79\n" \
    "s_xor_b32 s79, s79, %[par]\n" \
    "v_mfma_f32_16x16x32_bf16 v[120:123], v[24:27], a[124:127], v[120:123]\n" \
    "s_add_u32 s61, s61, s79\n" \
    "buffer_store_dwordx4 v[136:139], %[l16], %[ws], s61 offen sc1\n" \
    "v_mfma_f32_16x16x32_bf16 v[124:127], v[28:31], a[124:127], v[124:127]\n" \
    "s_add_u32 s61, s62, 196608\n" \
    "s_lshr_b32 s79, s61, 8\n" \
    "v_mfma_f32_16x16x32_bf16 v[120:123], v[32:35], a[128:131], v[120:123]\n" \
    "s_andn2_b32 s79, 0x2000, s79\n" \
    "s_xor_b32 s79, s79, %[par]\n" \
    "v_mfma_f32_16x16x32_bf16 v[124:127], v[36:39], a[128:131], v[124:127]\n" \
    "s_add_u32 s61, s61, s79\n" \
    "buffer_store_dwordx4 v[140:143], %[l16], %[ws], s61 offen sc1\n" \
    "v_mfma_f32_16x16x32_bf16 v[120:123], v[40:43], a[132:135], v[120:123]\n" \
    "v_cvt_pk_bf16_f32 v144, v112, v113\n" \
    "v_cvt_pk_bf16_f32 v145, v116, v117\n" \
    "v_mfma_f32_16x16x32_bf16 v[124:127], v[44:47], a[132:135], v[124:127]\n" \
    "v_cvt_pk_bf16_f32 v146, v114, v115\n" \
    "v_cvt_pk_bf16_f32 v147, v118, v119\n" \
    "v_mfma_f32_16x16x32_bf16 v[120:123], v[48:51], a[136:139], v[120:123]\n" \
    "v_mfma_f32_16x16x32_bf16 v[124:127], v[52:55], a[136:139], v[124:127]\n" \
    "v_mfma_f32_16x16x32_bf16 v[120:123], v[56:59], a[140:143], v[120:123]\n" \
    "v_mfma_f32_16x16x32_bf16 v[124:127], v[60:63], a[140:143], v[124:127]\n" \
    "v_mfma_f32_16x16x32_bf16 v[112:115], v[16:19], a[144:147], 0\n" \
    "s_add_u32 s61, s62, 262144\n" \
    "s_lshr_b32 s79, s61, 8\n" \
    "v_mfma_f32_16x16x32_bf16 v[116:119], v[20:23], a[144:147], 0\n" \
    "s_andn2_b32 s79, 0x2000, s79\n" \
    "s_xor_b32 s79, s79, %[par]\n" \
    "v_mfma_f32_16x16x32_bf16 v[112:115], v[24:27], a[148:151], v[112:115]\n" \
    "s_add_u32 s61, s61, s79\n" \
    "buffer_store_dwordx4 v[144:147], %[l16], %[ws], s61 offen sc1\n" \
    "v_mfma_f32_16x16x32_bf16 v[116:119], v[28:31], a[148:151], v[116:119]\n" \
    "v_cvt_pk_bf16_f32 v148, v120, v121\n" \
    "v_cvt_pk_bf16_f32 v149, v124, v125\n" \
    "v_mfma_f32_16x16x32_bf16 v[112:115], v[32:35], a[152:155], v[112:115]\n" \
    "v_cvt_pk_bf16_f32 v150, v122, v123\n" \
    "v_cvt_pk_bf16_f32 v151, v126, v127\n" \
    "v_mfma_f32_16x16x32_bf16 v[116:119], v[36:39], a[152:155], v[116:119]\n" \
    "s_add_u32 s61, s62, 327680\n" \
    "s_lshr_b32 s79, s61, 8\n" \
    "v_mfma_f32_16x16x32_bf16 v[112:115], v[40:43], a[156:159], v[112:115]\n" \
    "s_andn2_b32 s79, 0x2000, s79\n" \
    "s_xor_b32 s79, s79, %[par]\n" \
    "v_mfma_f32_16x16x32_bf16 v[116:119], v[44:47], a[156:159], v[116:119]\n" \
    "s_add_u32 s61, s61, s79\n" \
    "buffer_store_dwordx4 v[148:151], %[l16], %[ws], s61 offen sc1\n" \
    "v_mfma_f32_16x16x32_bf16 v[112:115], v[48:51], a[160:163], v[112:115]\n" \
    "v_mfma_f32_16x16x32_bf16 v[116:119], v[52:55], a[160:163], v[116:119]\n" \
    "v_mfma_f32_16x16x32_bf16 v[112:115], v[56:59], a[164:167], v[112:115]\n" \
    "v_mfma_f32_16x16x32_bf16 v[116:119], v[60:63], a[164:167], v[116:119]\n" \
    "v_mfma_f32_16x16x32_bf16 v[120:123], v[16:19], a[168:171], 0\n" \
    "ds_read_b64 v[64:65], %[l16] offset:12288\n" \
    "s_nop 0\n" \
    "v_mfma_f32_16x16x32_bf16 v[124:127], v[20:23], a[168:171], 0\n" \
    "ds_read_b64 v[66:67], %[l16] offset:13312\n" \
    "s_nop 0\n" \
    "v_mfma_f32_16x16x32_bf16 v[120:123], v[24:27], a[172:175], v[120:123]\n" \
    "ds_read_b64 v[68:69], %[l16] offset:12296\n" \
    "v_cvt_pk_bf16_f32 v152, v112, v113\n" \
    "v_mfma_f32_16x16x32_bf16 v[124:127], v[28:31], a[172:175], v[124:127]\n" \
    "ds_read_b64 v[70:71], %[l16] offset:13320\n" \
    "v_cvt_pk_bf16_f32 v153, v116, v117\n" \
    "v_mfma_f32_16x16x32_bf16 v[120:123], v[32:35], a[176:179], v[120:123]\n" \
    "ds_read_b64 v[72:73], %[l16] offset:14336\n" \
    "v_cvt_pk_bf16_f32 v154, v114, v115\n" \
    "v_mfma_f32_16x16x32_bf16 v[124:127], v[36:39], a[176:179], v[124:127]\n" \
    "ds_read_b64 v[74:75], %[l16] offset:15360\n" \
    "v_cvt_pk_bf16_f32 v155, v118, v119\n" \
    "v_mfma_f32_16x16x32_bf16 v[120:123], v[40:43], a[180:183], v[120:123]\n" \
    "ds_read_b64 v[76:77], %[l16] offset:14344\n" \
    "s_add_u32 s61, s62, 393216\n" \
    "v_mfma_f32_16x16x32_bf16 v[124:127], v[44:47], a[180:183], v[124:127]\n" \
    "ds_read_b64 v[78:79], %[l16] offset:15368\n" \
    "s_lshr_b32 s79, s61, 8\n" \
    "v_mfma_f32_16x16x32_bf16 v[120:123], v[48:51], a[184:187], v[120:123]\n" \
    "ds_read_b64 v[80:81], %[l16] offset:16384\n" \
    "s_andn2_b32 s79, 0x2000, s79\n" \
    "v_mfma_f32_16x16x32_bf16 v[124:127], v[52:55], a[184:187], v[124:127]\n" \
    "s_xor_b32 s79, s79, %[par]\n" \
    "s_add_u32 s61, s61, s79\n" \
    "v_mfma_f32_16x16x32_bf16 v[120:123], v[56:59], a[188:191], v[120:123]\n" \
    "buffer_store_dwordx4 v[152:155], %[l16], %[ws], s61 offen sc1\n" \
    "v_mfma_f32_16x16x32_bf16 v[124:127], v[60:63], a[188:191], v[124:127]\n" \
    "v_mfma_f32_16x16x32_bf16 v[112:115], v[16:19], a[192:195], 0\n" \
    "ds_read_b64 v[82:83], %[l16] offset:17408\n" \
    "s_nop 0\n" \
    "v_mfma_f32_16x16x32_bf16 v[116:119], v[20:23], a[192:195], 0\n" \
    "ds_read_b64 v[84:85], %[l16] offset:16392\n" \
    "s_nop 0\n" \
    "v_mfma_f32_16x16x32_bf16 v[112:115], v[24:27], a[196:199], v[112:115]\n" \
    "ds_read_b64 v[86:87], %[l16] offset:17416\n" \
    "v_cvt_pk_bf16_f32 v156, v120, v121\n" \
    "v_mfma_f32_16x16x32_bf16 v[116:119], v[28:31], a[196:199], v[116:119]\n" \
    "s_waitcnt lgkmcnt(8)\n" \
    "ds_read_b64 v[88:89], %[l16] offset:18432\n" \
    "v_cvt_pk_bf16_f32 v157, v124, v125\n" \
    "v_mfma_f32_16x16x32_bf16 v[112:115], v[32:35], a[200:203], v[112:115]\n" \
    "ds_read_b64 v[90:91], %[l16] offset:19456\n" \
    "v_cvt_pk_bf16_f32 v158, v122, v123\n" \
    "v_mfma_f32_16x16x32_bf16 v[116:119], v[36:39], a[200:203], v[116:119]\n" \
    "ds_read_b64 v[92:93], %[l16] offset:18440\n" \
    "v_cvt_pk_bf16_f32 v159, v126, v127\n" \
    "v_mfma_f32_16x16x32_bf16 v[112:115], v[40:43], a[204:207], v[112:115]\n" \
    "ds_read_b64 v[94:95], %[l16] offset:19464\n" \
    "s_add_u32 s61, s62, 458752\n" \
    "v_mfma_f32_16x16x32_bf16 v[116:119], v[44:47], a[204:207], v[116:119]\n" \
    "s_waitcnt lgkmcnt(8)\n" \
    "ds_read_b64 v[96:97], %[l16] offset:20480\n" \
    "s_lshr_b32 s79, s61, 8\n" \
    "v_mfma_f32_16x16x32_bf16 v[112:115], v[48:51], a[208:211], v[112:115]\n" \
    "ds_read_b64 v[98:99], %[l16] offset:21504\n" \
    "s_andn2_b32 s79, 0x2000, s79\n" \
    "v_mfma_f32_16x16x32_bf16 v[116:119], v[52:55], a[208:211], v[116:119]\n" \
    "s_xor_b32 s79, s79, %[par]\n" \
    "s_add_u32 s61, s61, s79\n" \
    "v_mfma_f32_16x16x32_bf16 v[112:115], v[56:59], a[212:215], v[112:115]\n" \
    "buffer_store_dwordx4 v[156:159], %[l16], %[ws], s61 offen sc1\n" \
    "v_mfma_f32_16x16x32_bf16 v[116:119], v[60:63], a[212:215], v[116:119]\n" \
    "v_mfma_f32_16x16x32_bf16 v[120:123], v[16:19], a[216:219], 0\n" \
    "ds_read_b64 v[100:101], %[l16] offset:20488\n" \
    "buffer_load_dword v184, %[one], %[ws], %[vflag] offen sc1\n" \
    "v_mfma_f32_16x16x32_bf16 v[124:127], v[20:23], a[216:219], 0\n" \
    "ds_read_b64 v[102:103], %[l16] offset:21512\n" \
    "s_nop 0\n" \
    "v_mfma_f32_16x16x32_bf16 v[120:123], v[24:27], a[220:223], v[120:123]\n" \
    "s_waitcnt lgkmcnt(8)\n" \
    "ds_read_b64 v[104:105], %[l16] offset:22528\n" \
    "v_cvt_pk_bf16_f32 v160, v112, v113\n" \
    "v_mfma_f32_16x16x32_bf16 v[124:127], v[28:31], a[220:223], v[124:127]\n" \
    "ds_read_b64 v[106:107], %[l16] offset:23552\n" \
    "v_cvt_pk_bf16_f32 v161, v116, v117\n" \
    "v_mfma_f32_16x16x32_bf16 v[120:123], v[32:35], a[224:227], v[120:123]\n" \
    "ds_read_b64 v[108:109], %[l16] offset:22536\n" \
    "v_cvt_pk_bf16_f32 v162, v114, v115\n" \
    "v_mfma_f32_16x16x32_bf16 v[124:127], v[36:39], a[224:227], v[124:127]\n" \
    "ds_read_b64 v[110:111], %[l16] offset:23560\n" \
    "v_cvt_pk_bf16_f32 v163, v118, v119\n" \
    "v_mfma_f32_16x16x32_bf16 v[120:123], v[40:43], a[228:231], v[120:123]\n" \
    "s_add_u32 s61, s62, 524288\n" \
    "s_lshr_b32 s79, s61, 8\n" \
    "v_mfma_f32_16x16x32_bf16 v[124:127], v[44:47], a[228:231], v[124:127]\n" \
    "s_andn2_b32 s79, 0x2000, s79\n" \
    "s_xor_b32 s79, s79, %[par]\n" \
    "v_mfma_f32_16x16x32_bf16 v[120:123], v[48:51], a[232:235], v[120:123]\n" \
    "s_add_u32 s61, s61, s79\n" \
    "buffer_store_dwordx4 v[160:163], %[l16], %[ws], s61 offen sc1\n" \
    "v_mfma_f32_16x16x32_bf16 v[124:127], v[52:55], a[232:235], v[124:127]\n" \
    "v_mfma_f32_16x16x32_bf16 v[120:123], v[56:59], a[236:239], v[120:123]\n" \
    "v_mfma_f32_16x16x32_bf16 v[124:127], v[60:63], a[236:239], v[124:127]\n" \
    "s_add_u32 s60, s60, 1\n" \
    "s_waitcnt lgkmcnt(0)\n" \
    "Lo_odd:\n" \
    "s_nop 1\n" \
    "v_mfma_f32_16x16x32_bf16 v[112:115], v[64:67], a[0:3], 0\n" \
    "s_add_u32 s61, s62, 589824\n" \
    "s_lshr_b32 s79, s61, 8\n" \
    "v_mfma_f32_16x16x32_bf16 v[116:119], v[68:71], a[0:3], 0\n" \
    "s_andn2_b32 s79, 0x2000, s79\n" \
    "s_xor_b32 s79, s79, %[par]\n" \
    "v_mfma_f32_16x16x32_bf16 v[112:115], v[72:75], a[4:7], v[112:115]\n" \
    "s_add_u32 s61, s61, s79\n" \
    "v_cvt_pk_bf16_f32 v164, v120, v121\n" \
    "v_mfma_f32_16x16x32_bf16 v[116:119], v[76:79], a[4:7], v[116:119]\n" \
    "v_cvt_pk_bf16_f32 v165, v124, v125\n" \
    "v_cvt_pk_bf16_f32 v166, v122, v123\n" \
    "v_mfma_f32_16x16x32_bf16 v[112:115], v[80:83], a[8:11], v[112:115]\n" \
    "v_cvt_pk_bf16_f32 v167, v126, v127\n" \
    "buffer_store_dwordx4 v[164:167], %[l16], %[ws], s61 offen sc1\n" \
    "v_mfma_f32_16x16x32_bf16 v[116:119], v[84:87], a[8:11], v[116:119]\n" \
    "s_add_u32 s62, s62, 2621440\n" \
    "v_mfma_f32_16x16x32_bf16 v[112:115], v[88:91], a[12:15], v[112:115]\n" \
    "v_mfma_f32_16x16x32_bf16 v[116:119], v[92:95], a[12:15], v[116:119]\n" \
    "v_mfma_f32_16x16x32_bf16 v[112:115], v[96:99], a[16:19], v[112:115]\n" \
    "v_mfma_f32_16x16x32_bf16 v[116:119], v[100:103], a[16:19], v[116:119]\n" \
    "v_mfma_f32_16x16x32_bf16 v[112:115], v[104:107], a[20:23], v[112:115]\n" \
    "v_mfma_f32_16x16x32_bf16 v[116:119], v[108:111], a[20:23], v[116:119]\n" \
    "v_mfma_f32_16x16x32_bf16 v[120:123], v[64:67], a[24:27], 0\n" \
    "s_nop 1\n" \
    "v_mfma_f32_16x16x32_bf16 v[124:127], v[68:71], a[24:27], 0\n" \
    "s_nop 1\n" \
    "v_mfma_f32_16x16x32_bf16 v[120:123], v[72:75], a[28:31], v[120:123]\n" \
    "v_cvt_pk_bf16_f32 v128, v112, v113\n" \
    "v_cvt_pk_bf16_f32 v129, v116, v117\n" \
    "v_mfma_f32_16x16x32_bf16 v[124:127], v[76:79], a[28:31], v[124:127]\n" \
    "v_cvt_pk_bf16_f32 v130, v114, v115\n" \
    "v_cvt_pk_bf16_f32 v131, v118, v119\n" \
    "v_mfma_f32_16x16x32_bf16 v[120:123], v[80:83], a[32:35], v[120:123]\n" \
    "v_mfma_f32_16x16x32_bf16 v[124:127], v[84:87], a[32:35], v[124:127]\n" \
    "v_mfma_f32_16x16x32_bf16 v[120:123], v[88:91], a[36:39], v[120:123]\n" \
    "v_mfma_f32_16x16x32_bf16 v[124:127], v[92:95], a[36:39], v[124:127]\n" \
    "v_mfma_f32_16x16x32_bf16 v[120:123], v[96:99], a[40:43], v[120:123]\n" \
    "v_mfma_f32_16x16x32_bf16 v[124:127], v[100:103], a[40:43], v[124:127]\n" \
    "v_mfma_f32_16x16x32_bf16 v[120:123], v[104:107], a[44:47], v[120:123]\n" \
    "v_mfma_f32_16x16x32_bf16 v[124:127], v[108:111], a[44:47], v[124:127]\n" \
    "v_mfma_f32_16x16x32_bf16 v[112:115], v[64:67], a[48:51], 0\n" \
    "s_nop 1\n" \
    "v_mfma_f32_16x16x32_bf16 v[116:119], v[68:71], a[48:51], 0\n" \
    "s_nop 1\n" \
    "v_mfma_f32_16x16x32_bf16 v[112:115], v[72:75], a[52:55], v[112:115]\n" \
    "v_cvt_pk_bf16_f32 v132, v120, v121\n" \
    "v_cvt_pk_bf16_f32 v133, v124, v125\n" \
    "v_mfma_f32_16x16x32_bf16 v[116:119], v[76:79], a[52:55], v[116:119]\n" \
    "v_cvt_pk_bf16_f32 v134, v122, v123\n" \
    "v_cvt_pk_bf16_f32 v135, v126, v127\n" \
    "v_mfma_f32_16x16x32_bf16 v[112:115], v[80:83], a[56:59], v[112:115]\n" \
    "v_mfma_f32_16x16x32_bf16 v[116:119], v[84:87], a[56:59], v[116:119]\n" \
    "v_mfma_f32_16x16x32_bf16 v[112:115], v[88:91], a[60:63], v[112:115]\n" \
    "v_mfma_f32_16x16x32_bf16 v[116:119], v[92:95], a[60:63], v[116:119]\n" \
    "v_mfma_f32_16x16x32_bf16 v[112:115], v[96:99], a[64:67], v[112:115]\n" \
    "v_mfma_f32_16x16x32_bf16 v[116:119], v[100:103], a[64:67], v[116:119]\n" \
    "v_mfma_f32_16x16x32_bf16 v[112:115], v[104:107], a[68:71], v[112:115]\n" \
    "v_mfma_f32_16x16x32_bf16 v[116:119], v[108:111], a[68:71], v[116:119]\n" \
    "v_mfma_f32_16x16x32_bf16 v[120:123], v[64:67], a[72:75], 0\n" \
    "s_nop 1\n" \
    "v_mfma_f32_16x16x32_bf16 v[124:127], v[68:71], a[72:75], 0\n" \
    "s_nop 1\n" \
    "v_mfma_f32_16x16x32_bf16 v[120:123], v[72:75], a[76:79], v[120:123]\n" \
    "v_cvt_pk_bf16_f32 v136, v112, v113\n" \
    "v_cvt_pk_bf16_f32 v137, v116, v117\n" \
    "v_mfma_f32_16x16x32_bf16 v[124:127], v[76:79], a[76:79], v[124:127]\n" \
    "v_cvt_pk_bf16_f32 v138, v114, v115\n" \
    "v_cvt_pk_bf16_f32 v139, v118, v119\n" \
    "v_mfma_f32_16x16x32_bf16 v[120:123], v[80:83], a[80:83], v[120:123]\n" \
    "v_mfma_f32_16x16x32_bf16 v[124:127], v[84:87], a[80:83], v[124:127]\n" \
    "v_mfma_f32_16x16x32_bf16 v[120:123], v[88:91], a[84:87], v[120:123]\n" \
    "v_mfma_f32_16x16x32_bf16 v[124:127], v[92:95], a[84:87], v[124:127]\n" \
    "v_mfma_f32_16x16x32_bf16 v[120:123], v[96:99], a[88:91], v[120:123]\n" \
    "v_mfma_f32_16x16x32_bf16 v[124:127], v[100:103], a[88:91], v[124:127]\n" \
    "v_mfma_f32_16x16x32_bf16 v[120:123], v[104:107], a[92:95], v[120:123]\n" \
    "v_mfma_f32_16x16x32_bf16 v[124:127], v[108:111], a[92:95], v[124:127]\n" \
    "s_waitcnt vmcnt(0)\n" \
    "v_readfirstlane_b32 s65, v184\n" \
    "ds_write_b128 %[pk], v[168:171]\n" \
    "ds_write_b128 %[pk], v[172:175] offset:1024\n" \
    "ds_write_b128 %[pk], v[176:179] offset:2048\n" \
    "v_mfma_f32_16x16x32_bf16 v[112:115], v[64:67], a[96:99], 0\n" \
    "s_add_u32 s61, s62, 0\n" \
    "s_lshr_b32 s79, s61, 8\n" \
    "v_mfma_f32_16x16x32_bf16 v[116:119], v[68:71], a[96:99], 0\n" \
    "s_andn2_b32 s79, 0x2000, s79\n" \
    "s_xor_b32 s79, s79, %[par]\n" \
    "v_mfma_f32_16x16x32_bf16 v[112:115], v[72:75], a[100:103], v[112:115]\n" \
    "s_add_u32 s61, s61, s79\n" \
    "buffer_store_dwordx4 v[128:131], %[l16], %[ws], s61 offen sc1\n" \
    "v_mfma_f32_16x16x32_bf16 v[116:119], v[76:79], a[100:103], v[116:119]\n" \
    "s_add_u32 s61, s62, 65536\n" \
    "s_lshr_b32 s79, s61, 8\n" \
    "v_mfma_f32_16x16x32_bf16 v[112:115], v[80:83], a[104:107], v[112:115]\n" \
    "s_andn2_b32 s79, 0x2000, s79\n" \
    "s_xor_b32 s79, s79, %[par]\n" \
    "v_mfma_f32_16x16x32_bf16 v[116:119], v[84:87], a[104:107], v[116:119]\n" \
    "s_add_u32 s61, s61, s79\n" \
    "buffer_store_dwordx4 v[132:135], %[l16], %[ws], s61 offen sc1\n" \
    "v_mfma_f32_16x16x32_bf16 v[112:115], v[88:91], a[108:111], v[112:115]\n" \
    "v_cvt_pk_bf16_f32 v140, v120, v121\n" \
    "v_cvt_pk_bf16_f32 v141, v124, v125\n" \
    "v_mfma_f32_16x16x32_bf16 v[116:119], v[92:95], a[108:111], v[116:119]\n" \
    "v_cvt_pk_bf16_f32 v142, v122, v123\n" \
    "v_cvt_pk_bf16_f32 v143, v126, v127\n" \
    "v_mfma_f32_16x16x32_bf16 v[112:115], v[96:99], a[112:115], v[112:115]\n" \
    "v_mfma_f32_16x16x32_bf16 v[116:119], v[100:103], a[112:115], v[116:119]\n" \
    "v_mfma_f32_16x16x32_bf16 v[112:115], v[104:107], a[116:119], v[112:115]\n" \
    "v_mfma_f32_16x16x32_bf16 v[116:119], v[108:111], a[116:119], v[116:119]\n" \
    "s_waitcnt lgkmcnt(0)\n" \
    "s_barrier\n" \
    "s_cmp_lg_u32 %[wave], 0\n" \
    "s_cbranch_scc1 Lo_pube\n" \
    "v_mov_b32 v180, s60\n" \
    "buffer_store_dword v180, %[one], %[ws], %[flag] offen sc1\n" \
    "Lo_pube:\n" \
    "s_add_u32 s72, s60, 2\n" \
    "s_min_u32 s72, s72, 63\n" \
    "s_cmp_lt_u32 s72, s65\n" \
    "s_cbranch_scc1 Lo_oke\n" \
    "s_mov_b32 s64, 1048576\n" \
    "Lo_we:\n" \
    "s_sub_u32 s64, s64, 1\n" \
    "s_cbranch_scc1 Lo_tmo\n" \
    "buffer_load_dword v184, %[one], %[ws], %[vflag] offen sc1\n" \
    "s_waitcnt vmcnt(0)\n" \
    "v_readfirstlane_b32 s65, v184\n" \
    "s_nop 3\n" \
    "s_cmp_lt_u32 s72, s65\n" \
    "s_cbranch_scc1 Lo_oke\n" \
    "s_sleep 2\n" \
    "s_branch Lo_we\n" \
    "Lo_oke:\n" \
    "s_mul_i32 s63, s72, 393216\n" \
    "s_add_u32 s63, s63, %[vb]\n" \
    "buffer_load_dwordx4 v[168:171], %[l16], %[ws], s63 offen sc1\n" \
    "buffer_load_dwordx4 v[172:175], %[l16], %[ws], s63 offen offset:1024 sc1\n" \
    "buffer_load_dwordx4 v[176:179], %[l16], %[ws], s63 offen offset:2048 sc1\n" \
    "v_mfma_f32_16x16x32_bf16 v[120:123], v[64:67], a[120:123], 0\n" \
    "s_add_u32 s61, s62, 131072\n" \
    "s_lshr_b32 s79, s61, 8\n" \
    "v_mfma_f32_16x16x32_bf16 v[124:127], v[68:71], a[120:123], 0\n" \
    "s_andn2_b32 s79, 0x2000, s79\n" \
    "s_xor_b32 s79, s79, %[par]\n" \
    "v_mfma_f32_16x16x32_bf16 v[120:123], v[72:75], a[124:127], v[120:123]\n" \
    "s_add_u32 s61, s61, s79\n" \
    "buffer_store_dwordx4 v[136:139], %[l16], %[ws], s61 offen sc1\n" \
    "v_mfma_f32_16x16x32_bf16 v[124:127], v[76:79], a[124:127], v[124:127]\n" \
    "s_add_u32 s61, s62, 196608\n" \
    "s_lshr_b32 s79, s61, 8\n" \
    "v_mfma_f32_16x16x32_bf16 v[120:123], v[80:83], a[128:131], v[120:123]\n" \
    "s_andn2_b32 s79, 0x2000, s79\n" \
    "s_xor_b32 s79, s79, %[par]\n" \
    "v_mfma_f32_16x16x32_bf16 v[124:127], v[84:87], a[128:131], v[124:127]\n" \
    "s_add_u32 s61, s61, s79\n" \
    "buffer_store_dwordx4 v[140:143], %[l16], %[ws], s61 offen sc1\n" \
    "v_mfma_f32_16x16x32_bf16 v[120:123], v[88:91], a[132:135], v[120:123]\n" \
    "v_cvt_pk_bf16_f32 v144, v112, v113\n" \
    "v_cvt_pk_bf16_f32 v145, v116, v117\n" \
    "v_mfma_f32_16x16x32_bf16 v[124:127], v[92:95], a[132:135], v[124:127]\n" \
    "v_cvt_pk_bf16_f32 v146, v114, v115\n" \
    "v_cvt_pk_bf16_f32 v147, v118, v119\n" \
    "v_mfma_f32_16x16x32_bf16 v[120:123], v[96:99], a[136:139], v[120:123]\n" \
    "v_mfma_f32_16x16x32_bf16 v[124:127], v[100:103], a[136:139], v[124:127]\n" \
    "v_mfma_f32_16x16x32_bf16 v[120:123], v[104:107], a[140:143], v[120:123]\n" \
    "v_mfma_f32_16x16x32_bf16 v[124:127], v[108:111], a[140:143], v[124:127]\n" \
    "v_mfma_f32_16x16x32_bf16 v[112:115], v[64:67], a[144:147], 0\n" \
    "s_add_u32 s61, s62, 262144\n" \
    "s_lshr_b32 s79, s61, 8\n" \
    "v_mfma_f32_16x16x32_bf16 v[116:119], v[68:71], a[144:147], 0\n" \
    "s_andn2_b32 s79, 0x2000, s79\n" \
    "s_xor_b32 s79, s79, %[par]\n" \
    "v_mfma_f32_16x16x32_bf16 v[112:115], v[72:75], a[148:151], v[112:115]\n" \
    "s_add_u32 s61, s61, s79\n" \
    "buffer_store_dwordx4 v[144:147], %[l16], %[ws], s61 offen sc1\n" \
    "v_mfma_f32_16x16x32_bf16 v[116:119], v[76:79], a[148:151], v[116:119]\n" \
    "v_cvt_pk_bf16_f32 v148, v120, v121\n" \
    "v_cvt_pk_bf16_f32 v149, v124, v125\n" \
    "v_mfma_f32_16x16x32_bf16 v[112:115], v[80:83], a[152:155], v[112:115]\n" \
    "v_cvt_pk_bf16_f32 v150, v122, v123\n" \
    "v_cvt_pk_bf16_f32 v151, v126, v127\n" \
    "v_mfma_f32_16x16x32_bf16 v[116:119], v[84:87], a[152:155], v[116:119]\n" \
    "s_add_u32 s61, s62, 327680\n" \
    "s_lshr_b32 s79, s61, 8\n" \
    "v_mfma_f32_16x16x32_bf16 v[112:115], v[88:91], a[156:159], v[112:115]\n" \
    "s_andn2_b32 s79, 0x2000, s79\n" \
    "s_xor_b32 s79, s79, %[par]\n" \
    "v_mfma_f32_16x16x32_bf16 v[116:119], v[92:95], a[156:159], v[116:119]\n" \
    "s_add_u32 s61, s61, s79\n" \
    "buffer_store_dwordx4 v[148:151], %[l16], %[ws], s61 offen sc1\n" \
    "v_mfma_f32_16x16x32_bf16 v[112:115], v[96:99], a[160:163], v[112:115]\n" \
    "v_mfma_f32_16x16x32_bf16 v[116:119], v[100:103], a[160:163], v[116:119]\n" \
    "v_mfma_f32_16x16x32_bf16 v[112:115], v[104:107], a[164:167], v[112:115]\n" \
    "v_mfma_f32_16x16x32_bf16 v[116:119], v[108:111], a[164:167], v[116:119]\n" \
    "v_mfma_f32_16x16x32_bf16 v[120:123], v[64:67], a[168:171], 0\n" \
    "ds_read_b64 v[16:17], %[l16]\n" \
    "s_nop 0\n" \
    "v_mfma_f32_16x16x32_bf16 v[124:127], v[68:71], a[168:171], 0\n" \
    "ds_read_b64 v[18:19], %[l16] offset:1024\n" \
    "s_nop 0\n" \
    "v_mfma_f32_16x16x32_bf16 v[120:123], v[72:75], a[172:175], v[120:123]\n" \
    "ds_read_b64 v[20:21], %[l16] offset:8\n" \
    "v_cvt_pk_bf16_f32 v152, v112, v113\n" \
    "v_mfma_f32_16x16x32_bf16 v[124:127], v[76:79], a[172:175], v[124:127]\n" \
    "ds_read_b64 v[22:23], %[l16] offset:1032\n" \
    "v_cvt_pk_bf16_f32 v153, v116, v117\n" \
    "v_mfma_f32_16x16x32_bf16 v[120:123], v[80:83], a[176:179], v[120:123]\n" \
    "ds_read_b64 v[24:25], %[l16] offset:2048\n" \
    "v_cvt_pk_bf16_f32 v154, v114, v115\n" \
    "v_mfma_f32_16x16x32_bf16 v[124:127], v[84:87], a[176:179], v[124:127]\n" \
    "ds_read_b64 v[26:27], %[l16] offset:3072\n" \
    "v_cvt_pk_bf16_f32 v155, v118, v119\n" \
    "v_mfma_f32_16x16x32_bf16 v[120:123], v[88:91], a[180:183], v[120:123]\n" \
    "ds_read_b64 v[28:29], %[l16] offset:2056\n" \
    "s_add_u32 s61, s62, 393216\n" \
    "v_mfma_f32_16x16x32_bf16 v[124:127], v[92:95], a[180:183], v[124:127]\n" \
    "ds_read_b64 v[30:31], %[l16] offset:3080\n" \
    "s_lshr_b32 s79, s61, 8\n" \
    "v_mfma_f32_16x16x32_bf16 v[120:123], v[96:99], a[184:187], v[120:123]\n" \
    "ds_read_b64 v[32:33], %[l16] offset:4096\n" \
    "s_andn2_b32 s79, 0x2000, s79\n" \
    "v_mfma_f32_16x16x32_bf16 v[124:127], v[100:103], a[184:187], v[124:127]\n" \
    "s_xor_b32 s79, s79, %[par]\n" \
    "s_add_u32 s61, s61, s79\n" \
    "v_mfma_f32_16x16x32_bf16 v[120:123], v[104:107], a[188:191], v[120:123]\n" \
    "buffer_store_dwordx4 v[152:155], %[l16], %[ws], s61 offen sc1\n" \
    "v_mfma_f32_16x16x32_bf16 v[124:127], v[108:111], a[188:191], v[124:127]\n" \
    "v_mfma_f32_16x16x32_bf16 v[112:115], v[64:67], a[192:195], 0\n" \
    "ds_read_b64 v[34:35], %[l16] offset:5120\n" \
    "s_nop 0\n" \
    "v_mfma_f32_16x16x32_bf16 v[116:119], v[68:71], a[192:195], 0\n" \
    "ds_read_b64 v[36:37], %[l16] offset:4104\n" \
    "s_nop 0\n" \
    "v_mfma_f32_16x16x32_bf16 v[112:115], v[72:75], a[196:199], v[112:115]\n" \
    "ds_read_b64 v[38:39], %[l16] offset:5128\n" \
    "v_cvt_pk_bf16_f32 v156, v120, v121\n" \
    "v_mfma_f32_16x16x32_bf16 v[116:119], v[76:79], a[196:199], v[116:119]\n" \
    "s_waitcnt lgkmcnt(8)\n" \
    "ds_read_b64 v[40:41], %[l16] offset:6144\n" \
    "v_cvt_pk_bf16_f32 v157, v124, v125\n" \
    "v_mfma_f32_16x16x32_bf16 v[112:115], v[80:83], a[200:203], v[112:115]\n" \
    "ds_read_b64 v[42:43], %[l16] offset:7168\n" \
    "v_cvt_pk_bf16_f32 v158, v122, v123\n" \
    "v_mfma_f32_16x16x32_bf16 v[116:119], v[84:87], a[200:203], v[116:119]\n" \
    "ds_read_b64 v[44:45], %[l16] offset:6152\n" \
    "v_cvt_pk_bf16_f32 v159, v126, v127\n" \
    "v_mfma_f32_16x16x32_bf16 v[112:115], v[88:91], a[204:207], v[112:115]\n" \
    "ds_read_b64 v[46:47], %[l16] offset:7176\n" \
    "s_add_u32 s61, s62, 458752\n" \
    "v_mfma_f32_16x16x32_bf16 v[116:119], v[92:95], a[204:207], v[116:119]\n" \
    "s_waitcnt lgkmcnt(8)\n" \
    "ds_read_b64 v[48:49], %[l16] offset:8192\n" \
    "s_lshr_b32 s79, s61, 8\n" \
    "v_mfma_f32_16x16x32_bf16 v[112:115], v[96:99], a[208:211], v[112:115]\n" \
    "ds_read_b64 v[50:51], %[l16] offset:9216\n" \
    "s_andn2_b32 s79, 0x2000, s79\n" \
    "v_mfma_f32_16x16x32_bf16 v[116:119], v[100:103], a[208:211], v[116:119]\n" \
    "s_xor_b32 s79, s79, %[par]\n" \
    "s_add_u32 s61, s61, s79\n" \
    "v_mfma_f32_16x16x32_bf16 v[112:115], v[104:107], a[212:215], v[112:115]\n" \
    "buffer_store_dwordx4 v[156:159], %[l16], %[ws], s61 offen sc1\n" \
    "v_mfma_f32_16x16x32_bf16 v[116:119], v[108:111], a[212:215], v[116:119]\n" \
    "v_mfma_f32_16x16x32_bf16 v[120:123], v[64:67], a[216:219], 0\n" \
    "ds_read_b64 v[52:53], %[l16] offset:8200\n" \
    "buffer_load_dword v184, %[one], %[ws], %[vflag] offen sc1\n" \
    "v_mfma_f32_16x16x32_bf16 v[124:127], v[68:71], a[216:219], 0\n" \
    "ds_read_b64 v[54:55], %[l16] offset:9224\n" \
    "s_nop 0\n" \
    "v_mfma_f32_16x16x32_bf16 v[120:123], v[72:75], a[220:223], v[120:123]\n" \
    "s_waitcnt lgkmcnt(8)\n" \
    "ds_read_b64 v[56:57], %[l16] offset:10240\n" \
    "v_cvt_pk_bf16_f32 v160, v112, v113\n" \
    "v_mfma_f32_16x16x32_bf16 v[124:127], v[76:79], a[220:223], v[124:127]\n" \
    "ds_read_b64 v[58:59], %[l16] offset:11264\n" \
    "v_cvt_pk_bf16_f32 v161, v116, v117\n" \
    "v_mfma_f32_16x16x32_bf16 v[120:123], v[80:83], a[224:227], v[120:123]\n" \
    "ds_read_b64 v[60:61], %[l16] offset:10248\n" \
    "v_cvt_pk_bf16_f32 v162, v114, v115\n" \
    "v_mfma_f32_16x16x32_bf16 v[124:127], v[84:87], a[224:227], v[124:127]\n" \
    "ds_read_b64 v[62:63], %[l16] offset:11272\n" \
    "v_cvt_pk_bf16_f32 v163, v118, v119\n" \
    "v_mfma_f32_16x16x32_bf16 v[120:123], v[88:91], a[228:231], v[120:123]\n" \
    "s_add_u32 s61, s62, 524288\n" \
    "s_lshr_b32 s79, s61, 8\n" \
    "v_mfma_f32_16x16x32_bf16 v[124:127], v[92:95], a[228:231], v[124:127]\n" \
    "s_andn2_b32 s79, 0x2000, s79\n" \
    "s_xor_b32 s79, s79, %[par]\n" \
    "v_mfma_f32_16x16x32_bf16 v[120:123], v[96:99], a[232:235], v[120:123]\n" \
    "s_add_u32 s61, s61, s79\n" \
    "buffer_store_dwordx4 v[160:163], %[l16], %[ws], s61 offen sc1\n" \
    "v_mfma_f32_16x16x32_bf16 v[124:127], v[100:103], a[232:235], v[124:127]\n" \
    "v_mfma_f32_16x16x32_bf16 v[120:123], v[104:107], a[236:239], v[120:123]\n" \
    "v_mfma_f32_16x16x32_bf16 v[124:127], v[108:111], a[236:239], v[124:127]\n" \
    "s_add_u32 s60, s60, 1\n" \
    "s_waitcnt lgkmcnt(0)\n" \
    "s_cmp_lt_u32 s60, 64\n" \
    "s_cbranch_scc1 Lo_loop\n" \
    "s_add_u32 s61, s62, 589824\n" \
    "s_lshr_b32 s79, s61, 8\n" \
    "s_andn2_b32 s79, 0x2000, s79\n" \
    "s_xor_b32 s79, s79, %[par]\n" \
    "s_add_u32 s61, s61, s79\n" \
    "v_cvt_pk_bf16_f32 v164, v120, v121\n" \
    "v_cvt_pk_bf16_f32 v165, v124, v125\n" \
    "v_cvt_pk_bf16_f32 v166, v122, v123\n" \
    "v_cvt_pk_bf16_f32 v167, v126, v127\n" \
    "buffer_store_dwordx4 v[164:167], %[l16], %[ws], s61 offen sc1\n" \
    "s_waitcnt vmcnt(0)\n" \
    "s_barrier\n" \
    "s_cmp_lg_u32 %[wave], 0\n" \
    "s_cbranch_scc1 Lo_pubz\n" \
    "v_mov_b32 v180, s60\n" \
    "buffer_store_dword v180, %[one], %[ws], %[flag] offen sc1\n" \
    "Lo_pubz:\n" \
    "s_waitcnt vmcnt(0)\n" \
    "s_branch Lo_end\n" \
    "Lo_tmo:\n" \
    "s_add_u32 s78, s60, 1792\n" \
    "v_mov_b32 v180, s78\n" \
    "v_mov_b32 v181, 0\n" \
    "s_mov_b32 s78, 253757696\n" \
    "s_nop 4\n" \
    "buffer_store_dword v180, v181, %[ws], s78 offen sc1\n" \
    "s_endpgm\n" \
    "Lo_end:\n"
#define ASM_Q \
    "s_mul_i32 s70, %[wave], 10240\n" \
    "s_add_u32 s63, s70, 250609664\n" \
    "s_add_u32 m0, s70, 0\n" \
    "s_nop 0\n" \
    "buffer_load_dwordx4 %[l16], %[ws], s63 offen lds\n" \
    "buffer_load_dwordx4 %[l16], %[ws], s63 offen offset:1024 lds\n" \
    "buffer_load_dwordx4 %[l16], %[ws], s63 offen offset:2048 lds\n" \
    "buffer_load_dwordx4 %[l16], %[ws], s63 offen offset:3072 lds\n" \
    "s_add_u32 m0, s70, 4096\n" \
    "s_add_u32 s63, s63, 4096\n" \
    "buffer_load_dwordx4 %[l16], %[ws], s63 offen lds\n" \
    "buffer_load_dwordx4 %[l16], %[ws], s63 offen offset:1024 lds\n" \
    "buffer_load_dwordx4 %[l16], %[ws], s63 offen offset:2048 lds\n" \
    "buffer_load_dwordx4 %[l16], %[ws], s63 offen offset:3072 lds\n" \
    "s_add_u32 m0, s70, 8192\n" \
    "s_add_u32 s63, s63, 4096\n" \
    "buffer_load_dwordx4 %[l16], %[ws], s63 offen lds\n" \
    "buffer_load_dwordx4 %[l16], %[ws], s63 offen offset:1024 lds\n" \
    "s_mul_i32 s71, %[wave], 3072\n" \
    "v_add_u32 v112, 65536, %[tr0]\n" \
    "v_add_u32 v113, 65536, %[tr1]\n" \
    "v_add_u32 v114, 65536, %[tr2]\n" \
    "v_add_u32 v115, 98304, %[tr0]\n" \
    "v_add_u32 v116, 98304, %[tr1]\n" \
    "v_add_u32 v117, 98304, %[tr2]\n" \
    "s_mov_b32 s61, 0\n" \
    "s_add_u32 m0, s71, 40960\n" \
    "s_nop 0\n" \
    "buffer_load_dwordx4 %[src0], %[w0], s61 offen lds\n" \
    "s_add_u32 m0, s71, 41984\n" \
    "s_nop 0\n" \
    "buffer_load_dwordx4 %[src1], %[w0], s61 offen lds\n" \
    "s_add_u32 m0, s71, 43008\n" \
    "s_nop 0\n" \
    "buffer_load_dwordx4 %[src2], %[w0], s61 offen lds\n" \
    "s_mov_b32 s61, 393216\n" \
    "s_add_u32 m0, s71, 53248\n" \
    "s_nop 0\n" \
    "buffer_load_dwordx4 %[src0], %[w0], s61 offen lds\n" \
    "s_add_u32 m0, s71, 54272\n" \
    "s_nop 0\n" \
    "buffer_load_dwordx4 %[src1], %[w0], s61 offen lds\n" \
    "s_add_u32 m0, s71, 55296\n" \
    "s_nop 0\n" \
    "buffer_load_dwordx4 %[src2], %[w0], s61 offen lds\n" \
    "s_mov_b32 s61, 786432\n" \
    "s_add_u32 m0, s71, 65536\n" \
    "s_nop 0\n" \
    "buffer_load_dwordx4 %[src0], %[w0], s61 offen lds\n" \
    "s_add_u32 m0, s71, 66560\n" \
    "s_nop 0\n" \
    "buffer_load_dwordx4 %[src1], %[w0], s61 offen lds\n" \
    "s_add_u32 m0, s71, 67584\n" \
    "s_nop 0\n" \
    "buffer_load_dwordx4 %[src2], %[w0], s61 offen lds\n" \
    "s_mov_b32 s61, 1179648\n" \
    "s_add_u32 m0, s71, 77824\n" \
    "s_nop 0\n" \
    "buffer_load_dwordx4 %[src0], %[w0], s61 offen lds\n" \
    "s_add_u32 m0, s71, 78848\n" \
    "s_nop 0\n" \
    "buffer_load_dwordx4 %[src1], %[w0], s61 offen lds\n" \
    "s_add_u32 m0, s71, 79872\n" \
    "s_nop 0\n" \
    "buffer_load_dwordx4 %[src2], %[w0], s61 offen lds\n" \
    "s_mov_b32 s61, 1572864\n" \
    "s_add_u32 m0, s71, 90112\n" \
    "s_nop 0\n" \
    "buffer_load_dwordx4 %[src0], %[w0], s61 offen lds\n" \
    "s_add_u32 m0, s71, 91136\n" \
    "s_nop 0\n" \
    "buffer_load_dwordx4 %[src1], %[w0], s61 offen lds\n" \
    "s_add_u32 m0, s71, 92160\n" \
    "s_nop 0\n" \
    "buffer_load_dwordx4 %[src2], %[w0], s61 offen lds\n" \
    "s_mov_b32 s61, 1966080\n" \
    "s_add_u32 m0, s71, 102400\n" \
    "s_nop 0\n" \
    "buffer_load_dwordx4 %[src0], %[w0], s61 offen lds\n" \
    "s_add_u32 m0, s71, 103424\n" \
    "s_nop 0\n" \
    "buffer_load_dwordx4 %[src1], %[w0], s61 offen lds\n" \
    "s_add_u32 m0, s71, 104448\n" \
    "s_nop 0\n" \
    "buffer_load_dwordx4 %[src2], %[w0], s61 offen lds\n" \
    "s_mov_b32 s61, 2359296\n" \
    "s_add_u32 m0, s71, 114688\n" \
    "s_nop 0\n" \
    "buffer_load_dwordx4 %[src0], %[w0], s61 offen lds\n" \
    "s_add_u32 m0, s71, 115712\n" \
    "s_nop 0\n" \
    "buffer_load_dwordx4 %[src1], %[w0], s61 offen lds\n" \
    "s_add_u32 m0, s71, 116736\n" \
    "s_nop 0\n" \
    "buffer_load_dwordx4 %[src2], %[w0], s61 offen lds\n" \
    "s_mov_b32 s61, 2752512\n" \
    "s_add_u32 m0, s71, 126976\n" \
    "s_nop 0\n" \
    "buffer_load_dwordx4 %[src0], %[w0], s61 offen lds\n" \
    "s_add_u32 m0, s71, 128000\n" \
    "s_nop 0\n" \
    "buffer_load_dwordx4 %[src1], %[w0], s61 offen lds\n" \
    "s_add_u32 m0, s71, 129024\n" \
    "s_nop 0\n" \
    "buffer_load_dwordx4 %[src2], %[w0], s61 offen lds\n" \
    "s_mov_b32 s61, 3145728\n" \
    "s_add_u32 m0, s71, 139264\n" \
    "s_nop 0\n" \
    "buffer_load_dwordx4 %[src0], %[w0], s61 offen lds\n" \
    "s_add_u32 m0, s71, 140288\n" \
    "s_nop 0\n" \
    "buffer_load_dwordx4 %[src1], %[w0], s61 offen lds\n" \
    "s_add_u32 m0, s71, 141312\n" \
    "s_nop 0\n" \
    "buffer_load_dwordx4 %[src2], %[w0], s61 offen lds\n" \
    "s_mov_b32 s61, 3538944\n" \
    "s_add_u32 m0, s71, 151552\n" \
    "s_nop 0\n" \
    "buffer_load_dwordx4 %[src0], %[w0], s61 offen lds\n" \
    "s_add_u32 m0, s71, 152576\n" \
    "s_nop 0\n" \
    "buffer_load_dwordx4 %[src1], %[w0], s61 offen lds\n" \
    "s_add_u32 m0, s71, 153600\n" \
    "s_nop 0\n" \
    "buffer_load_dwordx4 %[src2], %[w0], s61 offen lds\n" \
    "s_waitcnt vmcnt(24)\n" \
    "s_barrier\n" \
    "ds_read_b64_tr_b16 a[0:1], %[tr0] offset:40960\n" \
    "ds_read_b64_tr_b16 a[2:3], %[tr0] offset:42496\n" \
    "ds_read_b64_tr_b16 a[80:81], %[tr1] offset:40960\n" \
    "ds_read_b64_tr_b16 a[82:83], %[tr1] offset:42496\n" \
    "ds_read_b64_tr_b16 a[160:161], %[tr2] offset:40960\n" \
    "ds_read_b64_tr_b16 a[162:163], %[tr2] offset:42496\n" \
    "ds_read_b64_tr_b16 a[4:5], %[tr0] offset:53248\n" \
    "ds_read_b64_tr_b16 a[6:7], %[tr0] offset:54784\n" \
    "ds_read_b64_tr_b16 a[84:85], %[tr1] offset:53248\n" \
    "ds_read_b64_tr_b16 a[86:87], %[tr1] offset:54784\n" \
    "ds_read_b64_tr_b16 a[164:165], %[tr2] offset:53248\n" \
    "ds_read_b64_tr_b16 a[166:167], %[tr2] offset:54784\n" \
    "s_waitcnt lgkmcnt(0)\n" \
    "s_waitcnt vmcnt(18)\n" \
    "s_barrier\n" \
    "s_mov_b32 s61, 3932160\n" \
    "s_add_u32 m0, s71, 40960\n" \
    "s_nop 0\n" \
    "buffer_load_dwordx4 %[src0], %[w0], s61 offen lds\n" \
    "s_add_u32 m0, s71, 41984\n" \
    "s_nop 0\n" \
    "buffer_load_dwordx4 %[src1], %[w0], s61 offen lds\n" \
    "s_add_u32 m0, s71, 43008\n" \
    "s_nop 0\n" \
    "buffer_load_dwordx4 %[src2], %[w0], s61 offen lds\n" \
    "s_mov_b32 s61, 4325376\n" \
    "s_add_u32 m0, s71, 53248\n" \
    "s_nop 0\n" \
    "buffer_load_dwordx4 %[src0], %[w0], s61 offen lds\n" \
    "s_add_u32 m0, s71, 54272\n" \
    "s_nop 0\n" \
    "buffer_load_dwordx4 %[src1], %[w0], s61 offen lds\n" \
    "s_add_u32 m0, s71, 55296\n" \
    "s_nop 0\n" \
    "buffer_load_dwordx4 %[src2], %[w0], s61 offen lds\n" \
    "ds_read_b64_tr_b16 a[8:9], v112\n" \
    "ds_read_b64_tr_b16 a[10:11], v112 offset:1536\n" \
    "ds_read_b64_tr_b16 a[88:89], v113\n" \
    "ds_read_b64_tr_b16 a[90:91], v113 offset:1536\n" \
    "ds_read_b64_tr_b16 a[168:169], v114\n" \
    "ds_read_b64_tr_b16 a[170:171], v114 offset:1536\n" \
    "ds_read_b64_tr_b16 a[12:13], v112 offset:12288\n" \
    "ds_read_b64_tr_b16 a[14:15], v112 offset:13824\n" \
    "ds_read_b64_tr_b16 a[92:93], v113 offset:12288\n" \
    "ds_read_b64_tr_b16 a[94:95], v113 offset:13824\n" \
    "ds_read_b64_tr_b16 a[172:173], v114 offset:12288\n" \
    "ds_read_b64_tr_b16 a[174:175], v114 offset:13824\n" \
    "s_waitcnt lgkmcnt(0)\n" \
    "s_waitcnt vmcnt(18)\n" \
    "s_barrier\n" \
    "s_mov_b32 s61, 4718592\n" \
    "s_add_u32 m0, s71, 65536\n" \
    "s_nop 0\n" \
    "buffer_load_dwordx4 %[src0], %[w0], s61 offen lds\n" \
    "s_add_u32 m0, s71, 66560\n" \
    "s_nop 0\n" \
    "buffer_load_dwordx4 %[src1], %[w0], s61 offen lds\n" \
    "s_add_u32 m0, s71, 67584\n" \
    "s_nop 0\n" \
    "buffer_load_dwordx4 %[src2], %[w0], s61 offen lds\n" \
    "s_mov_b32 s61, 5111808\n" \
    "s_add_u32 m0, s71, 77824\n" \
    "s_nop 0\n" \
    "buffer_load_dwordx4 %[src0], %[w0], s61 offen lds\n" \
    "s_add_u32 m0, s71, 78848\n" \
    "s_nop 0\n" \
    "buffer_load_dwordx4 %[src1], %[w0], s61 offen lds\n" \
    "s_add_u32 m0, s71, 79872\n" \
    "s_nop 0\n" \
    "buffer_load_dwordx4 %[src2], %[w0], s61 offen lds\n" \
    "ds_read_b64_tr_b16 a[16:17], v112 offset:24576\n" \
    "ds_read_b64_tr_b16 a[18:19], v112 offset:26112\n" \
    "ds_read_b64_tr_b16 a[96:97], v113 offset:24576\n" \
    "ds_read_b64_tr_b16 a[98:99], v113 offset:26112\n" \
    "ds_read_b64_tr_b16 a[176:177], v114 offset:24576\n" \
    "ds_read_b64_tr_b16 a[178:179], v114 offset:26112\n" \
    "ds_read_b64_tr_b16 a[20:21], v112 offset:36864\n" \
    "ds_read_b64_tr_b16 a[22:23], v112 offset:38400\n" \
    "ds_read_b64_tr_b16 a[100:101], v113 offset:36864\n" \
    "ds_read_b64_tr_b16 a[102:103], v113 offset:38400\n" \
    "ds_read_b64_tr_b16 a[180:181], v114 offset:36864\n" \
    "ds_read_b64_tr_b16 a[182:183], v114 offset:38400\n" \
    "s_waitcnt lgkmcnt(0)\n" \
    "s_waitcnt vmcnt(18)\n" \
    "s_barrier\n" \
    "s_mov_b32 s61, 5505024\n" \
    "s_add_u32 m0, s71, 90112\n" \
    "s_nop 0\n" \
    "buffer_load_dwordx4 %[src0], %[w0], s61 offen lds\n" \
    "s_add_u32 m0, s71, 91136\n" \
    "s_nop 0\n" \
    "buffer_load_dwordx4 %[src1], %[w0], s61 offen lds\n" \
    "s_add_u32 m0, s71, 92160\n" \
    "s_nop 0\n" \
    "buffer_load_dwordx4 %[src2], %[w0], s61 offen lds\n" \
    "s_mov_b32 s61, 5898240\n" \
    "s_add_u32 m0, s71, 102400\n" \
    "s_nop 0\n" \
    "buffer_load_dwordx4 %[src0], %[w0], s61 offen lds\n" \
    "s_add_u32 m0, s71, 103424\n" \
    "s_nop 0\n" \
    "buffer_load_dwordx4 %[src1], %[w0], s61 offen lds\n" \
    "s_add_u32 m0, s71, 104448\n" \
    "s_nop 0\n" \
    "buffer_load_dwordx4 %[src2], %[w0], s61 offen lds\n" \
    "ds_read_b64_tr_b16 a[24:25], v112 offset:49152\n" \
    "ds_read_b64_tr_b16 a[26:27], v112 offset:50688\n" \
    "ds_read_b64_tr_b16 a[104:105], v113 offset:49152\n" \
    "ds_read_b64_tr_b16 a[106:107], v113 offset:50688\n" \
    "ds_read_b64_tr_b16 a[184:185], v114 offset:49152\n" \
    "ds_read_b64_tr_b16 a[186:187], v114 offset:50688\n" \
    "ds_read_b64_tr_b16 a[28:29], v112 offset:61440\n" \
    "ds_read_b64_tr_b16 a[30:31], v112 offset:62976\n" \
    "ds_read_b64_tr_b16 a[108:109], v113 offset:61440\n" \
    "ds_read_b64_tr_b16 a[110:111], v113 offset:62976\n" \
    "ds_read_b64_tr_b16 a[188:189], v114 offset:61440\n" \
    "ds_read_b64_tr_b16 a[190:191], v114 offset:62976\n" \
    "s_waitcnt lgkmcnt(0)\n" \
    "s_waitcnt vmcnt(18)\n" \
    "s_barrier\n" \
    "s_mov_b32 s61, 6291456\n" \
    "s_add_u32 m0, s71, 114688\n" \
    "s_nop 0\n" \
    "buffer_load_dwordx4 %[src0], %[w0], s61 offen lds\n" \
    "s_add_u32 m0, s71, 115712\n" \
    "s_nop 0\n" \
    "buffer_load_dwordx4 %[src1], %[w0], s61 offen lds\n" \
    "s_add_u32 m0, s71, 116736\n" \
    "s_nop 0\n" \
    "buffer_load_dwordx4 %[src2], %[w0], s61 offen lds\n" \
    "s_mov_b32 s61, 6684672\n" \
    "s_add_u32 m0, s71, 126976\n" \
    "s_nop 0\n" \
    "buffer_load_dwordx4 %[src0], %[w0], s61 offen lds\n" \
    "s_add_u32 m0, s71, 128000\n" \
    "s_nop 0\n" \
    "buffer_load_dwordx4 %[src1], %[w0], s61 offen lds\n" \
    "s_add_u32 m0, s71, 129024\n" \
    "s_nop 0\n" \
    "buffer_load_dwordx4 %[src2], %[w0], s61 offen lds\n" \
    "ds_read_b64_tr_b16 a[32:33], v115 offset:40960\n" \
    "ds_read_b64_tr_b16 a[34:35], v115 offset:42496\n" \
    "ds_read_b64_tr_b16 a[112:113], v116 offset:40960\n" \
    "ds_read_b64_tr_b16 a[114:115], v116 offset:42496\n" \
    "ds_read_b64_tr_b16 a[192:193], v117 offset:40960\n" \
    "ds_read_b64_tr_b16 a[194:195], v117 offset:42496\n" \
    "ds_read_b64_tr_b16 a[36:37], v115 offset:53248\n" \
    "ds_read_b64_tr_b16 a[38:39], v115 offset:54784\n" \
    "ds_read_b64_tr_b16 a[116:117], v116 offset:53248\n" \
    "ds_read_b64_tr_b16 a[118:119], v116 offset:54784\n" \
    "ds_read_b64_tr_b16 a[196:197], v117 offset:53248\n" \
    "ds_read_b64_tr_b16 a[198:199], v117 offset:54784\n" \
    "s_waitcnt lgkmcnt(0)\n" \
    "s_waitcnt vmcnt(18)\n" \
    "s_barrier\n" \
    "s_mov_b32 s61, 7077888\n" \
    "s_add_u32 m0, s71, 139264\n" \
    "s_nop 0\n" \
    "buffer_load_dwordx4 %[src0], %[w0], s61 offen lds\n" \
    "s_add_u32 m0, s71, 140288\n" \
    "s_nop 0\n" \
    "buffer_load_dwordx4 %[src1], %[w0], s61 offen lds\n" \
    "s_add_u32 m0, s71, 141312\n" \
    "s_nop 0\n" \
    "buffer_load_dwordx4 %[src2], %[w0], s61 offen lds\n" \
    "s_mov_b32 s61, 7471104\n" \
    "s_add_u32 m0, s71, 151552\n" \
    "s_nop 0\n" \
    "buffer_load_dwordx4 %[src0], %[w0], s61 offen lds\n" \
    "s_add_u32 m0, s71, 152576\n" \
    "s_nop 0\n" \
    "buffer_load_dwordx4 %[src1], %[w0], s61 offen lds\n" \
    "s_add_u32 m0, s71, 153600\n" \
    "s_nop 0\n" \
    "buffer_load_dwordx4 %[src2], %[w0], s61 offen lds\n" \
    "ds_read_b64_tr_b16 a[40:41], %[tr0] offset:40960\n" \
    "ds_read_b64_tr_b16 a[42:43], %[tr0] offset:42496\n" \
    "ds_read_b64_tr_b16 a[120:121], %[tr1] offset:40960\n" \
    "ds_read_b64_tr_b16 a[122:123], %[tr1] offset:42496\n" \
    "ds_read_b64_tr_b16 a[200:201], %[tr2] offset:40960\n" \
    "ds_read_b64_tr_b16 a[202:203], %[tr2] offset:42496\n" \
    "ds_read_b64_tr_b16 a[44:45], %[tr0] offset:53248\n" \
    "ds_read_b64_tr_b16 a[46:47], %[tr0] offset:54784\n" \
    "ds_read_b64_tr_b16 a[124:125], %[tr1] offset:53248\n" \
    "ds_read_b64_tr_b16 a[126:127], %[tr1] offset:54784\n" \
    "ds_read_b64_tr_b16 a[204:205], %[tr2] offset:53248\n" \
    "ds_read_b64_tr_b16 a[206:207], %[tr2] offset:54784\n" \
    "s_waitcnt lgkmcnt(0)\n" \
    "s_waitcnt vmcnt(18)\n" \
    "s_barrier\n" \
    "ds_read_b64_tr_b16 a[48:49], v112\n" \
    "ds_read_b64_tr_b16 a[50:51], v112 offset:1536\n" \
    "ds_read_b64_tr_b16 a[128:129], v113\n" \
    "ds_read_b64_tr_b16 a[130:131], v113 offset:1536\n" \
    "ds_read_b64_tr_b16 a[208:209], v114\n" \
    "ds_read_b64_tr_b16 a[210:211], v114 offset:1536\n" \
    "ds_read_b64_tr_b16 a[52:53], v112 offset:12288\n" \
    "ds_read_b64_tr_b16 a[54:55], v112 offset:13824\n" \
    "ds_read_b64_tr_b16 a[132:133], v113 offset:12288\n" \
    "ds_read_b64_tr_b16 a[134:135], v113 offset:13824\n" \
    "ds_read_b64_tr_b16 a[212:213], v114 offset:12288\n" \
    "ds_read_b64_tr_b16 a[214:215], v114 offset:13824\n" \
    "s_waitcnt lgkmcnt(0)\n" \
    "s_waitcnt vmcnt(12)\n" \
    "s_barrier\n" \
    "ds_read_b64_tr_b16 a[56:57], v112 offset:24576\n" \
    "ds_read_b64_tr_b16 a[58:59], v112 offset:26112\n" \
    "ds_read_b64_tr_b16 a[136:137], v113 offset:24576\n" \
    "ds_read_b64_tr_b16 a[138:139], v113 offset:26112\n" \
    "ds_read_b64_tr_b16 a[216:217], v114 offset:24576\n" \
    "ds_read_b64_tr_b16 a[218:219], v114 offset:26112\n" \
    "ds_read_b64_tr_b16 a[60:61], v112 offset:36864\n" \
    "ds_read_b64_tr_b16 a[62:63], v112 offset:38400\n" \
    "ds_read_b64_tr_b16 a[140:141], v113 offset:36864\n" \
    "ds_read_b64_tr_b16 a[142:143], v113 offset:38400\n" \
    "ds_read_b64_tr_b16 a[220:221], v114 offset:36864\n" \
    "ds_read_b64_tr_b16 a[222:223], v114 offset:38400\n" \
    "s_waitcnt lgkmcnt(0)\n" \
    "s_waitcnt vmcnt(6)\n" \
    "s_barrier\n" \
    "ds_read_b64_tr_b16 a[64:65], v112 offset:49152\n" \
    "ds_read_b64_tr_b16 a[66:67], v112 offset:50688\n" \
    "ds_read_b64_tr_b16 a[144:145], v113 offset:49152\n" \
    "ds_read_b64_tr_b16 a[146:147], v113 offset:50688\n" \
    "ds_read_b64_tr_b16 a[224:225], v114 offset:49152\n" \
    "ds_read_b64_tr_b16 a[226:227], v114 offset:50688\n" \
    "ds_read_b64_tr_b16 a[68:69], v112 offset:61440\n" \
    "ds_read_b64_tr_b16 a[70:71], v112 offset:62976\n" \
    "ds_read_b64_tr_b16 a[148:149], v113 offset:61440\n" \
    "ds_read_b64_tr_b16 a[150:151], v113 offset:62976\n" \
    "ds_read_b64_tr_b16 a[228:229], v114 offset:61440\n" \
    "ds_read_b64_tr_b16 a[230:231], v114 offset:62976\n" \
    "s_waitcnt lgkmcnt(0)\n" \
    "s_waitcnt vmcnt(0)\n" \
    "s_barrier\n" \
    "ds_read_b64_tr_b16 a[72:73], v115 offset:40960\n" \
    "ds_read_b64_tr_b16 a[74:75], v115 offset:42496\n" \
    "ds_read_b64_tr_b16 a[152:153], v116 offset:40960\n" \
    "ds_read_b64_tr_b16 a[154:155], v116 offset:42496\n" \
    "ds_read_b64_tr_b16 a[232:233], v117 offset:40960\n" \
    "ds_read_b64_tr_b16 a[234:235], v117 offset:42496\n" \
    "ds_read_b64_tr_b16 a[76:77], v115 offset:53248\n" \
    "ds_read_b64_tr_b16 a[78:79], v115 offset:54784\n" \
    "ds_read_b64_tr_b16 a[156:157], v116 offset:53248\n" \
    "ds_read_b64_tr_b16 a[158:159], v116 offset:54784\n" \
    "ds_read_b64_tr_b16 a[236:237], v117 offset:53248\n" \
    "ds_read_b64_tr_b16 a[238:239], v117 offset:54784\n" \
    "s_waitcnt lgkmcnt(0)\n" \
    "s_barrier\n" \
    "s_add_u32 s72, s70, 40960\n" \
    "s_add_u32 s63, s70, 250650624\n" \
    "s_add_u32 m0, s72, 0\n" \
    "s_nop 0\n" \
    "buffer_load_dwordx4 %[l16], %[ws], s63 offen lds\n" \
    "buffer_load_dwordx4 %[l16], %[ws], s63 offen offset:1024 lds\n" \
    "buffer_load_dwordx4 %[l16], %[ws], s63 offen offset:2048 lds\n" \
    "buffer_load_dwordx4 %[l16], %[ws], s63 offen offset:3072 lds\n" \
    "s_add_u32 m0, s72, 4096\n" \
    "s_add_u32 s63, s63, 4096\n" \
    "buffer_load_dwordx4 %[l16], %[ws], s63 offen lds\n" \
    "buffer_load_dwordx4 %[l16], %[ws], s63 offen offset:1024 lds\n" \
    "buffer_load_dwordx4 %[l16], %[ws], s63 offen offset:2048 lds\n" \
    "buffer_load_dwordx4 %[l16], %[ws], s63 offen offset:3072 lds\n" \
    "s_add_u32 m0, s72, 8192\n" \
    "s_add_u32 s63, s63, 4096\n" \
    "buffer_load_dwordx4 %[l16], %[ws], s63 offen lds\n" \
    "buffer_load_dwordx4 %[l16], %[ws], s63 offen offset:1024 lds\n" \
    "ds_read_b128 v[16:19], %[l16]\n" \
    "ds_read_b128 v[20:23], %[l16] offset:20480\n" \
    "ds_read_b128 v[24:27], %[l16] offset:1024\n" \
    "ds_read_b128 v[28:31], %[l16] offset:21504\n" \
    "s_mov_b32 s60, 0\n" \
    "s_mov_b32 s64, 0\n" \
    "v_mov_b32 v109, %[l16]\n" \
    "s_mov_b32 s69, 0x3dd53b95\n" \
    "s_mov_b32 s74, 0xbdd53b95\n" \
    "s_mov_b32 s66, 0\n" \
    "v_mov_b32 v147, 0xff800000\n" \
    "v_mov_b32 v111, 1\n" \
    "s_mov_b32 s75, 227540992\n" \
    "v_mov_b32 v140, 0\n" \
    "v_mov_b32 v141, 0\n" \
    "v_mov_b32 v142, 0\n" \
    "v_mov_b32 v143, 0\n" \
    "v_mov_b32 v144, 0\n" \
    "v_mov_b32 v145, 0\n" \
    "s_waitcnt lgkmcnt(0)\n" \
    "Lq_loop:\n" \
    "s_nop 1\n" \
    "v_mov_b32 v108, v109\n" \
    "s_add_u32 s64, s64, 40960\n" \
    "s_cmp_eq_u32 s64, 122880\n" \
    "s_cselect_b32 s64, 0, s64\n" \
    "v_add_u32 v109, s64, %[l16]\n" \
    "s_add_u32 s65, s64, 40960\n" \
    "s_cmp_eq_u32 s65, 122880\n" \
    "s_cselect_b32 s65, 0, s65\n" \
    "s_add_u32 s65, s65, s70\n" \
    "s_min_u32 s63, s60, 61\n" \
    "s_mul_i32 s63, s63, 40960\n" \
    "s_add_u32 s63, s63, s70\n" \
    "s_add_u32 s63, s63, 250691584\n" \
    "s_waitcnt vmcnt(0)\n" \
    "s_barrier\n" \
    "s_cmp_lg_u32 %[wave], 0\n" \
    "s_cbranch_scc1 Lq_sig\n" \
    "v_mov_b32 v111, s60\n" \
    "buffer_store_dword v111, %[one], %[ws], %[qflag] offen sc1\n" \
    "Lq_sig:\n" \
    "s_cmp_lt_u32 s60, s66\n" \
    "s_cbranch_scc0 Lq_kw\n" \
    "Lq_kok:\n" \
    "v_mfma_f32_16x16x32_bf16 v[48:51], a[0:3], v[16:19], 0\n" \
    "v_mfma_f32_16x16x16_bf16 v[120:123], v[72:73], v[84:85], 0\n" \
    "ds_read_b128 v[32:35], v108 offset:2048\n" \
    "v_mfma_f32_16x16x32_bf16 v[52:55], a[80:83], v[16:19], 0\n" \
    "v_mfma_f32_16x16x16_bf16 v[124:127], v[72:73], v[90:91], 0\n" \
    "ds_read_b128 v[36:39], v108 offset:22528\n" \
    "v_mfma_f32_16x16x32_bf16 v[56:59], a[160:163], v[16:19], 0\n" \
    "v_mfma_f32_16x16x16_bf16 v[128:131], v[74:75], v[90:91], 0\n" \
    "s_add_u32 m0, s65, 0\n" \
    "s_nop 0\n" \
    "buffer_load_dwordx4 %[l16], %[ws], s63 offen lds\n" \
    "v_mfma_f32_16x16x32_bf16 v[60:63], a[0:3], v[20:23], 0\n" \
    "v_mfma_f32_16x16x16_bf16 v[120:123], v[76:77], v[86:87], v[120:123]\n" \
    "v_mfma_f32_16x16x32_bf16 v[64:67], a[80:83], v[20:23], 0\n" \
    "v_mfma_f32_16x16x16_bf16 v[124:127], v[76:77], v[92:93], v[124:127]\n" \
    "buffer_load_dwordx4 %[l16], %[ws], s63 offen offset:1024 lds\n" \
    "v_mfma_f32_16x16x32_bf16 v[68:71], a[160:163], v[20:23], 0\n" \
    "v_mfma_f32_16x16x16_bf16 v[128:131], v[78:79], v[92:93], v[128:131]\n" \
    "v_mfma_f32_16x16x32_bf16 v[48:51], a[4:7], v[24:27], v[48:51]\n" \
    "v_mfma_f32_16x16x16_bf16 v[120:123], v[80:81], v[88:89], v[120:123]\n" \
    "ds_read_b128 v[40:43], v108 offset:3072\n" \
    "v_mfma_f32_16x16x32_bf16 v[52:55], a[84:87], v[24:27], v[52:55]\n" \
    "v_mfma_f32_16x16x16_bf16 v[124:127], v[80:81], v[94:95], v[124:127]\n" \
    "ds_read_b128 v[44:47], v108 offset:23552\n" \
    "v_mfma_f32_16x16x32_bf16 v[56:59], a[164:167], v[24:27], v[56:59]\n" \
    "v_mfma_f32_16x16x16_bf16 v[128:131], v[82:83], v[94:95], v[128:131]\n" \
    "buffer_load_dwordx4 %[l16], %[ws], s63 offen offset:2048 lds\n" \
    "v_mfma_f32_16x16x32_bf16 v[60:63], a[4:7], v[28:31], v[60:63]\n" \
    "ds_write_b128 %[xw], v[120:123]\n" \
    "v_mfma_f32_16x16x32_bf16 v[64:67], a[84:87], v[28:31], v[64:67]\n" \
    "buffer_load_dwordx4 %[l16], %[ws], s63 offen offset:3072 lds\n" \
    "v_mfma_f32_16x16x32_bf16 v[68:71], a[164:167], v[28:31], v[68:71]\n" \
    "ds_write_b128 %[xw], v[124:127] offset:2816\n" \
    "s_waitcnt lgkmcnt(5)\n" \
    "v_mfma_f32_16x16x32_bf16 v[48:51], a[8:11], v[32:35], v[48:51]\n" \
    "ds_write_b128 %[xw], v[128:131] offset:2880\n" \
    "ds_read_b128 v[16:19], v108 offset:4096\n" \
    "v_mfma_f32_16x16x32_bf16 v[52:55], a[88:91], v[32:35], v[52:55]\n" \
    "ds_read_b128 v[20:23], v108 offset:24576\n" \
    "v_mfma_f32_16x16x32_bf16 v[56:59], a[168:171], v[32:35], v[56:59]\n" \
    "s_add_u32 m0, s65, 4096\n" \
    "s_add_u32 s63, s63, 4096\n" \
    "buffer_load_dwordx4 %[l16], %[ws], s63 offen lds\n" \
    "s_waitcnt lgkmcnt(7)\n" \
    "v_mfma_f32_16x16x32_bf16 v[60:63], a[8:11], v[36:39], v[60:63]\n" \
    "v_mfma_f32_16x16x32_bf16 v[64:67], a[88:91], v[36:39], v[64:67]\n" \
    "buffer_load_dwordx4 %[l16], %[ws], s63 offen offset:1024 lds\n" \
    "v_mfma_f32_16x16x32_bf16 v[68:71], a[168:171], v[36:39], v[68:71]\n" \
    "s_waitcnt lgkmcnt(6)\n" \
    "v_mfma_f32_16x16x32_bf16 v[48:51], a[12:15], v[40:43], v[48:51]\n" \
    "ds_read_b128 v[24:27], v108 offset:5120\n" \
    "v_mfma_f32_16x16x32_bf16 v[52:55], a[92:95], v[40:43], v[52:55]\n" \
    "ds_read_b128 v[28:31], v108 offset:25600\n" \
    "v_mfma_f32_16x16x32_bf16 v[56:59], a[172:175], v[40:43], v[56:59]\n" \
    "buffer_load_dwordx4 %[l16], %[ws], s63 offen offset:2048 lds\n" \
    "s_waitcnt lgkmcnt(7)\n" \
    "v_mfma_f32_16x16x32_bf16 v[60:63], a[12:15], v[44:47], v[60:63]\n" \
    "v_mfma_f32_16x16x32_bf16 v[64:67], a[92:95], v[44:47], v[64:67]\n" \
    "buffer_load_dwordx4 %[l16], %[ws], s63 offen offset:3072 lds\n" \
    "v_mfma_f32_16x16x32_bf16 v[68:71], a[172:175], v[44:47], v[68:71]\n" \
    "s_mov_b32 s67, 1048576\n" \
    "Lq_kca:\n" \
    "v_max3_u32 v146, v140, v141, v142\n" \
    "v_max3_u32 v146, v146, v143, v144\n" \
    "v_max_u32 v146, v146, v145\n" \
    "v_cmp_eq_u32 vcc, -1, v146\n" \
    "s_cbranch_vccnz Lq_kra\n" \
    "s_waitcnt lgkmcnt(3)\n" \
    "v_mfma_f32_16x16x32_bf16 v[48:51], a[16:19], v[16:19], v[48:51]\n" \
    "v_mfma_f32_16x16x16_bf16 v[132:135], v[140:141], v[84:85], 0\n" \
    "ds_read_b128 v[32:35], v108 offset:6144\n" \
    "v_mfma_f32_16x16x32_bf16 v[52:55], a[96:99], v[16:19], v[52:55]\n" \
    "v_mfma_f32_16x16x16_bf16 v[136:139], v[140:141], v[90:91], 0\n" \
    "ds_read_b128 v[36:39], v108 offset:26624\n" \
    "v_mfma_f32_16x16x32_bf16 v[56:59], a[176:179], v[16:19], v[56:59]\n" \
    "v_mfma_f32_16x16x16_bf16 v[132:135], v[142:143], v[86:87], v[132:135]\n" \
    "s_add_u32 m0, s65, 8192\n" \
    "s_add_u32 s63, s63, 4096\n" \
    "buffer_load_dwordx4 %[l16], %[ws], s63 offen lds\n" \
    "s_waitcnt lgkmcnt(4)\n" \
    "v_mfma_f32_16x16x32_bf16 v[60:63], a[16:19], v[20:23], v[60:63]\n" \
    "v_mfma_f32_16x16x16_bf16 v[136:139], v[142:143], v[92:93], v[136:139]\n" \
    "v_mfma_f32_16x16x32_bf16 v[64:67], a[96:99], v[20:23], v[64:67]\n" \
    "v_mfma_f32_16x16x16_bf16 v[132:135], v[144:145], v[88:89], v[132:135]\n" \
    "buffer_load_dwordx4 %[l16], %[ws], s63 offen offset:1024 lds\n" \
    "v_mfma_f32_16x16x32_bf16 v[68:71], a[176:179], v[20:23], v[68:71]\n" \
    "v_mfma_f32_16x16x16_bf16 v[136:139], v[144:145], v[94:95], v[136:139]\n" \
    "s_waitcnt lgkmcnt(3)\n" \
    "v_mfma_f32_16x16x32_bf16 v[48:51], a[20:23], v[24:27], v[48:51]\n" \
    "ds_read_b128 v[40:43], v108 offset:7168\n" \
    "v_mfma_f32_16x16x32_bf16 v[52:55], a[100:103], v[24:27], v[52:55]\n" \
    "ds_read_b128 v[44:47], v108 offset:27648\n" \
    "v_mfma_f32_16x16x32_bf16 v[56:59], a[180:183], v[24:27], v[56:59]\n" \
    "ds_write_b128 %[xm], v[132:135]\n" \
    "s_waitcnt lgkmcnt(5)\n" \
    "v_mfma_f32_16x16x32_bf16 v[60:63], a[20:23], v[28:31], v[60:63]\n" \
    "ds_write_b128 %[xm], v[136:139] offset:2816\n" \
    "v_mfma_f32_16x16x32_bf16 v[64:67], a[100:103], v[28:31], v[64:67]\n" \
    "v_mfma_f32_16x16x32_bf16 v[68:71], a[180:183], v[28:31], v[68:71]\n" \
    "s_waitcnt lgkmcnt(0)\n" \
    "s_barrier\n" \
    "s_lshl_b32 s68, s60, 12\n" \
    "s_and_b32 s68, s68, 0x2000\n" \
    "s_xor_b32 s68, s68, %[par]\n" \
    "s_lshl_b32 s79, s60, 20\n" \
    "s_add_u32 s68, s68, s79\n" \
    "s_add_u32 s68, s68, %[ob]\n" \
    "s_lshl_b32 s72, s60, 10\n" \
    "s_and_b32 s72, s72, 0x2000\n" \
    "s_xor_b32 s72, s72, %[par]\n" \
    "s_lshl_b32 s79, s60, 18\n" \
    "s_add_u32 s72, s72, s79\n" \
    "s_add_u32 s72, s72, 142606336\n" \
    "v_mfma_f32_16x16x32_bf16 v[48:51], a[24:27], v[32:35], v[48:51]\n" \
    "ds_read_b64 v[148:149], %[xr]\n" \
    "ds_read_b128 v[16:19], v108 offset:8192\n" \
    "v_mfma_f32_16x16x32_bf16 v[52:55], a[104:107], v[32:35], v[52:55]\n" \
    "ds_read_b64 v[150:151], %[xr] offset:64\n" \
    "ds_read_b128 v[20:23], v108 offset:28672\n" \
    "v_mfma_f32_16x16x32_bf16 v[56:59], a[184:187], v[32:35], v[56:59]\n" \
    "ds_read_b32 v164, %[xr2]\n" \
    "ds_read_b64 v[152:153], %[xr] offset:5632\n" \
    "v_mfma_f32_16x16x32_bf16 v[60:63], a[24:27], v[36:39], v[60:63]\n" \
    "ds_read_b64 v[154:155], %[xr] offset:5696\n" \
    "ds_read_b32 v165, %[xr2] offset:5632\n" \
    "v_mfma_f32_16x16x32_bf16 v[64:67], a[104:107], v[36:39], v[64:67]\n" \
    "ds_read_b64 v[156:157], %[xr] offset:11264\n" \
    "ds_read_b64 v[158:159], %[xr] offset:11328\n" \
    "v_mfma_f32_16x16x32_bf16 v[68:71], a[184:187], v[36:39], v[68:71]\n" \
    "ds_read_b32 v166, %[xr2] offset:11264\n" \
    "ds_read_b64 v[160:161], %[xr] offset:16896\n" \
    "v_mfma_f32_16x16x32_bf16 v[48:51], a[28:31], v[40:43], v[48:51]\n" \
    "s_waitcnt lgkmcnt(8)\n" \
    "ds_read_b64 v[162:163], %[xr] offset:16960\n" \
    "ds_read_b128 v[24:27], v108 offset:9216\n" \
    "v_mfma_f32_16x16x32_bf16 v[52:55], a[108:111], v[40:43], v[52:55]\n" \
    "ds_read_b32 v167, %[xr2] offset:16896\n" \
    "ds_read_b128 v[28:31], v108 offset:29696\n" \
    "v_mfma_f32_16x16x32_bf16 v[56:59], a[188:191], v[40:43], v[56:59]\n" \
    "buffer_load_dwordx4 v[72:75], %[l16], %[ws], s68 offen sc1\n" \
    "buffer_load_dwordx4 v[76:79], %[l16], %[ws], s68 offen offset:1024 sc1\n" \
    "v_mfma_f32_16x16x32_bf16 v[60:63], a[28:31], v[44:47], v[60:63]\n" \
    "buffer_load_dwordx4 v[80:83], %[l16], %[ws], s68 offen offset:2048 sc1\n" \
    "s_waitcnt lgkmcnt(10)\n" \
    "v_add_f32 v148, v148, v152\n" \
    "v_mfma_f32_16x16x32_bf16 v[64:67], a[108:111], v[44:47], v[64:67]\n" \
    "v_add_f32 v149, v149, v153\n" \
    "s_waitcnt lgkmcnt(9)\n" \
    "v_add_f32 v150, v150, v154\n" \
    "v_mfma_f32_16x16x32_bf16 v[68:71], a[188:191], v[44:47], v[68:71]\n" \
    "v_add_f32 v151, v151, v155\n" \
    "s_waitcnt lgkmcnt(8)\n" \
    "v_add_f32 v164, v164, v165\n" \
    "v_mfma_f32_16x16x32_bf16 v[48:51], a[32:35], v[16:19], v[48:51]\n" \
    "s_waitcnt lgkmcnt(4)\n" \
    "v_add_f32 v156, v156, v160\n" \
    "ds_read_b128 v[32:35], v108 offset:10240\n" \
    "v_mfma_f32_16x16x32_bf16 v[52:55], a[112:115], v[16:19], v[52:55]\n" \
    "v_add_f32 v157, v157, v161\n" \
    "ds_read_b128 v[36:39], v108 offset:30720\n" \
    "v_mfma_f32_16x16x32_bf16 v[56:59], a[192:195], v[16:19], v[56:59]\n" \
    "s_waitcnt lgkmcnt(5)\n" \
    "v_add_f32 v158, v158, v162\n" \
    "v_add_f32 v159, v159, v163\n" \
    "v_mfma_f32_16x16x32_bf16 v[60:63], a[32:35], v[20:23], v[60:63]\n" \
    "s_waitcnt lgkmcnt(3)\n" \
    "v_add_f32 v166, v166, v167\n" \
    "v_add_f32 v148, v148, v156\n" \
    "v_mfma_f32_16x16x32_bf16 v[64:67], a[112:115], v[20:23], v[64:67]\n" \
    "v_add_f32 v149, v149, v157\n" \
    "v_add_f32 v150, v150, v158\n" \
    "v_mfma_f32_16x16x32_bf16 v[68:71], a[192:195], v[20:23], v[68:71]\n" \
    "v_add_f32 v151, v151, v159\n" \
    "v_add_f32 v164, v164, v166\n" \
    "v_mfma_f32_16x16x32_bf16 v[48:51], a[36:39], v[24:27], v[48:51]\n" \
    "v_cndmask_b32 v148, v147, v148, %[m0]\n" \
    "ds_read_b128 v[40:43], v108 offset:11264\n" \
    "v_mfma_f32_16x16x32_bf16 v[52:55], a[116:119], v[24:27], v[52:55]\n" \
    "v_cndmask_b32 v149, v147, v149, %[m1]\n" \
    "ds_read_b128 v[44:47], v108 offset:31744\n" \
    "v_mfma_f32_16x16x32_bf16 v[56:59], a[196:199], v[24:27], v[56:59]\n" \
    "v_cndmask_b32 v150, v147, v150, %[m2]\n" \
    "v_cndmask_b32 v151, v147, v151, %[m3]\n" \
    "s_waitcnt lgkmcnt(4)\n" \
    "v_mfma_f32_16x16x32_bf16 v[60:63], a[36:39], v[28:31], v[60:63]\n" \
    "v_max3_f32 v152, v148, v149, v164\n" \
    "v_max3_f32 v152, v152, v150, v151\n" \
    "v_mfma_f32_16x16x32_bf16 v[64:67], a[116:119], v[28:31], v[64:67]\n" \
    "v_mfma_f32_16x16x32_bf16 v[68:71], a[196:199], v[28:31], v[68:71]\n" \
    "v_max_f32_dpp v152, v152, v152 quad_perm:[1,0,3,2] row_mask:0xf bank_mask:0xf\n" \
    "s_waitcnt lgkmcnt(3)\n" \
    "v_mfma_f32_16x16x32_bf16 v[48:51], a[40:43], v[32:35], v[48:51]\n" \
    "v_max_f32_dpp v152, v152, v152 quad_perm:[2,3,0,1] row_mask:0xf bank_mask:0xf\n" \
    "ds_read_b128 v[16:19], v108 offset:12288\n" \
    "v_mfma_f32_16x16x32_bf16 v[52:55], a[120:123], v[32:35], v[52:55]\n" \
    "v_max_f32_dpp v152, v152, v152 row_half_mirror row_mask:0xf bank_mask:0xf\n" \
    "ds_read_b128 v[20:23], v108 offset:32768\n" \
    "v_mfma_f32_16x16x32_bf16 v[56:59], a[200:203], v[32:35], v[56:59]\n" \
    "v_mul_f32 v153, s74, v152\n" \
    "v_fma_f32 v148, v148, s69, v153\n" \
    "s_waitcnt lgkmcnt(4)\n" \
    "v_mfma_f32_16x16x32_bf16 v[60:63], a[40:43], v[36:39], v[60:63]\n" \
    "v_fma_f32 v149, v149, s69, v153\n" \
    "v_fma_f32 v150, v150, s69, v153\n" \
    "v_mfma_f32_16x16x32_bf16 v[64:67], a[120:123], v[36:39], v[64:67]\n" \
    "v_fma_f32 v151, v151, s69, v153\n" \
    "v_fma_f32 v164, v164, s69, v153\n" \
    "v_mfma_f32_16x16x32_bf16 v[68:71], a[200:203], v[36:39], v[68:71]\n" \
    "v_exp_f32 v148, v148\n" \
    "v_exp_f32 v149, v149\n" \
    "s_waitcnt lgkmcnt(3)\n" \
    "v_mfma_f32_16x16x32_bf16 v[48:51], a[44:47], v[40:43], v[48:51]\n" \
    "v_exp_f32 v150, v150\n" \
    "ds_read_b128 v[24:27], v108 offset:13312\n" \
    "v_mfma_f32_16x16x32_bf16 v[52:55], a[124:127], v[40:43], v[52:55]\n" \
    "v_exp_f32 v151, v151\n" \
    "ds_read_b128 v[28:31], v108 offset:33792\n" \
    "v_mfma_f32_16x16x32_bf16 v[56:59], a[204:207], v[40:43], v[56:59]\n" \
    "v_exp_f32 v164, v164\n" \
    "v_add_f32 v156, v148, v149\n" \
    "s_waitcnt lgkmcnt(4)\n" \
    "v_mfma_f32_16x16x32_bf16 v[60:63], a[44:47], v[44:47], v[60:63]\n" \
    "v_add_f32 v154, v150, v151\n" \
    "v_add_f32 v156, v156, v164\n" \
    "v_mfma_f32_16x16x32_bf16 v[64:67], a[124:127], v[44:47], v[64:67]\n" \
    "v_add_f32 v154, v154, v156\n" \
    "v_mfma_f32_16x16x32_bf16 v[68:71], a[204:207], v[44:47], v[68:71]\n" \
    "s_waitcnt lgkmcnt(3)\n" \
    "v_mfma_f32_16x16x32_bf16 v[48:51], a[48:51], v[16:19], v[48:51]\n" \
    "v_add_f32_dpp v154, v154, v154 quad_perm:[1,0,3,2] row_mask:0xf bank_mask:0xf\n" \
    "ds_read_b128 v[32:35], v108 offset:14336\n" \
    "v_mfma_f32_16x16x32_bf16 v[52:55], a[128:131], v[16:19], v[52:55]\n" \
    "v_add_f32_dpp v154, v154, v154 quad_perm:[2,3,0,1] row_mask:0xf bank_mask:0xf\n" \
    "ds_read_b128 v[36:39], v108 offset:34816\n" \
    "v_mfma_f32_16x16x32_bf16 v[56:59], a[208:211], v[16:19], v[56:59]\n" \
    "v_add_f32_dpp v154, v154, v154 row_half_mirror row_mask:0xf bank_mask:0xf\n" \
    "v_rcp_f32 v154, v154\n" \
    "s_waitcnt lgkmcnt(4)\n" \
    "v_mfma_f32_16x16x32_bf16 v[60:63], a[48:51], v[20:23], v[60:63]\n" \
    "v_mul_f32 v164, v164, v154\n" \
    "v_mul_f32 v148, v148, v154\n" \
    "v_mfma_f32_16x16x32_bf16 v[64:67], a[128:131], v[20:23], v[64:67]\n" \
    "v_mul_f32 v149, v149, v154\n" \
    "v_mul_f32 v150, v150, v154\n" \
    "v_mfma_f32_16x16x32_bf16 v[68:71], a[208:211], v[20:23], v[68:71]\n" \
    "v_mul_f32 v151, v151, v154\n" \
    "v_mov_b32_dpp v165, v164 quad_perm:[1,0,3,2] row_mask:0xf bank_mask:0xf\n" \
    "s_waitcnt lgkmcnt(3)\n" \
    "v_mfma_f32_16x16x32_bf16 v[48:51], a[52:55], v[24:27], v[48:51]\n" \
    "v_cvt_pk_bf16_f32 v148, v148, v149\n" \
    "ds_read_b128 v[40:43], v108 offset:15360\n" \
    "v_mfma_f32_16x16x32_bf16 v[52:55], a[132:135], v[24:27], v[52:55]\n" \
    "v_cvt_pk_bf16_f32 v149, v150, v151\n" \
    "ds_read_b128 v[44:47], v108 offset:35840\n" \
    "v_mfma_f32_16x16x32_bf16 v[56:59], a[212:215], v[24:27], v[56:59]\n" \
    "v_cvt_pk_bf16_f32 v164, v164, v165\n" \
    "v_min_u32 v148, -2, v148\n" \
    "s_waitcnt lgkmcnt(4)\n" \
    "v_mfma_f32_16x16x32_bf16 v[60:63], a[52:55], v[28:31], v[60:63]\n" \
    "v_min_u32 v149, -2, v149\n" \
    "v_min_u32 v164, -2, v164\n" \
    "v_mfma_f32_16x16x32_bf16 v[64:67], a[132:135], v[28:31], v[64:67]\n" \
    "buffer_store_dwordx2 v[148:149], %[pt], %[ws], s75 offen sc1\n" \
    "buffer_store_dword v164, %[ptm], %[ws], s75 offen sc1\n" \
    "v_mfma_f32_16x16x32_bf16 v[68:71], a[212:215], v[28:31], v[68:71]\n" \
    "buffer_load_dwordx4 v[140:143], %[km], %[ws], s72 offen sc1\n" \
    "buffer_load_dwordx2 v[144:145], %[km], %[ws], s72 offen offset:512 sc1\n" \
    "s_waitcnt lgkmcnt(3)\n" \
    "v_mfma_f32_16x16x32_bf16 v[48:51], a[56:59], v[32:35], v[48:51]\n" \
    "ds_read_b128 v[16:19], v108 offset:16384\n" \
    "v_mfma_f32_16x16x32_bf16 v[52:55], a[136:139], v[32:35], v[52:55]\n" \
    "ds_read_b128 v[20:23], v108 offset:36864\n" \
    "v_mfma_f32_16x16x32_bf16 v[56:59], a[216:219], v[32:35], v[56:59]\n" \
    "s_waitcnt lgkmcnt(4)\n" \
    "v_mfma_f32_16x16x32_bf16 v[60:63], a[56:59], v[36:39], v[60:63]\n" \
    "v_mfma_f32_16x16x32_bf16 v[64:67], a[136:139], v[36:39], v[64:67]\n" \
    "v_mfma_f32_16x16x32_bf16 v[68:71], a[216:219], v[36:39], v[68:71]\n" \
    "s_waitcnt lgkmcnt(3)\n" \
    "v_mfma_f32_16x16x32_bf16 v[48:51], a[60:63], v[40:43], v[48:51]\n" \
    "ds_read_b128 v[24:27], v108 offset:17408\n" \
    "v_mfma_f32_16x16x32_bf16 v[52:55], a[140:143], v[40:43], v[52:55]\n" \
    "ds_read_b128 v[28:31], v108 offset:37888\n" \
    "v_mfma_f32_16x16x32_bf16 v[56:59], a[220:223], v[40:43], v[56:59]\n" \
    "s_waitcnt lgkmcnt(4)\n" \
    "v_mfma_f32_16x16x32_bf16 v[60:63], a[60:63], v[44:47], v[60:63]\n" \
    "v_mfma_f32_16x16x32_bf16 v[64:67], a[140:143], v[44:47], v[64:67]\n" \
    "v_mfma_f32_16x16x32_bf16 v[68:71], a[220:223], v[44:47], v[68:71]\n" \
    "s_waitcnt lgkmcnt(3)\n" \
    "v_mfma_f32_16x16x32_bf16 v[48:51], a[64:67], v[16:19], v[48:51]\n" \
    "ds_read_b128 v[32:35], v108 offset:18432\n" \
    "v_mfma_f32_16x16x32_bf16 v[52:55], a[144:147], v[16:19], v[52:55]\n" \
    "ds_read_b128 v[36:39], v108 offset:38912\n" \
    "v_mfma_f32_16x16x32_bf16 v[56:59], a[224:227], v[16:19], v[56:59]\n" \
    "s_waitcnt lgkmcnt(4)\n" \
    "v_mfma_f32_16x16x32_bf16 v[60:63], a[64:67], v[20:23], v[60:63]\n" \
    "v_mfma_f32_16x16x32_bf16 v[64:67], a[144:147], v[20:23], v[64:67]\n" \
    "v_mfma_f32_16x16x32_bf16 v[68:71], a[224:227], v[20:23], v[68:71]\n" \
    "s_waitcnt lgkmcnt(3)\n" \
    "v_mfma_f32_16x16x32_bf16 v[48:51], a[68:71], v[24:27], v[48:51]\n" \
    "ds_read_b128 v[40:43], v108 offset:19456\n" \
    "v_mfma_f32_16x16x32_bf16 v[52:55], a[148:151], v[24:27], v[52:55]\n" \
    "ds_read_b128 v[44:47], v108 offset:39936\n" \
    "v_mfma_f32_16x16x32_bf16 v[56:59], a[228:231], v[24:27], v[56:59]\n" \
    "s_waitcnt lgkmcnt(4)\n" \
    "v_mfma_f32_16x16x32_bf16 v[60:63], a[68:71], v[28:31], v[60:63]\n" \
    "v_mfma_f32_16x16x32_bf16 v[64:67], a[148:151], v[28:31], v[64:67]\n" \
    "v_mfma_f32_16x16x32_bf16 v[68:71], a[228:231], v[28:31], v[68:71]\n" \
    "s_waitcnt lgkmcnt(3)\n" \
    "v_mfma_f32_16x16x32_bf16 v[48:51], a[72:75], v[32:35], v[48:51]\n" \
    "ds_read_b128 v[16:19], v109\n" \
    "v_mfma_f32_16x16x32_bf16 v[52:55], a[152:155], v[32:35], v[52:55]\n" \
    "ds_read_b128 v[20:23], v109 offset:20480\n" \
    "v_mfma_f32_16x16x32_bf16 v[56:59], a[232:235], v[32:35], v[56:59]\n" \
    "s_waitcnt lgkmcnt(4)\n" \
    "v_mfma_f32_16x16x32_bf16 v[60:63], a[72:75], v[36:39], v[60:63]\n" \
    "v_mfma_f32_16x16x32_bf16 v[64:67], a[152:155], v[36:39], v[64:67]\n" \
    "v_mfma_f32_16x16x32_bf16 v[68:71], a[232:235], v[36:39], v[68:71]\n" \
    "s_waitcnt lgkmcnt(3)\n" \
    "v_mfma_f32_16x16x32_bf16 v[48:51], a[76:79], v[40:43], v[48:51]\n" \
    "ds_read_b128 v[24:27], v109 offset:1024\n" \
    "v_mfma_f32_16x16x32_bf16 v[52:55], a[156:159], v[40:43], v[52:55]\n" \
    "ds_read_b128 v[28:31], v109 offset:21504\n" \
    "v_mfma_f32_16x16x32_bf16 v[56:59], a[236:239], v[40:43], v[56:59]\n" \
    "s_waitcnt lgkmcnt(4)\n" \
    "v_mfma_f32_16x16x32_bf16 v[60:63], a[76:79], v[44:47], v[60:63]\n" \
    "v_mfma_f32_16x16x32_bf16 v[64:67], a[156:159], v[44:47], v[64:67]\n" \
    "v_mfma_f32_16x16x32_bf16 v[68:71], a[236:239], v[44:47], v[68:71]\n" \
    "s_mul_i32 s75, s60, 81920\n" \
    "s_add_u32 s75, s75, 235929600\n" \
    "v_cvt_pk_bf16_f32 v84, v48, v49\n" \
    "v_cvt_pk_bf16_f32 v85, v50, v51\n" \
    "v_cvt_pk_bf16_f32 v86, v52, v53\n" \
    "v_cvt_pk_bf16_f32 v87, v54, v55\n" \
    "v_cvt_pk_bf16_f32 v88, v56, v57\n" \
    "v_cvt_pk_bf16_f32 v89, v58, v59\n" \
    "v_cvt_pk_bf16_f32 v90, v60, v61\n" \
    "v_cvt_pk_bf16_f32 v91, v62, v63\n" \
    "v_cvt_pk_bf16_f32 v92, v64, v65\n" \
    "v_cvt_pk_bf16_f32 v93, v66, v67\n" \
    "v_cvt_pk_bf16_f32 v94, v68, v69\n" \
    "v_cvt_pk_bf16_f32 v95, v70, v71\n" \
    "s_waitcnt lgkmcnt(0)\n" \
    "s_add_u32 s60, s60, 1\n" \
    "s_cmp_lt_u32 s60, 64\n" \
    "s_cbranch_scc1 Lq_loop\n" \
    "s_waitcnt vmcnt(0)\n" \
    "s_barrier\n" \
    "s_cmp_lg_u32 %[wave], 0\n" \
    "s_cbranch_scc1 Lq_sigy\n" \
    "v_mov_b32 v111, 64\n" \
    "buffer_store_dword v111, %[one], %[ws], %[qflag] offen sc1\n" \
    "Lq_sigy:\n" \
    "v_mfma_f32_16x16x16_bf16 v[120:123], v[72:73], v[84:85], 0\n" \
    "v_mfma_f32_16x16x16_bf16 v[124:127], v[72:73], v[90:91], 0\n" \
    "v_mfma_f32_16x16x16_bf16 v[128:131], v[74:75], v[90:91], 0\n" \
    "v_mfma_f32_16x16x16_bf16 v[120:123], v[76:77], v[86:87], v[120:123]\n" \
    "v_mfma_f32_16x16x16_bf16 v[124:127], v[76:77], v[92:93], v[124:127]\n" \
    "v_mfma_f32_16x16x16_bf16 v[128:131], v[78:79], v[92:93], v[128:131]\n" \
    "v_mfma_f32_16x16x16_bf16 v[120:123], v[80:81], v[88:89], v[120:123]\n" \
    "v_mfma_f32_16x16x16_bf16 v[124:127], v[80:81], v[94:95], v[124:127]\n" \
    "v_mfma_f32_16x16x16_bf16 v[128:131], v[82:83], v[94:95], v[128:131]\n" \
    "s_nop 5\n" \
    "ds_write_b128 %[xw], v[120:123]\n" \
    "ds_write_b128 %[xw], v[124:127] offset:2816\n" \
    "ds_write_b128 %[xw], v[128:131] offset:2880\n" \
    "s_mov_b32 s67, 1048576\n" \
    "Lq_kcb:\n" \
    "v_max3_u32 v146, v140, v141, v142\n" \
    "v_max3_u32 v146, v146, v143, v144\n" \
    "v_max_u32 v146, v146, v145\n" \
    "v_cmp_eq_u32 vcc, -1, v146\n" \
    "s_cbranch_vccnz Lq_krb\n" \
    "v_mfma_f32_16x16x16_bf16 v[132:135], v[140:141], v[84:85], 0\n" \
    "v_mfma_f32_16x16x16_bf16 v[136:139], v[140:141], v[90:91], 0\n" \
    "v_mfma_f32_16x16x16_bf16 v[132:135], v[142:143], v[86:87], v[132:135]\n" \
    "v_mfma_f32_16x16x16_bf16 v[136:139], v[142:143], v[92:93], v[136:139]\n" \
    "v_mfma_f32_16x16x16_bf16 v[132:135], v[144:145], v[88:89], v[132:135]\n" \
    "v_mfma_f32_16x16x16_bf16 v[136:139], v[144:145], v[94:95], v[136:139]\n" \
    "s_nop 6\n" \
    "ds_write_b128 %[xm], v[132:135]\n" \
    "ds_write_b128 %[xm], v[136:139] offset:2816\n" \
    "s_waitcnt lgkmcnt(0)\n" \
    "s_barrier\n" \
    "ds_read_b64 v[148:149], %[xr]\n" \
    "ds_read_b64 v[150:151], %[xr] offset:64\n" \
    "ds_read_b32 v164, %[xr2]\n" \
    "ds_read_b64 v[152:153], %[xr] offset:5632\n" \
    "ds_read_b64 v[154:155], %[xr] offset:5696\n" \
    "ds_read_b32 v165, %[xr2] offset:5632\n" \
    "ds_read_b64 v[156:157], %[xr] offset:11264\n" \
    "ds_read_b64 v[158:159], %[xr] offset:11328\n" \
    "ds_read_b32 v166, %[xr2] offset:11264\n" \
    "ds_read_b64 v[160:161], %[xr] offset:16896\n" \
    "ds_read_b64 v[162:163], %[xr] offset:16960\n" \
    "ds_read_b32 v167, %[xr2] offset:16896\n" \
    "s_waitcnt lgkmcnt(8)\n" \
    "v_add_f32 v148, v148, v152\n" \
    "v_add_f32 v149, v149, v153\n" \
    "s_waitcnt lgkmcnt(7)\n" \
    "v_add_f32 v150, v150, v154\n" \
    "v_add_f32 v151, v151, v155\n" \
    "s_waitcnt lgkmcnt(6)\n" \
    "v_add_f32 v164, v164, v165\n" \
    "s_waitcnt lgkmcnt(2)\n" \
    "v_add_f32 v156, v156, v160\n" \
    "v_add_f32 v157, v157, v161\n" \
    "s_waitcnt lgkmcnt(1)\n" \
    "v_add_f32 v158, v158, v162\n" \
    "v_add_f32 v159, v159, v163\n" \
    "s_waitcnt lgkmcnt(0)\n" \
    "v_add_f32 v166, v166, v167\n" \
    "v_add_f32 v148, v148, v156\n" \
    "v_add_f32 v149, v149, v157\n" \
    "v_add_f32 v150, v150, v158\n" \
    "v_add_f32 v151, v151, v159\n" \
    "v_add_f32 v164, v164, v166\n" \
    "v_cndmask_b32 v148, v147, v148, %[m0]\n" \
    "v_cndmask_b32 v149, v147, v149, %[m1]\n" \
    "v_cndmask_b32 v150, v147, v150, %[m2]\n" \
    "v_cndmask_b32 v151, v147, v151, %[m3]\n" \
    "v_max3_f32 v152, v148, v149, v164\n" \
    "v_max3_f32 v152, v152, v150, v151\n" \
    "s_nop 1\n" \
    "v_max_f32_dpp v152, v152, v152 quad_perm:[1,0,3,2] row_mask:0xf bank_mask:0xf\n" \
    "s_nop 1\n" \
    "v_max_f32_dpp v152, v152, v152 quad_perm:[2,3,0,1] row_mask:0xf bank_mask:0xf\n" \
    "s_nop 1\n" \
    "v_max_f32_dpp v152, v152, v152 row_half_mirror row_mask:0xf bank_mask:0xf\n" \
    "v_mul_f32 v153, s74, v152\n" \
    "v_fma_f32 v148, v148, s69, v153\n" \
    "v_fma_f32 v149, v149, s69, v153\n" \
    "v_fma_f32 v150, v150, s69, v153\n" \
    "v_fma_f32 v151, v151, s69, v153\n" \
    "v_fma_f32 v164, v164, s69, v153\n" \
    "v_exp_f32 v148, v148\n" \
    "v_exp_f32 v149, v149\n" \
    "v_exp_f32 v150, v150\n" \
    "v_exp_f32 v151, v151\n" \
    "v_exp_f32 v164, v164\n" \
    "v_add_f32 v156, v148, v149\n" \
    "v_add_f32 v154, v150, v151\n" \
    "v_add_f32 v156, v156, v164\n" \
    "v_add_f32 v154, v154, v156\n" \
    "s_nop 1\n" \
    "v_add_f32_dpp v154, v154, v154 quad_perm:[1,0,3,2] row_mask:0xf bank_mask:0xf\n" \
    "s_nop 1\n" \
    "v_add_f32_dpp v154, v154, v154 quad_perm:[2,3,0,1] row_mask:0xf bank_mask:0xf\n" \
    "s_nop 1\n" \
    "v_add_f32_dpp v154, v154, v154 row_half_mirror row_mask:0xf bank_mask:0xf\n" \
    "v_rcp_f32 v154, v154\n" \
    "s_nop 0\n" \
    "v_mul_f32 v164, v164, v154\n" \
    "v_mul_f32 v148, v148, v154\n" \
    "v_mul_f32 v149, v149, v154\n" \
    "v_mul_f32 v150, v150, v154\n" \
    "v_mul_f32 v151, v151, v154\n" \
    "v_mov_b32_dpp v165, v164 quad_perm:[1,0,3,2] row_mask:0xf bank_mask:0xf\n" \
    "v_cvt_pk_bf16_f32 v148, v148, v149\n" \
    "v_cvt_pk_bf16_f32 v149, v150, v151\n" \
    "v_cvt_pk_bf16_f32 v164, v164, v165\n" \
    "v_min_u32 v148, -2, v148\n" \
    "v_min_u32 v149, -2, v149\n" \
    "v_min_u32 v164, -2, v164\n" \
    "buffer_store_dwordx2 v[148:149], %[pt], %[ws], s75 offen sc1\n" \
    "buffer_store_dword v164, %[ptm], %[ws], s75 offen sc1\n" \
    "s_endpgm\n" \
    "Lq_kra:\n" \
    "s_sub_u32 s67, s67, 1\n" \
    "s_cbranch_scc1 Lq_tmo\n" \
    "buffer_load_dwordx4 v[140:143], %[km], %[ws], s72 offen sc1\n" \
    "buffer_load_dwordx2 v[144:145], %[km], %[ws], s72 offen offset:512 sc1\n" \
    "s_sleep 6\n" \
    "s_waitcnt vmcnt(0)\n" \
    "s_branch Lq_kca\n" \
    "Lq_krb:\n" \
    "s_sub_u32 s67, s67, 1\n" \
    "s_cbranch_scc1 Lq_tmo\n" \
    "buffer_load_dwordx4 v[140:143], %[km], %[ws], s72 offen sc1\n" \
    "buffer_load_dwordx2 v[144:145], %[km], %[ws], s72 offen offset:512 sc1\n" \
    "s_sleep 6\n" \
    "s_waitcnt vmcnt(0)\n" \
    "s_branch Lq_kcb\n" \
    "Lq_kw:\n" \
    "s_mov_b32 s67, 1048576\n" \
    "Lq_kw2:\n" \
    "s_sub_u32 s67, s67, 1\n" \
    "s_cbranch_scc1 Lq_tmo\n" \
    "buffer_load_dword v146, %[one], %[ws], %[flag] offen sc1\n" \
    "s_waitcnt vmcnt(0)\n" \
    "v_readfirstlane_b32 s66, v146\n" \
    "s_nop 3\n" \
    "s_cmp_lt_u32 s60, s66\n" \
    "s_cbranch_scc1 Lq_kok\n" \
    "s_sleep 2\n" \
    "s_branch Lq_kw2\n" \
    "Lq_tmo:\n" \
    "s_add_u32 s78, s60, 2304\n" \
    "v_mov_b32 v148, s78\n" \
    "v_mov_b32 v149, 0\n" \
    "s_mov_b32 s78, 253757696\n" \
    "s_nop 4\n" \
    "buffer_store_dword v148, v149, %[ws], s78 offen sc1\n" \
    "s_endpgm\n"
#define ASM_F1 \
    "s_mul_i32 s70, %[wave], 10240\n" \
    "s_add_u32 s63, s70, 250609664\n" \
    "s_add_u32 m0, s70, 0\n" \
    "s_nop 0\n" \
    "buffer_load_dwordx4 %[l16], %[ws], s63 offen lds\n" \
    "buffer_load_dwordx4 %[l16], %[ws], s63 offen offset:1024 lds\n" \
    "buffer_load_dwordx4 %[l16], %[ws], s63 offen offset:2048 lds\n" \
    "buffer_load_dwordx4 %[l16], %[ws], s63 offen offset:3072 lds\n" \
    "s_add_u32 m0, s70, 4096\n" \
    "s_add_u32 s63, s63, 4096\n" \
    "buffer_load_dwordx4 %[l16], %[ws], s63 offen lds\n" \
    "buffer_load_dwordx4 %[l16], %[ws], s63 offen offset:1024 lds\n" \
    "buffer_load_dwordx4 %[l16], %[ws], s63 offen offset:2048 lds\n" \
    "buffer_load_dwordx4 %[l16], %[ws], s63 offen offset:3072 lds\n" \
    "s_add_u32 m0, s70, 8192\n" \
    "s_add_u32 s63, s63, 4096\n" \
    "buffer_load_dwordx4 %[l16], %[ws], s63 offen lds\n" \
    "buffer_load_dwordx4 %[l16], %[ws], s63 offen offset:1024 lds\n" \
    "s_mul_i32 s71, %[wave], 3072\n" \
    "v_add_u32 v112, 65536, %[tr0]\n" \
    "v_add_u32 v113, 65536, %[tr1]\n" \
    "v_add_u32 v114, 65536, %[tr2]\n" \
    "v_add_u32 v115, 98304, %[tr0]\n" \
    "v_add_u32 v116, 98304, %[tr1]\n" \
    "v_add_u32 v117, 98304, %[tr2]\n" \
    "s_mov_b32 s61, 0\n" \
    "s_add_u32 m0, s71, 40960\n" \
    "s_nop 0\n" \
    "buffer_load_dwordx4 %[src0], %[w0], s61 offen lds\n" \
    "s_add_u32 m0, s71, 41984\n" \
    "s_nop 0\n" \
    "buffer_load_dwordx4 %[src1], %[w0], s61 offen lds\n" \
    "s_add_u32 m0, s71, 43008\n" \
    "s_nop 0\n" \
    "buffer_load_dwordx4 %[src2], %[w0], s61 offen lds\n" \
    "s_mov_b32 s61, 98304\n" \
    "s_add_u32 m0, s71, 53248\n" \
    "s_nop 0\n" \
    "buffer_load_dwordx4 %[src0], %[w0], s61 offen lds\n" \
    "s_add_u32 m0, s71, 54272\n" \
    "s_nop 0\n" \
    "buffer_load_dwordx4 %[src1], %[w0], s61 offen lds\n" \
    "s_add_u32 m0, s71, 55296\n" \
    "s_nop 0\n" \
    "buffer_load_dwordx4 %[src2], %[w0], s61 offen lds\n" \
    "s_mov_b32 s61, 196608\n" \
    "s_add_u32 m0, s71, 65536\n" \
    "s_nop 0\n" \
    "buffer_load_dwordx4 %[src0], %[w0], s61 offen lds\n" \
    "s_add_u32 m0, s71, 66560\n" \
    "s_nop 0\n" \
    "buffer_load_dwordx4 %[src1], %[w0], s61 offen lds\n" \
    "s_add_u32 m0, s71, 67584\n" \
    "s_nop 0\n" \
    "buffer_load_dwordx4 %[src2], %[w0], s61 offen lds\n" \
    "s_mov_b32 s61, 294912\n" \
    "s_add_u32 m0, s71, 77824\n" \
    "s_nop 0\n" \
    "buffer_load_dwordx4 %[src0], %[w0], s61 offen lds\n" \
    "s_add_u32 m0, s71, 78848\n" \
    "s_nop 0\n" \
    "buffer_load_dwordx4 %[src1], %[w0], s61 offen lds\n" \
    "s_add_u32 m0, s71, 79872\n" \
    "s_nop 0\n" \
    "buffer_load_dwordx4 %[src2], %[w0], s61 offen lds\n" \
    "s_mov_b32 s61, 393216\n" \
    "s_add_u32 m0, s71, 90112\n" \
    "s_nop 0\n" \
    "buffer_load_dwordx4 %[src0], %[w0], s61 offen lds\n" \
    "s_add_u32 m0, s71, 91136\n" \
    "s_nop 0\n" \
    "buffer_load_dwordx4 %[src1], %[w0], s61 offen lds\n" \
    "s_add_u32 m0, s71, 92160\n" \
    "s_nop 0\n" \
    "buffer_load_dwordx4 %[src2], %[w0], s61 offen lds\n" \
    "s_mov_b32 s61, 491520\n" \
    "s_add_u32 m0, s71, 102400\n" \
    "s_nop 0\n" \
    "buffer_load_dwordx4 %[src0], %[w0], s61 offen lds\n" \
    "s_add_u32 m0, s71, 103424\n" \
    "s_nop 0\n" \
    "buffer_load_dwordx4 %[src1], %[w0], s61 offen lds\n" \
    "s_add_u32 m0, s71, 104448\n" \
    "s_nop 0\n" \
    "buffer_load_dwordx4 %[src2], %[w0], s61 offen lds\n" \
    "s_mov_b32 s61, 589824\n" \
    "s_add_u32 m0, s71, 114688\n" \
    "s_nop 0\n" \
    "buffer_load_dwordx4 %[src0], %[w0], s61 offen lds\n" \
    "s_add_u32 m0, s71, 115712\n" \
    "s_nop 0\n" \
    "buffer_load_dwordx4 %[src1], %[w0], s61 offen lds\n" \
    "s_add_u32 m0, s71, 116736\n" \
    "s_nop 0\n" \
    "buffer_load_dwordx4 %[src2], %[w0], s61 offen lds\n" \
    "s_mov_b32 s61, 688128\n" \
    "s_add_u32 m0, s71, 126976\n" \
    "s_nop 0\n" \
    "buffer_load_dwordx4 %[src0], %[w0], s61 offen lds\n" \
    "s_add_u32 m0, s71, 128000\n" \
    "s_nop 0\n" \
    "buffer_load_dwordx4 %[src1], %[w0], s61 offen lds\n" \
    "s_add_u32 m0, s71, 129024\n" \
    "s_nop 0\n" \
    "buffer_load_dwordx4 %[src2], %[w0], s61 offen lds\n" \
    "s_mov_b32 s61, 786432\n" \
    "s_add_u32 m0, s71, 139264\n" \
    "s_nop 0\n" \
    "buffer_load_dwordx4 %[src0], %[w0], s61 offen lds\n" \
    "s_add_u32 m0, s71, 140288\n" \
    "s_nop 0\n" \
    "buffer_load_dwordx4 %[src1], %[w0], s61 offen lds\n" \
    "s_add_u32 m0, s71, 141312\n" \
    "s_nop 0\n" \
    "buffer_load_dwordx4 %[src2], %[w0], s61 offen lds\n" \
    "s_mov_b32 s61, 884736\n" \
    "s_add_u32 m0, s71, 151552\n" \
    "s_nop 0\n" \
    "buffer_load_dwordx4 %[src0], %[w0], s61 offen lds\n" \
    "s_add_u32 m0, s71, 152576\n" \
    "s_nop 0\n" \
    "buffer_load_dwordx4 %[src1], %[w0], s61 offen lds\n" \
    "s_add_u32 m0, s71, 153600\n" \
    "s_nop 0\n" \
    "buffer_load_dwordx4 %[src2], %[w0], s61 offen lds\n" \
    "s_waitcnt vmcnt(24)\n" \
    "s_barrier\n" \
    "ds_read_b64_tr_b16 a[0:1], %[tr0] offset:40960\n" \
    "ds_read_b64_tr_b16 a[2:3], %[tr0] offset:42496\n" \
    "ds_read_b64_tr_b16 a[80:81], %[tr1] offset:40960\n" \
    "ds_read_b64_tr_b16 a[82:83], %[tr1] offset:42496\n" \
    "ds_read_b64_tr_b16 a[160:161], %[tr2] offset:40960\n" \
    "ds_read_b64_tr_b16 a[162:163], %[tr2] offset:42496\n" \
    "ds_read_b64_tr_b16 a[4:5], %[tr0] offset:53248\n" \
    "ds_read_b64_tr_b16 a[6:7], %[tr0] offset:54784\n" \
    "ds_read_b64_tr_b16 a[84:85], %[tr1] offset:53248\n" \
    "ds_read_b64_tr_b16 a[86:87], %[tr1] offset:54784\n" \
    "ds_read_b64_tr_b16 a[164:165], %[tr2] offset:53248\n" \
    "ds_read_b64_tr_b16 a[166:167], %[tr2] offset:54784\n" \
    "s_waitcnt lgkmcnt(0)\n" \
    "s_waitcnt vmcnt(18)\n" \
    "s_barrier\n" \
    "s_mov_b32 s61, 983040\n" \
    "s_add_u32 m0, s71, 40960\n" \
    "s_nop 0\n" \
    "buffer_load_dwordx4 %[src0], %[w0], s61 offen lds\n" \
    "s_add_u32 m0, s71, 41984\n" \
    "s_nop 0\n" \
    "buffer_load_dwordx4 %[src1], %[w0], s61 offen lds\n" \
    "s_add_u32 m0, s71, 43008\n" \
    "s_nop 0\n" \
    "buffer_load_dwordx4 %[src2], %[w0], s61 offen lds\n" \
    "s_mov_b32 s61, 1081344\n" \
    "s_add_u32 m0, s71, 53248\n" \
    "s_nop 0\n" \
    "buffer_load_dwordx4 %[src0], %[w0], s61 offen lds\n" \
    "s_add_u32 m0, s71, 54272\n" \
    "s_nop 0\n" \
    "buffer_load_dwordx4 %[src1], %[w0], s61 offen lds\n" \
    "s_add_u32 m0, s71, 55296\n" \
    "s_nop 0\n" \
    "buffer_load_dwordx4 %[src2], %[w0], s61 offen lds\n" \
    "ds_read_b64_tr_b16 a[8:9], v112\n" \
    "ds_read_b64_tr_b16 a[10:11], v112 offset:1536\n" \
    "ds_read_b64_tr_b16 a[88:89], v113\n" \
    "ds_read_b64_tr_b16 a[90:91], v113 offset:1536\n" \
    "ds_read_b64_tr_b16 a[168:169], v114\n" \
    "ds_read_b64_tr_b16 a[170:171], v114 offset:1536\n" \
    "ds_read_b64_tr_b16 a[12:13], v112 offset:12288\n" \
    "ds_read_b64_tr_b16 a[14:15], v112 offset:13824\n" \
    "ds_read_b64_tr_b16 a[92:93], v113 offset:12288\n" \
    "ds_read_b64_tr_b16 a[94:95], v113 offset:13824\n" \
    "ds_read_b64_tr_b16 a[172:173], v114 offset:12288\n" \
    "ds_read_b64_tr_b16 a[174:175], v114 offset:13824\n" \
    "s_waitcnt lgkmcnt(0)\n" \
    "s_waitcnt vmcnt(18)\n" \
    "s_barrier\n" \
    "s_mov_b32 s61, 1179648\n" \
    "s_add_u32 m0, s71, 65536\n" \
    "s_nop 0\n" \
    "buffer_load_dwordx4 %[src0], %[w0], s61 offen lds\n" \
    "s_add_u32 m0, s71, 66560\n" \
    "s_nop 0\n" \
    "buffer_load_dwordx4 %[src1], %[w0], s61 offen lds\n" \
    "s_add_u32 m0, s71, 67584\n" \
    "s_nop 0\n" \
    "buffer_load_dwordx4 %[src2], %[w0], s61 offen lds\n" \
    "s_mov_b32 s61, 1277952\n" \
    "s_add_u32 m0, s71, 77824\n" \
    "s_nop 0\n" \
    "buffer_load_dwordx4 %[src0], %[w0], s61 offen lds\n" \
    "s_add_u32 m0, s71, 78848\n" \
    "s_nop 0\n" \
    "buffer_load_dwordx4 %[src1], %[w0], s61 offen lds\n" \
    "s_add_u32 m0, s71, 79872\n" \
    "s_nop 0\n" \
    "buffer_load_dwordx4 %[src2], %[w0], s61 offen lds\n" \
    "ds_read_b64_tr_b16 a[16:17], v112 offset:24576\n" \
    "ds_read_b64_tr_b16 a[18:19], v112 offset:26112\n" \
    "ds_read_b64_tr_b16 a[96:97], v113 offset:24576\n" \
    "ds_read_b64_tr_b16 a[98:99], v113 offset:26112\n" \
    "ds_read_b64_tr_b16 a[176:177], v114 offset:24576\n" \
    "ds_read_b64_tr_b16 a[178:179], v114 offset:26112\n" \
    "ds_read_b64_tr_b16 a[20:21], v112 offset:36864\n" \
    "ds_read_b64_tr_b16 a[22:23], v112 offset:38400\n" \
    "ds_read_b64_tr_b16 a[100:101], v113 offset:36864\n" \
    "ds_read_b64_tr_b16 a[102:103], v113 offset:38400\n" \
    "ds_read_b64_tr_b16 a[180:181], v114 offset:36864\n" \
    "ds_read_b64_tr_b16 a[182:183], v114 offset:38400\n" \
    "s_waitcnt lgkmcnt(0)\n" \
    "s_waitcnt vmcnt(18)\n" \
    "s_barrier\n" \
    "s_mov_b32 s61, 1376256\n" \
    "s_add_u32 m0, s71, 90112\n" \
    "s_nop 0\n" \
    "buffer_load_dwordx4 %[src0], %[w0], s61 offen lds\n" \
    "s_add_u32 m0, s71, 91136\n" \
    "s_nop 0\n" \
    "buffer_load_dwordx4 %[src1], %[w0], s61 offen lds\n" \
    "s_add_u32 m0, s71, 92160\n" \
    "s_nop 0\n" \
    "buffer_load_dwordx4 %[src2], %[w0], s61 offen lds\n" \
    "s_mov_b32 s61, 1474560\n" \
    "s_add_u32 m0, s71, 102400\n" \
    "s_nop 0\n" \
    "buffer_load_dwordx4 %[src0], %[w0], s61 offen lds\n" \
    "s_add_u32 m0, s71, 103424\n" \
    "s_nop 0\n" \
    "buffer_load_dwordx4 %[src1], %[w0], s61 offen lds\n" \
    "s_add_u32 m0, s71, 104448\n" \
    "s_nop 0\n" \
    "buffer_load_dwordx4 %[src2], %[w0], s61 offen lds\n" \
    "ds_read_b64_tr_b16 a[24:25], v112 offset:49152\n" \
    "ds_read_b64_tr_b16 a[26:27], v112 offset:50688\n" \
    "ds_read_b64_tr_b16 a[104:105], v113 offset:49152\n" \
    "ds_read_b64_tr_b16 a[106:107], v113 offset:50688\n" \
    "ds_read_b64_tr_b16 a[184:185], v114 offset:49152\n" \
    "ds_read_b64_tr_b16 a[186:187], v114 offset:50688\n" \
    "ds_read_b64_tr_b16 a[28:29], v112 offset:61440\n" \
    "ds_read_b64_tr_b16 a[30:31], v112 offset:62976\n" \
    "ds_read_b64_tr_b16 a[108:109], v113 offset:61440\n" \
    "ds_read_b64_tr_b16 a[110:111], v113 offset:62976\n" \
    "ds_read_b64_tr_b16 a[188:189], v114 offset:61440\n" \
    "ds_read_b64_tr_b16 a[190:191], v114 offset:62976\n" \
    "s_waitcnt lgkmcnt(0)\n" \
    "s_waitcnt vmcnt(18)\n" \
    "s_barrier\n" \
    "s_mov_b32 s61, 1572864\n" \
    "s_add_u32 m0, s71, 114688\n" \
    "s_nop 0\n" \
    "buffer_load_dwordx4 %[src0], %[w0], s61 offen lds\n" \
    "s_add_u32 m0, s71, 115712\n" \
    "s_nop 0\n" \
    "buffer_load_dwordx4 %[src1], %[w0], s61 offen lds\n" \
    "s_add_u32 m0, s71, 116736\n" \
    "s_nop 0\n" \
    "buffer_load_dwordx4 %[src2], %[w0], s61 offen lds\n" \
    "s_mov_b32 s61, 1671168\n" \
    "s_add_u32 m0, s71, 126976\n" \
    "s_nop 0\n" \
    "buffer_load_dwordx4 %[src0], %[w0], s61 offen lds\n" \
    "s_add_u32 m0, s71, 128000\n" \
    "s_nop 0\n" \
    "buffer_load_dwordx4 %[src1], %[w0], s61 offen lds\n" \
    "s_add_u32 m0, s71, 129024\n" \
    "s_nop 0\n" \
    "buffer_load_dwordx4 %[src2], %[w0], s61 offen lds\n" \
    "ds_read_b64_tr_b16 a[32:33], v115 offset:40960\n" \
    "ds_read_b64_tr_b16 a[34:35], v115 offset:42496\n" \
    "ds_read_b64_tr_b16 a[112:113], v116 offset:40960\n" \
    "ds_read_b64_tr_b16 a[114:115], v116 offset:42496\n" \
    "ds_read_b64_tr_b16 a[192:193], v117 offset:40960\n" \
    "ds_read_b64_tr_b16 a[194:195], v117 offset:42496\n" \
    "ds_read_b64_tr_b16 a[36:37], v115 offset:53248\n" \
    "ds_read_b64_tr_b16 a[38:39], v115 offset:54784\n" \
    "ds_read_b64_tr_b16 a[116:117], v116 offset:53248\n" \
    "ds_read_b64_tr_b16 a[118:119], v116 offset:54784\n" \
    "ds_read_b64_tr_b16 a[196:197], v117 offset:53248\n" \
    "ds_read_b64_tr_b16 a[198:199], v117 offset:54784\n" \
    "s_waitcnt lgkmcnt(0)\n" \
    "s_waitcnt vmcnt(18)\n" \
    "s_barrier\n" \
    "s_mov_b32 s61, 1769472\n" \
    "s_add_u32 m0, s71, 139264\n" \
    "s_nop 0\n" \
    "buffer_load_dwordx4 %[src0], %[w0], s61 offen lds\n" \
    "s_add_u32 m0, s71, 140288\n" \
    "s_nop 0\n" \
    "buffer_load_dwordx4 %[src1], %[w0], s61 offen lds\n" \
    "s_add_u32 m0, s71, 141312\n" \
    "s_nop 0\n" \
    "buffer_load_dwordx4 %[src2], %[w0], s61 offen lds\n" \
    "s_mov_b32 s61, 1867776\n" \
    "s_add_u32 m0, s71, 151552\n" \
    "s_nop 0\n" \
    "buffer_load_dwordx4 %[src0], %[w0], s61 offen lds\n" \
    "s_add_u32 m0, s71, 152576\n" \
    "s_nop 0\n" \
    "buffer_load_dwordx4 %[src1], %[w0], s61 offen lds\n" \
    "s_add_u32 m0, s71, 153600\n" \
    "s_nop 0\n" \
    "buffer_load_dwordx4 %[src2], %[w0], s61 offen lds\n" \
    "ds_read_b64_tr_b16 a[40:41], %[tr0] offset:40960\n" \
    "ds_read_b64_tr_b16 a[42:43], %[tr0] offset:42496\n" \
    "ds_read_b64_tr_b16 a[120:121], %[tr1] offset:40960\n" \
    "ds_read_b64_tr_b16 a[122:123], %[tr1] offset:42496\n" \
    "ds_read_b64_tr_b16 a[200:201], %[tr2] offset:40960\n" \
    "ds_read_b64_tr_b16 a[202:203], %[tr2] offset:42496\n" \
    "ds_read_b64_tr_b16 a[44:45], %[tr0] offset:53248\n" \
    "ds_read_b64_tr_b16 a[46:47], %[tr0] offset:54784\n" \
    "ds_read_b64_tr_b16 a[124:125], %[tr1] offset:53248\n" \
    "ds_read_b64_tr_b16 a[126:127], %[tr1] offset:54784\n" \
    "ds_read_b64_tr_b16 a[204:205], %[tr2] offset:53248\n" \
    "ds_read_b64_tr_b16 a[206:207], %[tr2] offset:54784\n" \
    "s_waitcnt lgkmcnt(0)\n" \
    "s_waitcnt vmcnt(18)\n" \
    "s_barrier\n" \
    "ds_read_b64_tr_b16 a[48:49], v112\n" \
    "ds_read_b64_tr_b16 a[50:51], v112 offset:1536\n" \
    "ds_read_b64_tr_b16 a[128:129], v113\n" \
    "ds_read_b64_tr_b16 a[130:131], v113 offset:1536\n" \
    "ds_read_b64_tr_b16 a[208:209], v114\n" \
    "ds_read_b64_tr_b16 a[210:211], v114 offset:1536\n" \
    "ds_read_b64_tr_b16 a[52:53], v112 offset:12288\n" \
    "ds_read_b64_tr_b16 a[54:55], v112 offset:13824\n" \
    "ds_read_b64_tr_b16 a[132:133], v113 offset:12288\n" \
    "ds_read_b64_tr_b16 a[134:135], v113 offset:13824\n" \
    "ds_read_b64_tr_b16 a[212:213], v114 offset:12288\n" \
    "ds_read_b64_tr_b16 a[214:215], v114 offset:13824\n" \
    "s_waitcnt lgkmcnt(0)\n" \
    "s_waitcnt vmcnt(12)\n" \
    "s_barrier\n" \
    "ds_read_b64_tr_b16 a[56:57], v112 offset:24576\n" \
    "ds_read_b64_tr_b16 a[58:59], v112 offset:26112\n" \
    "ds_read_b64_tr_b16 a[136:137], v113 offset:24576\n" \
    "ds_read_b64_tr_b16 a[138:139], v113 offset:26112\n" \
    "ds_read_b64_tr_b16 a[216:217], v114 offset:24576\n" \
    "ds_read_b64_tr_b16 a[218:219], v114 offset:26112\n" \
    "ds_read_b64_tr_b16 a[60:61], v112 offset:36864\n" \
    "ds_read_b64_tr_b16 a[62:63], v112 offset:38400\n" \
    "ds_read_b64_tr_b16 a[140:141], v113 offset:36864\n" \
    "ds_read_b64_tr_b16 a[142:143], v113 offset:38400\n" \
    "ds_read_b64_tr_b16 a[220:221], v114 offset:36864\n" \
    "ds_read_b64_tr_b16 a[222:223], v114 offset:38400\n" \
    "s_waitcnt lgkmcnt(0)\n" \
    "s_waitcnt vmcnt(6)\n" \
    "s_barrier\n" \
    "ds_read_b64_tr_b16 a[64:65], v112 offset:49152\n" \
    "ds_read_b64_tr_b16 a[66:67], v112 offset:50688\n" \
    "ds_read_b64_tr_b16 a[144:145], v113 offset:49152\n" \
    "ds_read_b64_tr_b16 a[146:147], v113 offset:50688\n" \
    "ds_read_b64_tr_b16 a[224:225], v114 offset:49152\n" \
    "ds_read_b64_tr_b16 a[226:227], v114 offset:50688\n" \
    "ds_read_b64_tr_b16 a[68:69], v112 offset:61440\n" \
    "ds_read_b64_tr_b16 a[70:71], v112 offset:62976\n" \
    "ds_read_b64_tr_b16 a[148:149], v113 offset:61440\n" \
    "ds_read_b64_tr_b16 a[150:151], v113 offset:62976\n" \
    "ds_read_b64_tr_b16 a[228:229], v114 offset:61440\n" \
    "ds_read_b64_tr_b16 a[230:231], v114 offset:62976\n" \
    "s_waitcnt lgkmcnt(0)\n" \
    "s_waitcnt vmcnt(0)\n" \
    "s_barrier\n" \
    "ds_read_b64_tr_b16 a[72:73], v115 offset:40960\n" \
    "ds_read_b64_tr_b16 a[74:75], v115 offset:42496\n" \
    "ds_read_b64_tr_b16 a[152:153], v116 offset:40960\n" \
    "ds_read_b64_tr_b16 a[154:155], v116 offset:42496\n" \
    "ds_read_b64_tr_b16 a[232:233], v117 offset:40960\n" \
    "ds_read_b64_tr_b16 a[234:235], v117 offset:42496\n" \
    "ds_read_b64_tr_b16 a[76:77], v115 offset:53248\n" \
    "ds_read_b64_tr_b16 a[78:79], v115 offset:54784\n" \
    "ds_read_b64_tr_b16 a[156:157], v116 offset:53248\n" \
    "ds_read_b64_tr_b16 a[158:159], v116 offset:54784\n" \
    "ds_read_b64_tr_b16 a[236:237], v117 offset:53248\n" \
    "ds_read_b64_tr_b16 a[238:239], v117 offset:54784\n" \
    "s_waitcnt lgkmcnt(0)\n" \
    "s_barrier\n" \
    "s_add_u32 s72, s70, 40960\n" \
    "s_add_u32 s63, s70, 250650624\n" \
    "s_add_u32 m0, s72, 0\n" \
    "s_nop 0\n" \
    "buffer_load_dwordx4 %[l16], %[ws], s63 offen lds\n" \
    "buffer_load_dwordx4 %[l16], %[ws], s63 offen offset:1024 lds\n" \
    "buffer_load_dwordx4 %[l16], %[ws], s63 offen offset:2048 lds\n" \
    "buffer_load_dwordx4 %[l16], %[ws], s63 offen offset:3072 lds\n" \
    "s_add_u32 m0, s72, 4096\n" \
    "s_add_u32 s63, s63, 4096\n" \
    "buffer_load_dwordx4 %[l16], %[ws], s63 offen lds\n" \
    "buffer_load_dwordx4 %[l16], %[ws], s63 offen offset:1024 lds\n" \
    "buffer_load_dwordx4 %[l16], %[ws], s63 offen offset:2048 lds\n" \
    "buffer_load_dwordx4 %[l16], %[ws], s63 offen offset:3072 lds\n" \
    "s_add_u32 m0, s72, 8192\n" \
    "s_add_u32 s63, s63, 4096\n" \
    "buffer_load_dwordx4 %[l16], %[ws], s63 offen lds\n" \
    "buffer_load_dwordx4 %[l16], %[ws], s63 offen offset:1024 lds\n" \
    "ds_read_b128 v[16:19], %[l16]\n" \
    "ds_read_b128 v[20:23], %[l16] offset:20480\n" \
    "ds_read_b128 v[24:27], %[l16] offset:1024\n" \
    "ds_read_b128 v[28:31], %[l16] offset:21504\n" \
    "s_mov_b32 s60, 0\n" \
    "s_mov_b32 s64, 0\n" \
    "s_mov_b32 s62, %[ob]\n" \
    "v_mov_b32 v109, %[l16]\n" \
    "v_lshlrev_b32 v120, 16, %[b0]\n" \
    "v_and_b32 v121, 0xffff0000, %[b0]\n" \
    "v_lshlrev_b32 v122, 16, %[b1]\n" \
    "v_and_b32 v123, 0xffff0000, %[b1]\n" \
    "v_lshlrev_b32 v124, 16, %[b2]\n" \
    "v_and_b32 v125, 0xffff0000, %[b2]\n" \
    "v_lshlrev_b32 v126, 16, %[b3]\n" \
    "v_and_b32 v127, 0xffff0000, %[b3]\n" \
    "v_lshlrev_b32 v128, 16, %[b4]\n" \
    "v_and_b32 v129, 0xffff0000, %[b4]\n" \
    "v_lshlrev_b32 v130, 16, %[b5]\n" \
    "v_and_b32 v131, 0xffff0000, %[b5]\n" \
    "v_mov_b32 v108, v109\n" \
    "s_add_u32 s64, s64, 40960\n" \
    "s_cmp_eq_u32 s64, 122880\n" \
    "s_cselect_b32 s64, 0, s64\n" \
    "v_add_u32 v109, s64, %[l16]\n" \
    "s_add_u32 s65, s64, 40960\n" \
    "s_cmp_eq_u32 s65, 122880\n" \
    "s_cselect_b32 s65, 0, s65\n" \
    "s_add_u32 s65, s65, s70\n" \
    "s_min_u32 s63, s60, 61\n" \
    "s_mul_i32 s63, s63, 40960\n" \
    "s_add_u32 s63, s63, s70\n" \
    "s_add_u32 s63, s63, 250691584\n" \
    "s_waitcnt lgkmcnt(3)\n" \
    "v_mfma_f32_16x16x32_bf16 v[48:51], a[0:3], v[16:19], 0\n" \
    "ds_read_b128 v[32:35], v108 offset:2048\n" \
    "v_mfma_f32_16x16x32_bf16 v[52:55], a[80:83], v[16:19], 0\n" \
    "ds_read_b128 v[36:39], v108 offset:22528\n" \
    "v_mfma_f32_16x16x32_bf16 v[56:59], a[160:163], v[16:19], 0\n" \
    "s_waitcnt lgkmcnt(4)\n" \
    "v_mfma_f32_16x16x32_bf16 v[60:63], a[0:3], v[20:23], 0\n" \
    "v_mfma_f32_16x16x32_bf16 v[64:67], a[80:83], v[20:23], 0\n" \
    "v_mfma_f32_16x16x32_bf16 v[68:71], a[160:163], v[20:23], 0\n" \
    "s_waitcnt lgkmcnt(3)\n" \
    "v_mfma_f32_16x16x32_bf16 v[48:51], a[4:7], v[24:27], v[48:51]\n" \
    "ds_read_b128 v[40:43], v108 offset:3072\n" \
    "v_mfma_f32_16x16x32_bf16 v[52:55], a[84:87], v[24:27], v[52:55]\n" \
    "ds_read_b128 v[44:47], v108 offset:23552\n" \
    "v_mfma_f32_16x16x32_bf16 v[56:59], a[164:167], v[24:27], v[56:59]\n" \
    "s_waitcnt lgkmcnt(4)\n" \
    "v_mfma_f32_16x16x32_bf16 v[60:63], a[4:7], v[28:31], v[60:63]\n" \
    "v_mfma_f32_16x16x32_bf16 v[64:67], a[84:87], v[28:31], v[64:67]\n" \
    "v_mfma_f32_16x16x32_bf16 v[68:71], a[164:167], v[28:31], v[68:71]\n" \
    "s_waitcnt lgkmcnt(3)\n" \
    "v_mfma_f32_16x16x32_bf16 v[48:51], a[8:11], v[32:35], v[48:51]\n" \
    "ds_read_b128 v[16:19], v108 offset:4096\n" \
    "v_mfma_f32_16x16x32_bf16 v[52:55], a[88:91], v[32:35], v[52:55]\n" \
    "ds_read_b128 v[20:23], v108 offset:24576\n" \
    "v_mfma_f32_16x16x32_bf16 v[56:59], a[168:171], v[32:35], v[56:59]\n" \
    "s_waitcnt lgkmcnt(4)\n" \
    "v_mfma_f32_16x16x32_bf16 v[60:63], a[8:11], v[36:39], v[60:63]\n" \
    "v_mfma_f32_16x16x32_bf16 v[64:67], a[88:91], v[36:39], v[64:67]\n" \
    "v_mfma_f32_16x16x32_bf16 v[68:71], a[168:171], v[36:39], v[68:71]\n" \
    "s_waitcnt lgkmcnt(3)\n" \
    "v_mfma_f32_16x16x32_bf16 v[48:51], a[12:15], v[40:43], v[48:51]\n" \
    "ds_read_b128 v[24:27], v108 offset:5120\n" \
    "v_mfma_f32_16x16x32_bf16 v[52:55], a[92:95], v[40:43], v[52:55]\n" \
    "ds_read_b128 v[28:31], v108 offset:25600\n" \
    "v_mfma_f32_16x16x32_bf16 v[56:59], a[172:175], v[40:43], v[56:59]\n" \
    "s_waitcnt lgkmcnt(4)\n" \
    "v_mfma_f32_16x16x32_bf16 v[60:63], a[12:15], v[44:47], v[60:63]\n" \
    "v_mfma_f32_16x16x32_bf16 v[64:67], a[92:95], v[44:47], v[64:67]\n" \
    "v_mfma_f32_16x16x32_bf16 v[68:71], a[172:175], v[44:47], v[68:71]\n" \
    "s_waitcnt lgkmcnt(3)\n" \
    "v_mfma_f32_16x16x32_bf16 v[48:51], a[16:19], v[16:19], v[48:51]\n" \
    "ds_read_b128 v[32:35], v108 offset:6144\n" \
    "v_mfma_f32_16x16x32_bf16 v[52:55], a[96:99], v[16:19], v[52:55]\n" \
    "ds_read_b128 v[36:39], v108 offset:26624\n" \
    "v_mfma_f32_16x16x32_bf16 v[56:59], a[176:179], v[16:19], v[56:59]\n" \
    "s_waitcnt lgkmcnt(4)\n" \
    "v_mfma_f32_16x16x32_bf16 v[60:63], a[16:19], v[20:23], v[60:63]\n" \
    "v_mfma_f32_16x16x32_bf16 v[64:67], a[96:99], v[20:23], v[64:67]\n" \
    "v_mfma_f32_16x16x32_bf16 v[68:71], a[176:179], v[20:23], v[68:71]\n" \
    "s_waitcnt lgkmcnt(3)\n" \
    "v_mfma_f32_16x16x32_bf16 v[48:51], a[20:23], v[24:27], v[48:51]\n" \
    "ds_read_b128 v[40:43], v108 offset:7168\n" \
    "v_mfma_f32_16x16x32_bf16 v[52:55], a[100:103], v[24:27], v[52:55]\n" \
    "ds_read_b128 v[44:47], v108 offset:27648\n" \
    "v_mfma_f32_16x16x32_bf16 v[56:59], a[180:183], v[24:27], v[56:59]\n" \
    "s_waitcnt lgkmcnt(4)\n" \
    "v_mfma_f32_16x16x32_bf16 v[60:63], a[20:23], v[28:31], v[60:63]\n" \
    "v_mfma_f32_16x16x32_bf16 v[64:67], a[100:103], v[28:31], v[64:67]\n" \
    "v_mfma_f32_16x16x32_bf16 v[68:71], a[180:183], v[28:31], v[68:71]\n" \
    "s_waitcnt lgkmcnt(3)\n" \
    "v_mfma_f32_16x16x32_bf16 v[48:51], a[24:27], v[32:35], v[48:51]\n" \
    "ds_read_b128 v[16:19], v108 offset:8192\n" \
    "v_mfma_f32_16x16x32_bf16 v[52:55], a[104:107], v[32:35], v[52:55]\n" \
    "ds_read_b128 v[20:23], v108 offset:28672\n" \
    "v_mfma_f32_16x16x32_bf16 v[56:59], a[184:187], v[32:35], v[56:59]\n" \
    "s_waitcnt lgkmcnt(4)\n" \
    "v_mfma_f32_16x16x32_bf16 v[60:63], a[24:27], v[36:39], v[60:63]\n" \
    "v_mfma_f32_16x16x32_bf16 v[64:67], a[104:107], v[36:39], v[64:67]\n" \
    "v_mfma_f32_16x16x32_bf16 v[68:71], a[184:187], v[36:39], v[68:71]\n" \
    "s_waitcnt lgkmcnt(3)\n" \
    "v_mfma_f32_16x16x32_bf16 v[48:51], a[28:31], v[40:43], v[48:51]\n" \
    "ds_read_b128 v[24:27], v108 offset:9216\n" \
    "v_mfma_f32_16x16x32_bf16 v[52:55], a[108:111], v[40:43], v[52:55]\n" \
    "ds_read_b128 v[28:31], v108 offset:29696\n" \
    "v_mfma_f32_16x16x32_bf16 v[56:59], a[188:191], v[40:43], v[56:59]\n" \
    "s_waitcnt lgkmcnt(4)\n" \
    "v_mfma_f32_16x16x32_bf16 v[60:63], a[28:31], v[44:47], v[60:63]\n" \
    "v_mfma_f32_16x16x32_bf16 v[64:67], a[108:111], v[44:47], v[64:67]\n" \
    "v_mfma_f32_16x16x32_bf16 v[68:71], a[188:191], v[44:47], v[68:71]\n" \
    "s_waitcnt lgkmcnt(3)\n" \
    "v_mfma_f32_16x16x32_bf16 v[48:51], a[32:35], v[16:19], v[48:51]\n" \
    "ds_read_b128 v[32:35], v108 offset:10240\n" \
    "v_mfma_f32_16x16x32_bf16 v[52:55], a[112:115], v[16:19], v[52:55]\n" \
    "ds_read_b128 v[36:39], v108 offset:30720\n" \
    "v_mfma_f32_16x16x32_bf16 v[56:59], a[192:195], v[16:19], v[56:59]\n" \
    "s_waitcnt lgkmcnt(4)\n" \
    "v_mfma_f32_16x16x32_bf16 v[60:63], a[32:35], v[20:23], v[60:63]\n" \
    "v_mfma_f32_16x16x32_bf16 v[64:67], a[112:115], v[20:23], v[64:67]\n" \
    "v_mfma_f32_16x16x32_bf16 v[68:71], a[192:195], v[20:23], v[68:71]\n" \
    "s_waitcnt lgkmcnt(3)\n" \
    "v_mfma_f32_16x16x32_bf16 v[48:51], a[36:39], v[24:27], v[48:51]\n" \
    "ds_read_b128 v[40:43], v108 offset:11264\n" \
    "v_mfma_f32_16x16x32_bf16 v[52:55], a[116:119], v[24:27], v[52:55]\n" \
    "ds_read_b128 v[44:47], v108 offset:31744\n" \
    "v_mfma_f32_16x16x32_bf16 v[56:59], a[196:199], v[24:27], v[56:59]\n" \
    "s_waitcnt lgkmcnt(4)\n" \
    "v_mfma_f32_16x16x32_bf16 v[60:63], a[36:39], v[28:31], v[60:63]\n" \
    "v_mfma_f32_16x16x32_bf16 v[64:67], a[116:119], v[28:31], v[64:67]\n" \
    "v_mfma_f32_16x16x32_bf16 v[68:71], a[196:199], v[28:31], v[68:71]\n" \
    "s_waitcnt lgkmcnt(3)\n" \
    "v_mfma_f32_16x16x32_bf16 v[48:51], a[40:43], v[32:35], v[48:51]\n" \
    "ds_read_b128 v[16:19], v108 offset:12288\n" \
    "v_mfma_f32_16x16x32_bf16 v[52:55], a[120:123], v[32:35], v[52:55]\n" \
    "ds_read_b128 v[20:23], v108 offset:32768\n" \
    "v_mfma_f32_16x16x32_bf16 v[56:59], a[200:203], v[32:35], v[56:59]\n" \
    "s_waitcnt lgkmcnt(4)\n" \
    "v_mfma_f32_16x16x32_bf16 v[60:63], a[40:43], v[36:39], v[60:63]\n" \
    "v_mfma_f32_16x16x32_bf16 v[64:67], a[120:123], v[36:39], v[64:67]\n" \
    "v_mfma_f32_16x16x32_bf16 v[68:71], a[200:203], v[36:39], v[68:71]\n" \
    "s_waitcnt lgkmcnt(3)\n" \
    "v_mfma_f32_16x16x32_bf16 v[48:51], a[44:47], v[40:43], v[48:51]\n" \
    "ds_read_b128 v[24:27], v108 offset:13312\n" \
    "v_mfma_f32_16x16x32_bf16 v[52:55], a[124:127], v[40:43], v[52:55]\n" \
    "ds_read_b128 v[28:31], v108 offset:33792\n" \
    "v_mfma_f32_16x16x32_bf16 v[56:59], a[204:207], v[40:43], v[56:59]\n" \
    "s_waitcnt lgkmcnt(4)\n" \
    "v_mfma_f32_16x16x32_bf16 v[60:63], a[44:47], v[44:47], v[60:63]\n" \
    "v_mfma_f32_16x16x32_bf16 v[64:67], a[124:127], v[44:47], v[64:67]\n" \
    "v_mfma_f32_16x16x32_bf16 v[68:71], a[204:207], v[44:47], v[68:71]\n" \
    "s_waitcnt lgkmcnt(3)\n" \
    "v_mfma_f32_16x16x32_bf16 v[48:51], a[48:51], v[16:19], v[48:51]\n" \
    "ds_read_b128 v[32:35], v108 offset:14336\n" \
    "v_mfma_f32_16x16x32_bf16 v[52:55], a[128:131], v[16:19], v[52:55]\n" \
    "ds_read_b128 v[36:39], v108 offset:34816\n" \
    "v_mfma_f32_16x16x32_bf16 v[56:59], a[208:211], v[16:19], v[56:59]\n" \
    "s_waitcnt lgkmcnt(4)\n" \
    "v_mfma_f32_16x16x32_bf16 v[60:63], a[48:51], v[20:23], v[60:63]\n" \
    "v_mfma_f32_16x16x32_bf16 v[64:67], a[128:131], v[20:23], v[64:67]\n" \
    "v_mfma_f32_16x16x32_bf16 v[68:71], a[208:211], v[20:23], v[68:71]\n" \
    "s_waitcnt vmcnt(0)\n" \
    "s_barrier\n" \
    "s_waitcnt lgkmcnt(3)\n" \
    "v_mfma_f32_16x16x32_bf16 v[48:51], a[52:55], v[24:27], v[48:51]\n" \
    "ds_read_b128 v[40:43], v108 offset:15360\n" \
    "v_mfma_f32_16x16x32_bf16 v[52:55], a[132:135], v[24:27], v[52:55]\n" \
    "ds_read_b128 v[44:47], v108 offset:35840\n" \
    "v_mfma_f32_16x16x32_bf16 v[56:59], a[212:215], v[24:27], v[56:59]\n" \
    "s_add_u32 m0, s65, 0\n" \
    "s_nop 0\n" \
    "buffer_load_dwordx4 %[l16], %[ws], s63 offen lds\n" \
    "s_waitcnt lgkmcnt(4)\n" \
    "v_mfma_f32_16x16x32_bf16 v[60:63], a[52:55], v[28:31], v[60:63]\n" \
    "v_mfma_f32_16x16x32_bf16 v[64:67], a[132:135], v[28:31], v[64:67]\n" \
    "buffer_load_dwordx4 %[l16], %[ws], s63 offen offset:1024 lds\n" \
    "v_mfma_f32_16x16x32_bf16 v[68:71], a[212:215], v[28:31], v[68:71]\n" \
    "s_waitcnt lgkmcnt(3)\n" \
    "v_mfma_f32_16x16x32_bf16 v[48:51], a[56:59], v[32:35], v[48:51]\n" \
    "ds_read_b128 v[16:19], v108 offset:16384\n" \
    "v_mfma_f32_16x16x32_bf16 v[52:55], a[136:139], v[32:35], v[52:55]\n" \
    "ds_read_b128 v[20:23], v108 offset:36864\n" \
    "v_mfma_f32_16x16x32_bf16 v[56:59], a[216:219], v[32:35], v[56:59]\n" \
    "buffer_load_dwordx4 %[l16], %[ws], s63 offen offset:2048 lds\n" \
    "s_waitcnt lgkmcnt(4)\n" \
    "v_mfma_f32_16x16x32_bf16 v[60:63], a[56:59], v[36:39], v[60:63]\n" \
    "v_mfma_f32_16x16x32_bf16 v[64:67], a[136:139], v[36:39], v[64:67]\n" \
    "buffer_load_dwordx4 %[l16], %[ws], s63 offen offset:3072 lds\n" \
    "v_mfma_f32_16x16x32_bf16 v[68:71], a[216:219], v[36:39], v[68:71]\n" \
    "s_waitcnt lgkmcnt(3)\n" \
    "v_mfma_f32_16x16x32_bf16 v[48:51], a[60:63], v[40:43], v[48:51]\n" \
    "ds_read_b128 v[24:27], v108 offset:17408\n" \
    "v_mfma_f32_16x16x32_bf16 v[52:55], a[140:143], v[40:43], v[52:55]\n" \
    "ds_read_b128 v[28:31], v108 offset:37888\n" \
    "v_mfma_f32_16x16x32_bf16 v[56:59], a[220:223], v[40:43], v[56:59]\n" \
    "s_add_u32 m0, s65, 4096\n" \
    "s_add_u32 s63, s63, 4096\n" \
    "buffer_load_dwordx4 %[l16], %[ws], s63 offen lds\n" \
    "s_waitcnt lgkmcnt(4)\n" \
    "v_mfma_f32_16x16x32_bf16 v[60:63], a[60:63], v[44:47], v[60:63]\n" \
    "v_mfma_f32_16x16x32_bf16 v[64:67], a[140:143], v[44:47], v[64:67]\n" \
    "buffer_load_dwordx4 %[l16], %[ws], s63 offen offset:1024 lds\n" \
    "v_mfma_f32_16x16x32_bf16 v[68:71], a[220:223], v[44:47], v[68:71]\n" \
    "s_waitcnt lgkmcnt(3)\n" \
    "v_mfma_f32_16x16x32_bf16 v[48:51], a[64:67], v[16:19], v[48:51]\n" \
    "ds_read_b128 v[32:35], v108 offset:18432\n" \
    "v_mfma_f32_16x16x32_bf16 v[52:55], a[144:147], v[16:19], v[52:55]\n" \
    "ds_read_b128 v[36:39], v108 offset:38912\n" \
    "v_mfma_f32_16x16x32_bf16 v[56:59], a[224:227], v[16:19], v[56:59]\n" \
    "buffer_load_dwordx4 %[l16], %[ws], s63 offen offset:2048 lds\n" \
    "s_waitcnt lgkmcnt(4)\n" \
    "v_mfma_f32_16x16x32_bf16 v[60:63], a[64:67], v[20:23], v[60:63]\n" \
    "v_mfma_f32_16x16x32_bf16 v[64:67], a[144:147], v[20:23], v[64:67]\n" \
    "buffer_load_dwordx4 %[l16], %[ws], s63 offen offset:3072 lds\n" \
    "v_mfma_f32_16x16x32_bf16 v[68:71], a[224:227], v[20:23], v[68:71]\n" \
    "s_waitcnt lgkmcnt(3)\n" \
    "v_mfma_f32_16x16x32_bf16 v[48:51], a[68:71], v[24:27], v[48:51]\n" \
    "ds_read_b128 v[40:43], v108 offset:19456\n" \
    "v_mfma_f32_16x16x32_bf16 v[52:55], a[148:151], v[24:27], v[52:55]\n" \
    "ds_read_b128 v[44:47], v108 offset:39936\n" \
    "v_mfma_f32_16x16x32_bf16 v[56:59], a[228:231], v[24:27], v[56:59]\n" \
    "s_add_u32 m0, s65, 8192\n" \
    "s_add_u32 s63, s63, 4096\n" \
    "buffer_load_dwordx4 %[l16], %[ws], s63 offen lds\n" \
    "s_waitcnt lgkmcnt(4)\n" \
    "v_mfma_f32_16x16x32_bf16 v[60:63], a[68:71], v[28:31], v[60:63]\n" \
    "v_mfma_f32_16x16x32_bf16 v[64:67], a[148:151], v[28:31], v[64:67]\n" \
    "buffer_load_dwordx4 %[l16], %[ws], s63 offen offset:1024 lds\n" \
    "v_mfma_f32_16x16x32_bf16 v[68:71], a[228:231], v[28:31], v[68:71]\n" \
    "s_waitcnt lgkmcnt(3)\n" \
    "v_mfma_f32_16x16x32_bf16 v[48:51], a[72:75], v[32:35], v[48:51]\n" \
    "ds_read_b128 v[16:19], v109\n" \
    "v_mfma_f32_16x16x32_bf16 v[52:55], a[152:155], v[32:35], v[52:55]\n" \
    "ds_read_b128 v[20:23], v109 offset:20480\n" \
    "v_mfma_f32_16x16x32_bf16 v[56:59], a[232:235], v[32:35], v[56:59]\n" \
    "s_waitcnt lgkmcnt(4)\n" \
    "v_mfma_f32_16x16x32_bf16 v[60:63], a[72:75], v[36:39], v[60:63]\n" \
    "v_mfma_f32_16x16x32_bf16 v[64:67], a[152:155], v[36:39], v[64:67]\n" \
    "v_mfma_f32_16x16x32_bf16 v[68:71], a[232:235], v[36:39], v[68:71]\n" \
    "s_waitcnt lgkmcnt(3)\n" \
    "v_mfma_f32_16x16x32_bf16 v[48:51], a[76:79], v[40:43], v[48:51]\n" \
    "ds_read_b128 v[24:27], v109 offset:1024\n" \
    "v_mfma_f32_16x16x32_bf16 v[52:55], a[156:159], v[40:43], v[52:55]\n" \
    "ds_read_b128 v[28:31], v109 offset:21504\n" \
    "v_mfma_f32_16x16x32_bf16 v[56:59], a[236:239], v[40:43], v[56:59]\n" \
    "s_waitcnt lgkmcnt(4)\n" \
    "v_mfma_f32_16x16x32_bf16 v[60:63], a[76:79], v[44:47], v[60:63]\n" \
    "v_mfma_f32_16x16x32_bf16 v[64:67], a[156:159], v[44:47], v[64:67]\n" \
    "v_mfma_f32_16x16x32_bf16 v[68:71], a[236:239], v[44:47], v[68:71]\n" \
    "s_add_u32 s60, s60, 1\n" \
    "v_cvt_pk_bf16_f32 v49, v48, v49\n" \
    "v_cvt_pk_bf16_f32 v51, v50, v51\n" \
    "s_nop 2\n" \
    "v_cvt_pk_bf16_f32 v61, v60, v61\n" \
    "v_cvt_pk_bf16_f32 v63, v62, v63\n" \
    "v_lshlrev_b32 v48, 16, v49\n" \
    "v_lshlrev_b32 v50, 16, v51\n" \
    "v_lshlrev_b32 v60, 16, v61\n" \
    "v_lshlrev_b32 v62, 16, v63\n" \
    "v_and_b32 v49, 0xffff0000, v49\n" \
    "v_and_b32 v51, 0xffff0000, v51\n" \
    "v_and_b32 v61, 0xffff0000, v61\n" \
    "v_and_b32 v63, 0xffff0000, v63\n" \
    "v_add_f32 v48, v48, v120\n" \
    "v_add_f32 v50, v50, v122\n" \
    "v_add_f32 v60, v60, v120\n" \
    "v_add_f32 v62, v62, v122\n" \
    "v_add_f32 v49, v49, v121\n" \
    "v_add_f32 v51, v51, v123\n" \
    "v_add_f32 v61, v61, v121\n" \
    "v_add_f32 v63, v63, v123\n" \
    "v_cvt_pk_bf16_f32 v49, v48, v49\n" \
    "v_cvt_pk_bf16_f32 v51, v50, v51\n" \
    "v_cvt_pk_bf16_f32 v61, v60, v61\n" \
    "v_cvt_pk_bf16_f32 v63, v62, v63\n" \
    "v_lshlrev_b32 v48, 16, v49\n" \
    "v_lshlrev_b32 v50, 16, v51\n" \
    "v_lshlrev_b32 v60, 16, v61\n" \
    "v_lshlrev_b32 v62, 16, v63\n" \
    "v_and_b32 v49, 0xffff0000, v49\n" \
    "v_and_b32 v51, 0xffff0000, v51\n" \
    "v_and_b32 v61, 0xffff0000, v61\n" \
    "v_and_b32 v63, 0xffff0000, v63\n" \
    "v_max_f32 v48, 0, v48\n" \
    "v_max_f32 v50, 0, v50\n" \
    "v_max_f32 v60, 0, v60\n" \
    "v_max_f32 v62, 0, v62\n" \
    "v_max_f32 v49, 0, v49\n" \
    "v_max_f32 v51, 0, v51\n" \
    "v_max_f32 v61, 0, v61\n" \
    "v_max_f32 v63, 0, v63\n" \
    "v_mul_f32 v48, v48, v48\n" \
    "v_mul_f32 v50, v50, v50\n" \
    "v_mul_f32 v60, v60, v60\n" \
    "v_mul_f32 v62, v62, v62\n" \
    "v_mul_f32 v49, v49, v49\n" \
    "v_mul_f32 v51, v51, v51\n" \
    "v_mul_f32 v61, v61, v61\n" \
    "v_mul_f32 v63, v63, v63\n" \
    "v_cvt_pk_bf16_f32 v96, v48, v49\n" \
    "v_cvt_pk_bf16_f32 v97, v50, v51\n" \
    "v_cvt_pk_bf16_f32 v98, v60, v61\n" \
    "v_cvt_pk_bf16_f32 v99, v62, v63\n" \
    "buffer_store_dwordx4 v[96:99], %[l16], %[ws], s62 offen sc1\n" \
    "v_cvt_pk_bf16_f32 v53, v52, v53\n" \
    "v_cvt_pk_bf16_f32 v55, v54, v55\n" \
    "v_cvt_pk_bf16_f32 v65, v64, v65\n" \
    "v_cvt_pk_bf16_f32 v67, v66, v67\n" \
    "v_lshlrev_b32 v52, 16, v53\n" \
    "v_lshlrev_b32 v54, 16, v55\n" \
    "v_lshlrev_b32 v64, 16, v65\n" \
    "v_lshlrev_b32 v66, 16, v67\n" \
    "v_and_b32 v53, 0xffff0000, v53\n" \
    "v_and_b32 v55, 0xffff0000, v55\n" \
    "v_and_b32 v65, 0xffff0000, v65\n" \
    "v_and_b32 v67, 0xffff0000, v67\n" \
    "v_add_f32 v52, v52, v124\n" \
    "v_add_f32 v54, v54, v126\n" \
    "v_add_f32 v64, v64, v124\n" \
    "v_add_f32 v66, v66, v126\n" \
    "v_add_f32 v53, v53, v125\n" \
    "v_add_f32 v55, v55, v127\n" \
    "v_add_f32 v65, v65, v125\n" \
    "v_add_f32 v67, v67, v127\n" \
    "v_cvt_pk_bf16_f32 v53, v52, v53\n" \
    "v_cvt_pk_bf16_f32 v55, v54, v55\n" \
    "v_cvt_pk_bf16_f32 v65, v64, v65\n" \
    "v_cvt_pk_bf16_f32 v67, v66, v67\n" \
    "v_lshlrev_b32 v52, 16, v53\n" \
    "v_lshlrev_b32 v54, 16, v55\n" \
    "v_lshlrev_b32 v64, 16, v65\n" \
    "v_lshlrev_b32 v66, 16, v67\n" \
    "v_and_b32 v53, 0xffff0000, v53\n" \
    "v_and_b32 v55, 0xffff0000, v55\n" \
    "v_and_b32 v65, 0xffff0000, v65\n" \
    "v_and_b32 v67, 0xffff0000, v67\n" \
    "v_max_f32 v52, 0, v52\n" \
    "v_max_f32 v54, 0, v54\n" \
    "v_max_f32 v64, 0, v64\n" \
    "v_max_f32 v66, 0, v66\n" \
    "v_max_f32 v53, 0, v53\n" \
    "v_max_f32 v55, 0, v55\n" \
    "v_max_f32 v65, 0, v65\n" \
    "v_max_f32 v67, 0, v67\n" \
    "v_mul_f32 v52, v52, v52\n" \
    "v_mul_f32 v54, v54, v54\n" \
    "v_mul_f32 v64, v64, v64\n" \
    "v_mul_f32 v66, v66, v66\n" \
    "v_mul_f32 v53, v53, v53\n" \
    "v_mul_f32 v55, v55, v55\n" \
    "v_mul_f32 v65, v65, v65\n" \
    "v_mul_f32 v67, v67, v67\n" \
    "v_cvt_pk_bf16_f32 v100, v52, v53\n" \
    "v_cvt_pk_bf16_f32 v101, v54, v55\n" \
    "v_cvt_pk_bf16_f32 v102, v64, v65\n" \
    "v_cvt_pk_bf16_f32 v103, v66, v67\n" \
    "buffer_store_dwordx4 v[100:103], %[l16], %[ws], s62 offen offset:1024 sc1\n" \
    "v_cvt_pk_bf16_f32 v57, v56, v57\n" \
    "v_cvt_pk_bf16_f32 v59, v58, v59\n" \
    "v_cvt_pk_bf16_f32 v69, v68, v69\n" \
    "v_cvt_pk_bf16_f32 v71, v70, v71\n" \
    "v_lshlrev_b32 v56, 16, v57\n" \
    "v_lshlrev_b32 v58, 16, v59\n" \
    "v_lshlrev_b32 v68, 16, v69\n" \
    "v_lshlrev_b32 v70, 16, v71\n" \
    "v_and_b32 v57, 0xffff0000, v57\n" \
    "v_and_b32 v59, 0xffff0000, v59\n" \
    "v_and_b32 v69, 0xffff0000, v69\n" \
    "v_and_b32 v71, 0xffff0000, v71\n" \
    "v_add_f32 v56, v56, v128\n" \
    "v_add_f32 v58, v58, v130\n" \
    "v_add_f32 v68, v68, v128\n" \
    "v_add_f32 v70, v70, v130\n" \
    "v_add_f32 v57, v57, v129\n" \
    "v_add_f32 v59, v59, v131\n" \
    "v_add_f32 v69, v69, v129\n" \
    "v_add_f32 v71, v71, v131\n" \
    "v_cvt_pk_bf16_f32 v57, v56, v57\n" \
    "v_cvt_pk_bf16_f32 v59, v58, v59\n" \
    "v_cvt_pk_bf16_f32 v69, v68, v69\n" \
    "v_cvt_pk_bf16_f32 v71, v70, v71\n" \
    "v_lshlrev_b32 v56, 16, v57\n" \
    "v_lshlrev_b32 v58, 16, v59\n" \
    "v_lshlrev_b32 v68, 16, v69\n" \
    "v_lshlrev_b32 v70, 16, v71\n" \
    "v_and_b32 v57, 0xffff0000, v57\n" \
    "v_and_b32 v59, 0xffff0000, v59\n" \
    "v_and_b32 v69, 0xffff0000, v69\n" \
    "v_and_b32 v71, 0xffff0000, v71\n" \
    "v_max_f32 v56, 0, v56\n" \
    "v_max_f32 v58, 0, v58\n" \
    "v_max_f32 v68, 0, v68\n" \
    "v_max_f32 v70, 0, v70\n" \
    "v_max_f32 v57, 0, v57\n" \
    "v_max_f32 v59, 0, v59\n" \
    "v_max_f32 v69, 0, v69\n" \
    "v_max_f32 v71, 0, v71\n" \
    "v_mul_f32 v56, v56, v56\n" \
    "v_mul_f32 v58, v58, v58\n" \
    "v_mul_f32 v68, v68, v68\n" \
    "v_mul_f32 v70, v70, v70\n" \
    "v_mul_f32 v57, v57, v57\n" \
    "v_mul_f32 v59, v59, v59\n" \
    "v_mul_f32 v69, v69, v69\n" \
    "v_mul_f32 v71, v71, v71\n" \
    "v_cvt_pk_bf16_f32 v104, v56, v57\n" \
    "v_cvt_pk_bf16_f32 v105, v58, v59\n" \
    "v_cvt_pk_bf16_f32 v106, v68, v69\n" \
    "v_cvt_pk_bf16_f32 v107, v70, v71\n" \
    "buffer_store_dwordx4 v[104:107], %[l16], %[ws], s62 offen offset:2048 sc1\n" \
    "s_waitcnt vmcnt(0)\n" \
    "s_barrier\n" \
    "s_cmp_lg_u32 %[wave], 0\n" \
    "s_cbranch_scc1 Lf_fla\n" \
    "v_mov_b32 v110, s60\n" \
    "buffer_store_dword v110, %[one], %[ws], %[flag] offen sc1\n" \
    "Lf_fla:\n" \
    "s_add_u32 s62, s62, 98304\n" \
    "v_mov_b32 v108, v109\n" \
    "s_add_u32 s64, s64, 40960\n" \
    "s_cmp_eq_u32 s64, 122880\n" \
    "s_cselect_b32 s64, 0, s64\n" \
    "v_add_u32 v109, s64, %[l16]\n" \
    "s_add_u32 s65, s64, 40960\n" \
    "s_cmp_eq_u32 s65, 122880\n" \
    "s_cselect_b32 s65, 0, s65\n" \
    "s_add_u32 s65, s65, s70\n" \
    "s_min_u32 s63, s60, 61\n" \
    "s_mul_i32 s63, s63, 40960\n" \
    "s_add_u32 s63, s63, s70\n" \
    "s_add_u32 s63, s63, 250691584\n" \
    "s_waitcnt lgkmcnt(3)\n" \
    "v_mfma_f32_16x16x32_bf16 v[72:75], a[0:3], v[16:19], 0\n" \
    "ds_read_b128 v[32:35], v108 offset:2048\n" \
    "v_mfma_f32_16x16x32_bf16 v[76:79], a[80:83], v[16:19], 0\n" \
    "ds_read_b128 v[36:39], v108 offset:22528\n" \
    "v_mfma_f32_16x16x32_bf16 v[80:83], a[160:163], v[16:19], 0\n" \
    "s_waitcnt lgkmcnt(4)\n" \
    "v_mfma_f32_16x16x32_bf16 v[84:87], a[0:3], v[20:23], 0\n" \
    "v_mfma_f32_16x16x32_bf16 v[88:91], a[80:83], v[20:23], 0\n" \
    "v_mfma_f32_16x16x32_bf16 v[92:95], a[160:163], v[20:23], 0\n" \
    "s_waitcnt lgkmcnt(3)\n" \
    "v_mfma_f32_16x16x32_bf16 v[72:75], a[4:7], v[24:27], v[72:75]\n" \
    "ds_read_b128 v[40:43], v108 offset:3072\n" \
    "v_mfma_f32_16x16x32_bf16 v[76:79], a[84:87], v[24:27], v[76:79]\n" \
    "ds_read_b128 v[44:47], v108 offset:23552\n" \
    "v_mfma_f32_16x16x32_bf16 v[80:83], a[164:167], v[24:27], v[80:83]\n" \
    "s_waitcnt lgkmcnt(4)\n" \
    "v_mfma_f32_16x16x32_bf16 v[84:87], a[4:7], v[28:31], v[84:87]\n" \
    "v_mfma_f32_16x16x32_bf16 v[88:91], a[84:87], v[28:31], v[88:91]\n" \
    "v_mfma_f32_16x16x32_bf16 v[92:95], a[164:167], v[28:31], v[92:95]\n" \
    "s_waitcnt lgkmcnt(3)\n" \
    "v_mfma_f32_16x16x32_bf16 v[72:75], a[8:11], v[32:35], v[72:75]\n" \
    "ds_read_b128 v[16:19], v108 offset:4096\n" \
    "v_mfma_f32_16x16x32_bf16 v[76:79], a[88:91], v[32:35], v[76:79]\n" \
    "ds_read_b128 v[20:23], v108 offset:24576\n" \
    "v_mfma_f32_16x16x32_bf16 v[80:83], a[168:171], v[32:35], v[80:83]\n" \
    "s_waitcnt lgkmcnt(4)\n" \
    "v_mfma_f32_16x16x32_bf16 v[84:87], a[8:11], v[36:39], v[84:87]\n" \
    "v_mfma_f32_16x16x32_bf16 v[88:91], a[88:91], v[36:39], v[88:91]\n" \
    "v_mfma_f32_16x16x32_bf16 v[92:95], a[168:171], v[36:39], v[92:95]\n" \
    "s_waitcnt lgkmcnt(3)\n" \
    "v_mfma_f32_16x16x32_bf16 v[72:75], a[12:15], v[40:43], v[72:75]\n" \
    "ds_read_b128 v[24:27], v108 offset:5120\n" \
    "v_mfma_f32_16x16x32_bf16 v[76:79], a[92:95], v[40:43], v[76:79]\n" \
    "ds_read_b128 v[28:31], v108 offset:25600\n" \
    "v_mfma_f32_16x16x32_bf16 v[80:83], a[172:175], v[40:43], v[80:83]\n" \
    "s_waitcnt lgkmcnt(4)\n" \
    "v_mfma_f32_16x16x32_bf16 v[84:87], a[12:15], v[44:47], v[84:87]\n" \
    "v_mfma_f32_16x16x32_bf16 v[88:91], a[92:95], v[44:47], v[88:91]\n" \
    "v_mfma_f32_16x16x32_bf16 v[92:95], a[172:175], v[44:47], v[92:95]\n" \
    "s_waitcnt vmcnt(0)\n" \
    "s_barrier\n" \
    "s_waitcnt lgkmcnt(3)\n" \
    "v_mfma_f32_16x16x32_bf16 v[72:75], a[16:19], v[16:19], v[72:75]\n" \
    "ds_read_b128 v[32:35], v108 offset:6144\n" \
    "v_mfma_f32_16x16x32_bf16 v[76:79], a[96:99], v[16:19], v[76:79]\n" \
    "ds_read_b128 v[36:39], v108 offset:26624\n" \
    "v_mfma_f32_16x16x32_bf16 v[80:83], a[176:179], v[16:19], v[80:83]\n" \
    "s_add_u32 m0, s65, 0\n" \
    "s_nop 0\n" \
    "buffer_load_dwordx4 %[l16], %[ws], s63 offen lds\n" \
    "s_waitcnt lgkmcnt(4)\n" \
    "v_mfma_f32_16x16x32_bf16 v[84:87], a[16:19], v[20:23], v[84:87]\n" \
    "v_mfma_f32_16x16x32_bf16 v[88:91], a[96:99], v[20:23], v[88:91]\n" \
    "buffer_load_dwordx4 %[l16], %[ws], s63 offen offset:1024 lds\n" \
    "v_mfma_f32_16x16x32_bf16 v[92:95], a[176:179], v[20:23], v[92:95]\n" \
    "s_waitcnt lgkmcnt(3)\n" \
    "v_mfma_f32_16x16x32_bf16 v[72:75], a[20:23], v[24:27], v[72:75]\n" \
    "ds_read_b128 v[40:43], v108 offset:7168\n" \
    "v_mfma_f32_16x16x32_bf16 v[76:79], a[100:103], v[24:27], v[76:79]\n" \
    "ds_read_b128 v[44:47], v108 offset:27648\n" \
    "v_mfma_f32_16x16x32_bf16 v[80:83], a[180:183], v[24:27], v[80:83]\n" \
    "buffer_load_dwordx4 %[l16], %[ws], s63 offen offset:2048 lds\n" \
    "s_waitcnt lgkmcnt(4)\n" \
    "v_mfma_f32_16x16x32_bf16 v[84:87], a[20:23], v[28:31], v[84:87]\n" \
    "v_mfma_f32_16x16x32_bf16 v[88:91], a[100:103], v[28:31], v[88:91]\n" \
    "buffer_load_dwordx4 %[l16], %[ws], s63 offen offset:3072 lds\n" \
    "v_mfma_f32_16x16x32_bf16 v[92:95], a[180:183], v[28:31], v[92:95]\n" \
    "s_waitcnt lgkmcnt(3)\n" \
    "v_mfma_f32_16x16x32_bf16 v[72:75], a[24:27], v[32:35], v[72:75]\n" \
    "ds_read_b128 v[16:19], v108 offset:8192\n" \
    "v_mfma_f32_16x16x32_bf16 v[76:79], a[104:107], v[32:35], v[76:79]\n" \
    "ds_read_b128 v[20:23], v108 offset:28672\n" \
    "v_mfma_f32_16x16x32_bf16 v[80:83], a[184:187], v[32:35], v[80:83]\n" \
    "s_add_u32 m0, s65, 4096\n" \
    "s_add_u32 s63, s63, 4096\n" \
    "buffer_load_dwordx4 %[l16], %[ws], s63 offen lds\n" \
    "s_waitcnt lgkmcnt(4)\n" \
    "v_mfma_f32_16x16x32_bf16 v[84:87], a[24:27], v[36:39], v[84:87]\n" \
    "v_mfma_f32_16x16x32_bf16 v[88:91], a[104:107], v[36:39], v[88:91]\n" \
    "buffer_load_dwordx4 %[l16], %[ws], s63 offen offset:1024 lds\n" \
    "v_mfma_f32_16x16x32_bf16 v[92:95], a[184:187], v[36:39], v[92:95]\n" \
    "s_waitcnt lgkmcnt(3)\n" \
    "v_mfma_f32_16x16x32_bf16 v[72:75], a[28:31], v[40:43], v[72:75]\n" \
    "ds_read_b128 v[24:27], v108 offset:9216\n" \
    "v_mfma_f32_16x16x32_bf16 v[76:79], a[108:111], v[40:43], v[76:79]\n" \
    "ds_read_b128 v[28:31], v108 offset:29696\n" \
    "v_mfma_f32_16x16x32_bf16 v[80:83], a[188:191], v[40:43], v[80:83]\n" \
    "buffer_load_dwordx4 %[l16], %[ws], s63 offen offset:2048 lds\n" \
    "s_waitcnt lgkmcnt(4)\n" \
    "v_mfma_f32_16x16x32_bf16 v[84:87], a[28:31], v[44:47], v[84:87]\n" \
    "v_mfma_f32_16x16x32_bf16 v[88:91], a[108:111], v[44:47], v[88:91]\n" \
    "buffer_load_dwordx4 %[l16], %[ws], s63 offen offset:3072 lds\n" \
    "v_mfma_f32_16x16x32_bf16 v[92:95], a[188:191], v[44:47], v[92:95]\n" \
    "s_waitcnt lgkmcnt(3)\n" \
    "v_mfma_f32_16x16x32_bf16 v[72:75], a[32:35], v[16:19], v[72:75]\n" \
    "ds_read_b128 v[32:35], v108 offset:10240\n" \
    "v_mfma_f32_16x16x32_bf16 v[76:79], a[112:115], v[16:19], v[76:79]\n" \
    "ds_read_b128 v[36:39], v108 offset:30720\n" \
    "v_mfma_f32_16x16x32_bf16 v[80:83], a[192:195], v[16:19], v[80:83]\n" \
    "s_add_u32 m0, s65, 8192\n" \
    "s_add_u32 s63, s63, 4096\n" \
    "buffer_load_dwordx4 %[l16], %[ws], s63 offen lds\n" \
    "s_waitcnt lgkmcnt(4)\n" \
    "v_mfma_f32_16x16x32_bf16 v[84:87], a[32:35], v[20:23], v[84:87]\n" \
    "v_mfma_f32_16x16x32_bf16 v[88:91], a[112:115], v[20:23], v[88:91]\n" \
    "buffer_load_dwordx4 %[l16], %[ws], s63 offen offset:1024 lds\n" \
    "v_mfma_f32_16x16x32_bf16 v[92:95], a[192:195], v[20:23], v[92:95]\n" \
    "s_waitcnt lgkmcnt(3)\n" \
    "v_mfma_f32_16x16x32_bf16 v[72:75], a[36:39], v[24:27], v[72:75]\n" \
    "ds_read_b128 v[40:43], v108 offset:11264\n" \
    "v_mfma_f32_16x16x32_bf16 v[76:79], a[116:119], v[24:27], v[76:79]\n" \
    "ds_read_b128 v[44:47], v108 offset:31744\n" \
    "v_mfma_f32_16x16x32_bf16 v[80:83], a[196:199], v[24:27], v[80:83]\n" \
    "s_waitcnt lgkmcnt(4)\n" \
    "v_mfma_f32_16x16x32_bf16 v[84:87], a[36:39], v[28:31], v[84:87]\n" \
    "v_mfma_f32_16x16x32_bf16 v[88:91], a[116:119], v[28:31], v[88:91]\n" \
    "v_mfma_f32_16x16x32_bf16 v[92:95], a[196:199], v[28:31], v[92:95]\n" \
    "s_waitcnt lgkmcnt(3)\n" \
    "v_mfma_f32_16x16x32_bf16 v[72:75], a[40:43], v[32:35], v[72:75]\n" \
    "ds_read_b128 v[16:19], v108 offset:12288\n" \
    "v_mfma_f32_16x16x32_bf16 v[76:79], a[120:123], v[32:35], v[76:79]\n" \
    "ds_read_b128 v[20:23], v108 offset:32768\n" \
    "v_mfma_f32_16x16x32_bf16 v[80:83], a[200:203], v[32:35], v[80:83]\n" \
    "s_waitcnt lgkmcnt(4)\n" \
    "v_mfma_f32_16x16x32_bf16 v[84:87], a[40:43], v[36:39], v[84:87]\n" \
    "v_mfma_f32_16x16x32_bf16 v[88:91], a[120:123], v[36:39], v[88:91]\n" \
    "v_mfma_f32_16x16x32_bf16 v[92:95], a[200:203], v[36:39], v[92:95]\n" \
    "s_waitcnt lgkmcnt(3)\n" \
    "v_mfma_f32_16x16x32_bf16 v[72:75], a[44:47], v[40:43], v[72:75]\n" \
    "ds_read_b128 v[24:27], v108 offset:13312\n" \
    "v_mfma_f32_16x16x32_bf16 v[76:79], a[124:127], v[40:43], v[76:79]\n" \
    "ds_read_b128 v[28:31], v108 offset:33792\n" \
    "v_mfma_f32_16x16x32_bf16 v[80:83], a[204:207], v[40:43], v[80:83]\n" \
    "s_waitcnt lgkmcnt(4)\n" \
    "v_mfma_f32_16x16x32_bf16 v[84:87], a[44:47], v[44:47], v[84:87]\n" \
    "v_mfma_f32_16x16x32_bf16 v[88:91], a[124:127], v[44:47], v[88:91]\n" \
    "v_mfma_f32_16x16x32_bf16 v[92:95], a[204:207], v[44:47], v[92:95]\n" \
    "s_waitcnt lgkmcnt(3)\n" \
    "v_mfma_f32_16x16x32_bf16 v[72:75], a[48:51], v[16:19], v[72:75]\n" \
    "ds_read_b128 v[32:35], v108 offset:14336\n" \
    "v_mfma_f32_16x16x32_bf16 v[76:79], a[128:131], v[16:19], v[76:79]\n" \
    "ds_read_b128 v[36:39], v108 offset:34816\n" \
    "v_mfma_f32_16x16x32_bf16 v[80:83], a[208:211], v[16:19], v[80:83]\n" \
    "s_waitcnt lgkmcnt(4)\n" \
    "v_mfma_f32_16x16x32_bf16 v[84:87], a[48:51], v[20:23], v[84:87]\n" \
    "v_mfma_f32_16x16x32_bf16 v[88:91], a[128:131], v[20:23], v[88:91]\n" \
    "v_mfma_f32_16x16x32_bf16 v[92:95], a[208:211], v[20:23], v[92:95]\n" \
    "s_waitcnt lgkmcnt(3)\n" \
    "v_mfma_f32_16x16x32_bf16 v[72:75], a[52:55], v[24:27], v[72:75]\n" \
    "ds_read_b128 v[40:43], v108 offset:15360\n" \
    "v_mfma_f32_16x16x32_bf16 v[76:79], a[132:135], v[24:27], v[76:79]\n" \
    "ds_read_b128 v[44:47], v108 offset:35840\n" \
    "v_mfma_f32_16x16x32_bf16 v[80:83], a[212:215], v[24:27], v[80:83]\n" \
    "s_waitcnt lgkmcnt(4)\n" \
    "v_mfma_f32_16x16x32_bf16 v[84:87], a[52:55], v[28:31], v[84:87]\n" \
    "v_mfma_f32_16x16x32_bf16 v[88:91], a[132:135], v[28:31], v[88:91]\n" \
    "v_mfma_f32_16x16x32_bf16 v[92:95], a[212:215], v[28:31], v[92:95]\n" \
    "s_waitcnt lgkmcnt(3)\n" \
    "v_mfma_f32_16x16x32_bf16 v[72:75], a[56:59], v[32:35], v[72:75]\n" \
    "ds_read_b128 v[16:19], v108 offset:16384\n" \
    "v_mfma_f32_16x16x32_bf16 v[76:79], a[136:139], v[32:35], v[76:79]\n" \
    "ds_read_b128 v[20:23], v108 offset:36864\n" \
    "v_mfma_f32_16x16x32_bf16 v[80:83], a[216:219], v[32:35], v[80:83]\n" \
    "s_waitcnt lgkmcnt(4)\n" \
    "v_mfma_f32_16x16x32_bf16 v[84:87], a[56:59], v[36:39], v[84:87]\n" \
    "v_mfma_f32_16x16x32_bf16 v[88:91], a[136:139], v[36:39], v[88:91]\n" \
    "v_mfma_f32_16x16x32_bf16 v[92:95], a[216:219], v[36:39], v[92:95]\n" \
    "s_waitcnt lgkmcnt(3)\n" \
    "v_mfma_f32_16x16x32_bf16 v[72:75], a[60:63], v[40:43], v[72:75]\n" \
    "ds_read_b128 v[24:27], v108 offset:17408\n" \
    "v_mfma_f32_16x16x32_bf16 v[76:79], a[140:143], v[40:43], v[76:79]\n" \
    "ds_read_b128 v[28:31], v108 offset:37888\n" \
    "v_mfma_f32_16x16x32_bf16 v[80:83], a[220:223], v[40:43], v[80:83]\n" \
    "s_waitcnt lgkmcnt(4)\n" \
    "v_mfma_f32_16x16x32_bf16 v[84:87], a[60:63], v[44:47], v[84:87]\n" \
    "v_mfma_f32_16x16x32_bf16 v[88:91], a[140:143], v[44:47], v[88:91]\n" \
    "v_mfma_f32_16x16x32_bf16 v[92:95], a[220:223], v[44:47], v[92:95]\n" \
    "s_waitcnt lgkmcnt(3)\n" \
    "v_mfma_f32_16x16x32_bf16 v[72:75], a[64:67], v[16:19], v[72:75]\n" \
    "ds_read_b128 v[32:35], v108 offset:18432\n" \
    "v_mfma_f32_16x16x32_bf16 v[76:79], a[144:147], v[16:19], v[76:79]\n" \
    "ds_read_b128 v[36:39], v108 offset:38912\n" \
    "v_mfma_f32_16x16x32_bf16 v[80:83], a[224:227], v[16:19], v[80:83]\n" \
    "s_waitcnt lgkmcnt(4)\n" \
    "v_mfma_f32_16x16x32_bf16 v[84:87], a[64:67], v[20:23], v[84:87]\n" \
    "v_mfma_f32_16x16x32_bf16 v[88:91], a[144:147], v[20:23], v[88:91]\n" \
    "v_mfma_f32_16x16x32_bf16 v[92:95], a[224:227], v[20:23], v[92:95]\n" \
    "s_waitcnt lgkmcnt(3)\n" \
    "v_mfma_f32_16x16x32_bf16 v[72:75], a[68:71], v[24:27], v[72:75]\n" \
    "ds_read_b128 v[40:43], v108 offset:19456\n" \
    "v_mfma_f32_16x16x32_bf16 v[76:79], a[148:151], v[24:27], v[76:79]\n" \
    "ds_read_b128 v[44:47], v108 offset:39936\n" \
    "v_mfma_f32_16x16x32_bf16 v[80:83], a[228:231], v[24:27], v[80:83]\n" \
    "s_waitcnt lgkmcnt(4)\n" \
    "v_mfma_f32_16x16x32_bf16 v[84:87], a[68:71], v[28:31], v[84:87]\n" \
    "v_mfma_f32_16x16x32_bf16 v[88:91], a[148:151], v[28:31], v[88:91]\n" \
    "v_mfma_f32_16x16x32_bf16 v[92:95], a[228:231], v[28:31], v[92:95]\n" \
    "s_waitcnt lgkmcnt(3)\n" \
    "v_mfma_f32_16x16x32_bf16 v[72:75], a[72:75], v[32:35], v[72:75]\n" \
    "ds_read_b128 v[16:19], v109\n" \
    "v_mfma_f32_16x16x32_bf16 v[76:79], a[152:155], v[32:35], v[76:79]\n" \
    "ds_read_b128 v[20:23], v109 offset:20480\n" \
    "v_mfma_f32_16x16x32_bf16 v[80:83], a[232:235], v[32:35], v[80:83]\n" \
    "s_waitcnt lgkmcnt(4)\n" \
    "v_mfma_f32_16x16x32_bf16 v[84:87], a[72:75], v[36:39], v[84:87]\n" \
    "v_mfma_f32_16x16x32_bf16 v[88:91], a[152:155], v[36:39], v[88:91]\n" \
    "v_mfma_f32_16x16x32_bf16 v[92:95], a[232:235], v[36:39], v[92:95]\n" \
    "s_waitcnt lgkmcnt(3)\n" \
    "v_mfma_f32_16x16x32_bf16 v[72:75], a[76:79], v[40:43], v[72:75]\n" \
    "ds_read_b128 v[24:27], v109 offset:1024\n" \
    "v_mfma_f32_16x16x32_bf16 v[76:79], a[156:159], v[40:43], v[76:79]\n" \
    "ds_read_b128 v[28:31], v109 offset:21504\n" \
    "v_mfma_f32_16x16x32_bf16 v[80:83], a[236:239], v[40:43], v[80:83]\n" \
    "s_waitcnt lgkmcnt(4)\n" \
    "v_mfma_f32_16x16x32_bf16 v[84:87], a[76:79], v[44:47], v[84:87]\n" \
    "v_mfma_f32_16x16x32_bf16 v[88:91], a[156:159], v[44:47], v[88:91]\n" \
    "v_mfma_f32_16x16x32_bf16 v[92:95], a[236:239], v[44:47], v[92:95]\n" \
    "s_add_u32 s60, s60, 1\n" \
    "Lf_loop:\n" \
    "v_mov_b32 v108, v109\n" \
    "s_add_u32 s64, s64, 40960\n" \
    "s_cmp_eq_u32 s64, 122880\n" \
    "s_cselect_b32 s64, 0, s64\n" \
    "v_add_u32 v109, s64, %[l16]\n" \
    "s_add_u32 s65, s64, 40960\n" \
    "s_cmp_eq_u32 s65, 122880\n" \
    "s_cselect_b32 s65, 0, s65\n" \
    "s_add_u32 s65, s65, s70\n" \
    "s_min_u32 s63, s60, 61\n" \
    "s_mul_i32 s63, s63, 40960\n" \
    "s_add_u32 s63, s63, s70\n" \
    "s_add_u32 s63, s63, 250691584\n" \
    "s_waitcnt lgkmcnt(3)\n" \
    "v_mfma_f32_16x16x32_bf16 v[48:51], a[0:3], v[16:19], 0\n" \
    "ds_read_b128 v[32:35], v108 offset:2048\n" \
    "v_mfma_f32_16x16x32_bf16 v[52:55], a[80:83], v[16:19], 0\n" \
    "ds_read_b128 v[36:39], v108 offset:22528\n" \
    "v_mfma_f32_16x16x32_bf16 v[56:59], a[160:163], v[16:19], 0\n" \
    "s_waitcnt lgkmcnt(4)\n" \
    "v_mfma_f32_16x16x32_bf16 v[60:63], a[0:3], v[20:23], 0\n" \
    "v_mfma_f32_16x16x32_bf16 v[64:67], a[80:83], v[20:23], 0\n" \
    "v_cvt_pk_bf16_f32 v73, v72, v73\n" \
    "v_cvt_pk_bf16_f32 v75, v74, v75\n" \
    "v_mfma_f32_16x16x32_bf16 v[68:71], a[160:163], v[20:23], 0\n" \
    "v_cvt_pk_bf16_f32 v85, v84, v85\n" \
    "v_cvt_pk_bf16_f32 v87, v86, v87\n" \
    "s_waitcnt lgkmcnt(3)\n" \
    "v_mfma_f32_16x16x32_bf16 v[48:51], a[4:7], v[24:27], v[48:51]\n" \
    "v_lshlrev_b32 v72, 16, v73\n" \
    "ds_read_b128 v[40:43], v108 offset:3072\n" \
    "v_mfma_f32_16x16x32_bf16 v[52:55], a[84:87], v[24:27], v[52:55]\n" \
    "v_lshlrev_b32 v74, 16, v75\n" \
    "ds_read_b128 v[44:47], v108 offset:23552\n" \
    "v_mfma_f32_16x16x32_bf16 v[56:59], a[164:167], v[24:27], v[56:59]\n" \
    "v_lshlrev_b32 v84, 16, v85\n" \
    "v_lshlrev_b32 v86, 16, v87\n" \
    "s_waitcnt lgkmcnt(4)\n" \
    "v_mfma_f32_16x16x32_bf16 v[60:63], a[4:7], v[28:31], v[60:63]\n" \
    "v_and_b32 v73, 0xffff0000, v73\n" \
    "v_and_b32 v75, 0xffff0000, v75\n" \
    "v_mfma_f32_16x16x32_bf16 v[64:67], a[84:87], v[28:31], v[64:67]\n" \
    "v_and_b32 v85, 0xffff0000, v85\n" \
    "v_and_b32 v87, 0xffff0000, v87\n" \
    "v_mfma_f32_16x16x32_bf16 v[68:71], a[164:167], v[28:31], v[68:71]\n" \
    "v_add_f32 v72, v72, v120\n" \
    "v_add_f32 v74, v74, v122\n" \
    "s_waitcnt lgkmcnt(3)\n" \
    "v_mfma_f32_16x16x32_bf16 v[48:51], a[8:11], v[32:35], v[48:51]\n" \
    "v_add_f32 v84, v84, v120\n" \
    "ds_read_b128 v[16:19], v108 offset:4096\n" \
    "v_mfma_f32_16x16x32_bf16 v[52:55], a[88:91], v[32:35], v[52:55]\n" \
    "v_add_f32 v86, v86, v122\n" \
    "ds_read_b128 v[20:23], v108 offset:24576\n" \
    "v_mfma_f32_16x16x32_bf16 v[56:59], a[168:171], v[32:35], v[56:59]\n" \
    "v_add_f32 v73, v73, v121\n" \
    "v_add_f32 v75, v75, v123\n" \
    "s_waitcnt lgkmcnt(4)\n" \
    "v_mfma_f32_16x16x32_bf16 v[60:63], a[8:11], v[36:39], v[60:63]\n" \
    "v_add_f32 v85, v85, v121\n" \
    "v_add_f32 v87, v87, v123\n" \
    "v_mfma_f32_16x16x32_bf16 v[64:67], a[88:91], v[36:39], v[64:67]\n" \
    "v_cvt_pk_bf16_f32 v73, v72, v73\n" \
    "v_cvt_pk_bf16_f32 v75, v74, v75\n" \
    "v_mfma_f32_16x16x32_bf16 v[68:71], a[168:171], v[36:39], v[68:71]\n" \
    "v_cvt_pk_bf16_f32 v85, v84, v85\n" \
    "v_cvt_pk_bf16_f32 v87, v86, v87\n" \
    "s_waitcnt lgkmcnt(3)\n" \
    "v_mfma_f32_16x16x32_bf16 v[48:51], a[12:15], v[40:43], v[48:51]\n" \
    "v_lshlrev_b32 v72, 16, v73\n" \
    "ds_read_b128 v[24:27], v108 offset:5120\n" \
    "v_mfma_f32_16x16x32_bf16 v[52:55], a[92:95], v[40:43], v[52:55]\n" \
    "v_lshlrev_b32 v74, 16, v75\n" \
    "ds_read_b128 v[28:31], v108 offset:25600\n" \
    "v_mfma_f32_16x16x32_bf16 v[56:59], a[172:175], v[40:43], v[56:59]\n" \
    "v_lshlrev_b32 v84, 16, v85\n" \
    "v_lshlrev_b32 v86, 16, v87\n" \
    "s_waitcnt lgkmcnt(4)\n" \
    "v_mfma_f32_16x16x32_bf16 v[60:63], a[12:15], v[44:47], v[60:63]\n" \
    "v_and_b32 v73, 0xffff0000, v73\n" \
    "v_and_b32 v75, 0xffff0000, v75\n" \
    "v_mfma_f32_16x16x32_bf16 v[64:67], a[92:95], v[44:47], v[64:67]\n" \
    "v_and_b32 v85, 0xffff0000, v85\n" \
    "v_and_b32 v87, 0xffff0000, v87\n" \
    "v_mfma_f32_16x16x32_bf16 v[68:71], a[172:175], v[44:47], v[68:71]\n" \
    "v_max_f32 v72, 0, v72\n" \
    "v_max_f32 v74, 0, v74\n" \
    "s_waitcnt vmcnt(0)\n" \
    "s_barrier\n" \
    "s_sub_u32 s73, s60, 1\n" \
    "s_cmp_lg_u32 %[wave], 0\n" \
    "s_cbranch_scc1 Lf_pubc\n" \
    "v_mov_b32 v110, s73\n" \
    "buffer_store_dword v110, %[one], %[ws], %[flag] offen sc1\n" \
    "Lf_pubc:\n" \
    "s_waitcnt lgkmcnt(3)\n" \
    "v_mfma_f32_16x16x32_bf16 v[48:51], a[16:19], v[16:19], v[48:51]\n" \
    "v_max_f32 v84, 0, v84\n" \
    "ds_read_b128 v[32:35], v108 offset:6144\n" \
    "v_mfma_f32_16x16x32_bf16 v[52:55], a[96:99], v[16:19], v[52:55]\n" \
    "v_max_f32 v86, 0, v86\n" \
    "ds_read_b128 v[36:39], v108 offset:26624\n" \
    "v_mfma_f32_16x16x32_bf16 v[56:59], a[176:179], v[16:19], v[56:59]\n" \
    "v_max_f32 v73, 0, v73\n" \
    "s_add_u32 m0, s65, 0\n" \
    "s_nop 0\n" \
    "buffer_load_dwordx4 %[l16], %[ws], s63 offen lds\n" \
    "s_waitcnt lgkmcnt(4)\n" \
    "v_mfma_f32_16x16x32_bf16 v[60:63], a[16:19], v[20:23], v[60:63]\n" \
    "v_max_f32 v75, 0, v75\n" \
    "v_max_f32 v85, 0, v85\n" \
    "v_mfma_f32_16x16x32_bf16 v[64:67], a[96:99], v[20:23], v[64:67]\n" \
    "v_max_f32 v87, 0, v87\n" \
    "buffer_load_dwordx4 %[l16], %[ws], s63 offen offset:1024 lds\n" \
    "v_mfma_f32_16x16x32_bf16 v[68:71], a[176:179], v[20:23], v[68:71]\n" \
    "v_mul_f32 v72, v72, v72\n" \
    "v_mul_f32 v74, v74, v74\n" \
    "s_waitcnt lgkmcnt(3)\n" \
    "v_mfma_f32_16x16x32_bf16 v[48:51], a[20:23], v[24:27], v[48:51]\n" \
    "v_mul_f32 v84, v84, v84\n" \
    "ds_read_b128 v[40:43], v108 offset:7168\n" \
    "v_mfma_f32_16x16x32_bf16 v[52:55], a[100:103], v[24:27], v[52:55]\n" \
    "v_mul_f32 v86, v86, v86\n" \
    "ds_read_b128 v[44:47], v108 offset:27648\n" \
    "v_mfma_f32_16x16x32_bf16 v[56:59], a[180:183], v[24:27], v[56:59]\n" \
    "v_mul_f32 v73, v73, v73\n" \
    "buffer_load_dwordx4 %[l16], %[ws], s63 offen offset:2048 lds\n" \
    "s_waitcnt lgkmcnt(4)\n" \
    "v_mfma_f32_16x16x32_bf16 v[60:63], a[20:23], v[28:31], v[60:63]\n" \
    "v_mul_f32 v75, v75, v75\n" \
    "v_mul_f32 v85, v85, v85\n" \
    "v_mfma_f32_16x16x32_bf16 v[64:67], a[100:103], v[28:31], v[64:67]\n" \
    "v_mul_f32 v87, v87, v87\n" \
    "buffer_load_dwordx4 %[l16], %[ws], s63 offen offset:3072 lds\n" \
    "v_mfma_f32_16x16x32_bf16 v[68:71], a[180:183], v[28:31], v[68:71]\n" \
    "v_cvt_pk_bf16_f32 v96, v72, v73\n" \
    "v_cvt_pk_bf16_f32 v97, v74, v75\n" \
    "s_waitcnt lgkmcnt(3)\n" \
    "v_mfma_f32_16x16x32_bf16 v[48:51], a[24:27], v[32:35], v[48:51]\n" \
    "v_cvt_pk_bf16_f32 v98, v84, v85\n" \
    "ds_read_b128 v[16:19], v108 offset:8192\n" \
    "v_mfma_f32_16x16x32_bf16 v[52:55], a[104:107], v[32:35], v[52:55]\n" \
    "v_cvt_pk_bf16_f32 v99, v86, v87\n" \
    "ds_read_b128 v[20:23], v108 offset:28672\n" \
    "v_mfma_f32_16x16x32_bf16 v[56:59], a[184:187], v[32:35], v[56:59]\n" \
    "buffer_store_dwordx4 v[96:99], %[l16], %[ws], s62 offen sc1\n" \
    "s_add_u32 m0, s65, 4096\n" \
    "s_add_u32 s63, s63, 4096\n" \
    "buffer_load_dwordx4 %[l16], %[ws], s63 offen lds\n" \
    "s_waitcnt lgkmcnt(4)\n" \
    "v_mfma_f32_16x16x32_bf16 v[60:63], a[24:27], v[36:39], v[60:63]\n" \
    "v_cvt_pk_bf16_f32 v77, v76, v77\n" \
    "v_cvt_pk_bf16_f32 v79, v78, v79\n" \
    "v_mfma_f32_16x16x32_bf16 v[64:67], a[104:107], v[36:39], v[64:67]\n" \
    "v_cvt_pk_bf16_f32 v89, v88, v89\n" \
    "buffer_load_dwordx4 %[l16], %[ws], s63 offen offset:1024 lds\n" \
    "v_mfma_f32_16x16x32_bf16 v[68:71], a[184:187], v[36:39], v[68:71]\n" \
    "v_cvt_pk_bf16_f32 v91, v90, v91\n" \
    "v_lshlrev_b32 v76, 16, v77\n" \
    "s_waitcnt lgkmcnt(3)\n" \
    "v_mfma_f32_16x16x32_bf16 v[48:51], a[28:31], v[40:43], v[48:51]\n" \
    "v_lshlrev_b32 v78, 16, v79\n" \
    "ds_read_b128 v[24:27], v108 offset:9216\n" \
    "v_mfma_f32_16x16x32_bf16 v[52:55], a[108:111], v[40:43], v[52:55]\n" \
    "v_lshlrev_b32 v88, 16, v89\n" \
    "ds_read_b128 v[28:31], v108 offset:29696\n" \
    "v_mfma_f32_16x16x32_bf16 v[56:59], a[188:191], v[40:43], v[56:59]\n" \
    "v_lshlrev_b32 v90, 16, v91\n" \
    "buffer_load_dwordx4 %[l16], %[ws], s63 offen offset:2048 lds\n" \
    "s_waitcnt lgkmcnt(4)\n" \
    "v_mfma_f32_16x16x32_bf16 v[60:63], a[28:31], v[44:47], v[60:63]\n" \
    "v_and_b32 v77, 0xffff0000, v77\n" \
    "v_and_b32 v79, 0xffff0000, v79\n" \
    "v_mfma_f32_16x16x32_bf16 v[64:67], a[108:111], v[44:47], v[64:67]\n" \
    "v_and_b32 v89, 0xffff0000, v89\n" \
    "buffer_load_dwordx4 %[l16], %[ws], s63 offen offset:3072 lds\n" \
    "v_mfma_f32_16x16x32_bf16 v[68:71], a[188:191], v[44:47], v[68:71]\n" \
    "v_and_b32 v91, 0xffff0000, v91\n" \
    "v_add_f32 v76, v76, v124\n" \
    "s_waitcnt lgkmcnt(3)\n" \
    "v_mfma_f32_16x16x32_bf16 v[48:51], a[32:35], v[16:19], v[48:51]\n" \
    "v_add_f32 v78, v78, v126\n" \
    "ds_read_b128 v[32:35], v108 offset:10240\n" \
    "v_mfma_f32_16x16x32_bf16 v[52:55], a[112:115], v[16:19], v[52:55]\n" \
    "v_add_f32 v88, v88, v124\n" \
    "ds_read_b128 v[36:39], v108 offset:30720\n" \
    "v_mfma_f32_16x16x32_bf16 v[56:59], a[192:195], v[16:19], v[56:59]\n" \
    "v_add_f32 v90, v90, v126\n" \
    "s_add_u32 m0, s65, 8192\n" \
    "s_add_u32 s63, s63, 4096\n" \
    "buffer_load_dwordx4 %[l16], %[ws], s63 offen lds\n" \
    "s_waitcnt lgkmcnt(4)\n" \
    "v_mfma_f32_16x16x32_bf16 v[60:63], a[32:35], v[20:23], v[60:63]\n" \
    "v_add_f32 v77, v77, v125\n" \
    "v_add_f32 v79, v79, v127\n" \
    "v_mfma_f32_16x16x32_bf16 v[64:67], a[112:115], v[20:23], v[64:67]\n" \
    "v_add_f32 v89, v89, v125\n" \
    "buffer_load_dwordx4 %[l16], %[ws], s63 offen offset:1024 lds\n" \
    "v_mfma_f32_16x16x32_bf16 v[68:71], a[192:195], v[20:23], v[68:71]\n" \
    "v_add_f32 v91, v91, v127\n" \
    "v_cvt_pk_bf16_f32 v77, v76, v77\n" \
    "s_waitcnt lgkmcnt(3)\n" \
    "v_mfma_f32_16x16x32_bf16 v[48:51], a[36:39], v[24:27], v[48:51]\n" \
    "v_cvt_pk_bf16_f32 v79, v78, v79\n" \
    "ds_read_b128 v[40:43], v108 offset:11264\n" \
    "v_mfma_f32_16x16x32_bf16 v[52:55], a[116:119], v[24:27], v[52:55]\n" \
    "v_cvt_pk_bf16_f32 v89, v88, v89\n" \
    "ds_read_b128 v[44:47], v108 offset:31744\n" \
    "v_mfma_f32_16x16x32_bf16 v[56:59], a[196:199], v[24:27], v[56:59]\n" \
    "v_cvt_pk_bf16_f32 v91, v90, v91\n" \
    "v_lshlrev_b32 v76, 16, v77\n" \
    "s_waitcnt lgkmcnt(4)\n" \
    "v_mfma_f32_16x16x32_bf16 v[60:63], a[36:39], v[28:31], v[60:63]\n" \
    "v_lshlrev_b32 v78, 16, v79\n" \
    "v_lshlrev_b32 v88, 16, v89\n" \
    "v_mfma_f32_16x16x32_bf16 v[64:67], a[116:119], v[28:31], v[64:67]\n" \
    "v_lshlrev_b32 v90, 16, v91\n" \
    "v_and_b32 v77, 0xffff0000, v77\n" \
    "v_mfma_f32_16x16x32_bf16 v[68:71], a[196:199], v[28:31], v[68:71]\n" \
    "v_and_b32 v79, 0xffff0000, v79\n" \
    "v_and_b32 v89, 0xffff0000, v89\n" \
    "s_waitcnt lgkmcnt(3)\n" \
    "v_mfma_f32_16x16x32_bf16 v[48:51], a[40:43], v[32:35], v[48:51]\n" \
    "v_and_b32 v91, 0xffff0000, v91\n" \
    "ds_read_b128 v[16:19], v108 offset:12288\n" \
    "v_mfma_f32_16x16x32_bf16 v[52:55], a[120:123], v[32:35], v[52:55]\n" \
    "v_max_f32 v76, 0, v76\n" \
    "ds_read_b128 v[20:23], v108 offset:32768\n" \
    "v_mfma_f32_16x16x32_bf16 v[56:59], a[200:203], v[32:35], v[56:59]\n" \
    "v_max_f32 v78, 0, v78\n" \
    "v_max_f32 v88, 0, v88\n" \
    "s_waitcnt lgkmcnt(4)\n" \
    "v_mfma_f32_16x16x32_bf16 v[60:63], a[40:43], v[36:39], v[60:63]\n" \
    "v_max_f32 v90, 0, v90\n" \
    "v_max_f32 v77, 0, v77\n" \
    "v_mfma_f32_16x16x32_bf16 v[64:67], a[120:123], v[36:39], v[64:67]\n" \
    "v_max_f32 v79, 0, v79\n" \
    "v_max_f32 v89, 0, v89\n" \
    "v_mfma_f32_16x16x32_bf16 v[68:71], a[200:203], v[36:39], v[68:71]\n" \
    "v_max_f32 v91, 0, v91\n" \
    "v_mul_f32 v76, v76, v76\n" \
    "s_waitcnt lgkmcnt(3)\n" \
    "v_mfma_f32_16x16x32_bf16 v[48:51], a[44:47], v[40:43], v[48:51]\n" \
    "v_mul_f32 v78, v78, v78\n" \
    "ds_read_b128 v[24:27], v108 offset:13312\n" \
    "v_mfma_f32_16x16x32_bf16 v[52:55], a[124:127], v[40:43], v[52:55]\n" \
    "v_mul_f32 v88, v88, v88\n" \
    "ds_read_b128 v[28:31], v108 offset:33792\n" \
    "v_mfma_f32_16x16x32_bf16 v[56:59], a[204:207], v[40:43], v[56:59]\n" \
    "v_mul_f32 v90, v90, v90\n" \
    "v_mul_f32 v77, v77, v77\n" \
    "s_waitcnt lgkmcnt(4)\n" \
    "v_mfma_f32_16x16x32_bf16 v[60:63], a[44:47], v[44:47], v[60:63]\n" \
    "v_mul_f32 v79, v79, v79\n" \
    "v_mul_f32 v89, v89, v89\n" \
    "v_mfma_f32_16x16x32_bf16 v[64:67], a[124:127], v[44:47], v[64:67]\n" \
    "v_mul_f32 v91, v91, v91\n" \
    "v_cvt_pk_bf16_f32 v100, v76, v77\n" \
    "v_mfma_f32_16x16x32_bf16 v[68:71], a[204:207], v[44:47], v[68:71]\n" \
    "v_cvt_pk_bf16_f32 v101, v78, v79\n" \
    "v_cvt_pk_bf16_f32 v102, v88, v89\n" \
    "s_waitcnt lgkmcnt(3)\n" \
    "v_mfma_f32_16x16x32_bf16 v[48:51], a[48:51], v[16:19], v[48:51]\n" \
    "v_cvt_pk_bf16_f32 v103, v90, v91\n" \
    "ds_read_b128 v[32:35], v108 offset:14336\n" \
    "v_mfma_f32_16x16x32_bf16 v[52:55], a[128:131], v[16:19], v[52:55]\n" \
    "buffer_store_dwordx4 v[100:103], %[l16], %[ws], s62 offen offset:1024 sc1\n" \
    "ds_read_b128 v[36:39], v108 offset:34816\n" \
    "v_mfma_f32_16x16x32_bf16 v[56:59], a[208:211], v[16:19], v[56:59]\n" \
    "v_cvt_pk_bf16_f32 v81, v80, v81\n" \
    "v_cvt_pk_bf16_f32 v83, v82, v83\n" \
    "s_waitcnt lgkmcnt(4)\n" \
    "v_mfma_f32_16x16x32_bf16 v[60:63], a[48:51], v[20:23], v[60:63]\n" \
    "v_cvt_pk_bf16_f32 v93, v92, v93\n" \
    "v_cvt_pk_bf16_f32 v95, v94, v95\n" \
    "v_mfma_f32_16x16x32_bf16 v[64:67], a[128:131], v[20:23], v[64:67]\n" \
    "v_lshlrev_b32 v80, 16, v81\n" \
    "v_lshlrev_b32 v82, 16, v83\n" \
    "v_mfma_f32_16x16x32_bf16 v[68:71], a[208:211], v[20:23], v[68:71]\n" \
    "v_lshlrev_b32 v92, 16, v93\n" \
    "v_lshlrev_b32 v94, 16, v95\n" \
    "s_waitcnt lgkmcnt(3)\n" \
    "v_mfma_f32_16x16x32_bf16 v[48:51], a[52:55], v[24:27], v[48:51]\n" \
    "v_and_b32 v81, 0xffff0000, v81\n" \
    "ds_read_b128 v[40:43], v108 offset:15360\n" \
    "v_mfma_f32_16x16x32_bf16 v[52:55], a[132:135], v[24:27], v[52:55]\n" \
    "v_and_b32 v83, 0xffff0000, v83\n" \
    "ds_read_b128 v[44:47], v108 offset:35840\n" \
    "v_mfma_f32_16x16x32_bf16 v[56:59], a[212:215], v[24:27], v[56:59]\n" \
    "v_and_b32 v93, 0xffff0000, v93\n" \
    "v_and_b32 v95, 0xffff0000, v95\n" \
    "s_waitcnt lgkmcnt(4)\n" \
    "v_mfma_f32_16x16x32_bf16 v[60:63], a[52:55], v[28:31], v[60:63]\n" \
    "v_add_f32 v80, v80, v128\n" \
    "v_add_f32 v82, v82, v130\n" \
    "v_mfma_f32_16x16x32_bf16 v[64:67], a[132:135], v[28:31], v[64:67]\n" \
    "v_add_f32 v92, v92, v128\n" \
    "v_add_f32 v94, v94, v130\n" \
    "v_mfma_f32_16x16x32_bf16 v[68:71], a[212:215], v[28:31], v[68:71]\n" \
    "v_add_f32 v81, v81, v129\n" \
    "v_add_f32 v83, v83, v131\n" \
    "s_waitcnt lgkmcnt(3)\n" \
    "v_mfma_f32_16x16x32_bf16 v[48:51], a[56:59], v[32:35], v[48:51]\n" \
    "v_add_f32 v93, v93, v129\n" \
    "ds_read_b128 v[16:19], v108 offset:16384\n" \
    "v_mfma_f32_16x16x32_bf16 v[52:55], a[136:139], v[32:35], v[52:55]\n" \
    "v_add_f32 v95, v95, v131\n" \
    "ds_read_b128 v[20:23], v108 offset:36864\n" \
    "v_mfma_f32_16x16x32_bf16 v[56:59], a[216:219], v[32:35], v[56:59]\n" \
    "v_cvt_pk_bf16_f32 v81, v80, v81\n" \
    "v_cvt_pk_bf16_f32 v83, v82, v83\n" \
    "s_waitcnt lgkmcnt(4)\n" \
    "v_mfma_f32_16x16x32_bf16 v[60:63], a[56:59], v[36:39], v[60:63]\n" \
    "v_cvt_pk_bf16_f32 v93, v92, v93\n" \
    "v_cvt_pk_bf16_f32 v95, v94, v95\n" \
    "v_mfma_f32_16x16x32_bf16 v[64:67], a[136:139], v[36:39], v[64:67]\n" \
    "v_lshlrev_b32 v80, 16, v81\n" \
    "v_lshlrev_b32 v82, 16, v83\n" \
    "v_mfma_f32_16x16x32_bf16 v[68:71], a[216:219], v[36:39], v[68:71]\n" \
    "v_lshlrev_b32 v92, 16, v93\n" \
    "v_lshlrev_b32 v94, 16, v95\n" \
    "s_waitcnt lgkmcnt(3)\n" \
    "v_mfma_f32_16x16x32_bf16 v[48:51], a[60:63], v[40:43], v[48:51]\n" \
    "v_and_b32 v81, 0xffff0000, v81\n" \
    "ds_read_b128 v[24:27], v108 offset:17408\n" \
    "v_mfma_f32_16x16x32_bf16 v[52:55], a[140:143], v[40:43], v[52:55]\n" \
    "v_and_b32 v83, 0xffff0000, v83\n" \
    "ds_read_b128 v[28:31], v108 offset:37888\n" \
    "v_mfma_f32_16x16x32_bf16 v[56:59], a[220:223], v[40:43], v[56:59]\n" \
    "v_and_b32 v93, 0xffff0000, v93\n" \
    "v_and_b32 v95, 0xffff0000, v95\n" \
    "s_waitcnt lgkmcnt(4)\n" \
    "v_mfma_f32_16x16x32_bf16 v[60:63], a[60:63], v[44:47], v[60:63]\n" \
    "v_max_f32 v80, 0, v80\n" \
    "v_max_f32 v82, 0, v82\n" \
    "v_mfma_f32_16x16x32_bf16 v[64:67], a[140:143], v[44:47], v[64:67]\n" \
    "v_max_f32 v92, 0, v92\n" \
    "v_max_f32 v94, 0, v94\n" \
    "v_mfma_f32_16x16x32_bf16 v[68:71], a[220:223], v[44:47], v[68:71]\n" \
    "v_max_f32 v81, 0, v81\n" \
    "v_max_f32 v83, 0, v83\n" \
    "s_waitcnt lgkmcnt(3)\n" \
    "v_mfma_f32_16x16x32_bf16 v[48:51], a[64:67], v[16:19], v[48:51]\n" \
    "v_max_f32 v93, 0, v93\n" \
    "ds_read_b128 v[32:35], v108 offset:18432\n" \
    "v_mfma_f32_16x16x32_bf16 v[52:55], a[144:147], v[16:19], v[52:55]\n" \
    "v_max_f32 v95, 0, v95\n" \
    "ds_read_b128 v[36:39], v108 offset:38912\n" \
    "v_mfma_f32_16x16x32_bf16 v[56:59], a[224:227], v[16:19], v[56:59]\n" \
    "v_mul_f32 v80, v80, v80\n" \
    "v_mul_f32 v82, v82, v82\n" \
    "s_waitcnt lgkmcnt(4)\n" \
    "v_mfma_f32_16x16x32_bf16 v[60:63], a[64:67], v[20:23], v[60:63]\n" \
    "v_mul_f32 v92, v92, v92\n" \
    "v_mul_f32 v94, v94, v94\n" \
    "v_mfma_f32_16x16x32_bf16 v[64:67], a[144:147], v[20:23], v[64:67]\n" \
    "v_mul_f32 v81, v81, v81\n" \
    "v_mul_f32 v83, v83, v83\n" \
    "v_mfma_f32_16x16x32_bf16 v[68:71], a[224:227], v[20:23], v[68:71]\n" \
    "v_mul_f32 v93, v93, v93\n" \
    "v_mul_f32 v95, v95, v95\n" \
    "s_waitcnt lgkmcnt(3)\n" \
    "v_mfma_f32_16x16x32_bf16 v[48:51], a[68:71], v[24:27], v[48:51]\n" \
    "v_cvt_pk_bf16_f32 v104, v80, v81\n" \
    "ds_read_b128 v[40:43], v108 offset:19456\n" \
    "v_mfma_f32_16x16x32_bf16 v[52:55], a[148:151], v[24:27], v[52:55]\n" \
    "v_cvt_pk_bf16_f32 v105, v82, v83\n" \
    "ds_read_b128 v[44:47], v108 offset:39936\n" \
    "v_mfma_f32_16x16x32_bf16 v[56:59], a[228:231], v[24:27], v[56:59]\n" \
    "v_cvt_pk_bf16_f32 v106, v92, v93\n" \
    "v_cvt_pk_bf16_f32 v107, v94, v95\n" \
    "s_waitcnt lgkmcnt(4)\n" \
    "v_mfma_f32_16x16x32_bf16 v[60:63], a[68:71], v[28:31], v[60:63]\n" \
    "buffer_store_dwordx4 v[104:107], %[l16], %[ws], s62 offen offset:2048 sc1\n" \
    "v_mfma_f32_16x16x32_bf16 v[64:67], a[148:151], v[28:31], v[64:67]\n" \
    "v_mfma_f32_16x16x32_bf16 v[68:71], a[228:231], v[28:31], v[68:71]\n" \
    "s_waitcnt lgkmcnt(3)\n" \
    "v_mfma_f32_16x16x32_bf16 v[48:51], a[72:75], v[32:35], v[48:51]\n" \
    "ds_read_b128 v[16:19], v109\n" \
    "v_mfma_f32_16x16x32_bf16 v[52:55], a[152:155], v[32:35], v[52:55]\n" \
    "ds_read_b128 v[20:23], v109 offset:20480\n" \
    "v_mfma_f32_16x16x32_bf16 v[56:59], a[232:235], v[32:35], v[56:59]\n" \
    "s_waitcnt lgkmcnt(4)\n" \
    "v_mfma_f32_16x16x32_bf16 v[60:63], a[72:75], v[36:39], v[60:63]\n" \
    "v_mfma_f32_16x16x32_bf16 v[64:67], a[152:155], v[36:39], v[64:67]\n" \
    "v_mfma_f32_16x16x32_bf16 v[68:71], a[232:235], v[36:39], v[68:71]\n" \
    "s_waitcnt lgkmcnt(3)\n" \
    "v_mfma_f32_16x16x32_bf16 v[48:51], a[76:79], v[40:43], v[48:51]\n" \
    "ds_read_b128 v[24:27], v109 offset:1024\n" \
    "v_mfma_f32_16x16x32_bf16 v[52:55], a[156:159], v[40:43], v[52:55]\n" \
    "ds_read_b128 v[28:31], v109 offset:21504\n" \
    "v_mfma_f32_16x16x32_bf16 v[56:59], a[236:239], v[40:43], v[56:59]\n" \
    "s_waitcnt lgkmcnt(4)\n" \
    "v_mfma_f32_16x16x32_bf16 v[60:63], a[76:79], v[44:47], v[60:63]\n" \
    "v_mfma_f32_16x16x32_bf16 v[64:67], a[156:159], v[44:47], v[64:67]\n" \
    "v_mfma_f32_16x16x32_bf16 v[68:71], a[236:239], v[44:47], v[68:71]\n" \
    "s_add_u32 s62, s62, 98304\n" \
    "s_add_u32 s60, s60, 1\n" \
    "v_mov_b32 v108, v109\n" \
    "s_add_u32 s64, s64, 40960\n" \
    "s_cmp_eq_u32 s64, 122880\n" \
    "s_cselect_b32 s64, 0, s64\n" \
    "v_add_u32 v109, s64, %[l16]\n" \
    "s_add_u32 s65, s64, 40960\n" \
    "s_cmp_eq_u32 s65, 122880\n" \
    "s_cselect_b32 s65, 0, s65\n" \
    "s_add_u32 s65, s65, s70\n" \
    "s_min_u32 s63, s60, 61\n" \
    "s_mul_i32 s63, s63, 40960\n" \
    "s_add_u32 s63, s63, s70\n" \
    "s_add_u32 s63, s63, 250691584\n" \
    "s_waitcnt lgkmcnt(3)\n" \
    "v_mfma_f32_16x16x32_bf16 v[72:75], a[0:3], v[16:19], 0\n" \
    "ds_read_b128 v[32:35], v108 offset:2048\n" \
    "v_mfma_f32_16x16x32_bf16 v[76:79], a[80:83], v[16:19], 0\n" \
    "ds_read_b128 v[36:39], v108 offset:22528\n" \
    "v_mfma_f32_16x16x32_bf16 v[80:83], a[160:163], v[16:19], 0\n" \
    "s_waitcnt lgkmcnt(4)\n" \
    "v_mfma_f32_16x16x32_bf16 v[84:87], a[0:3], v[20:23], 0\n" \
    "v_mfma_f32_16x16x32_bf16 v[88:91], a[80:83], v[20:23], 0\n" \
    "v_cvt_pk_bf16_f32 v49, v48, v49\n" \
    "v_cvt_pk_bf16_f32 v51, v50, v51\n" \
    "v_mfma_f32_16x16x32_bf16 v[92:95], a[160:163], v[20:23], 0\n" \
    "v_cvt_pk_bf16_f32 v61, v60, v61\n" \
    "v_cvt_pk_bf16_f32 v63, v62, v63\n" \
    "s_waitcnt lgkmcnt(3)\n" \
    "v_mfma_f32_16x16x32_bf16 v[72:75], a[4:7], v[24:27], v[72:75]\n" \
    "v_lshlrev_b32 v48, 16, v49\n" \
    "ds_read_b128 v[40:43], v108 offset:3072\n" \
    "v_mfma_f32_16x16x32_bf16 v[76:79], a[84:87], v[24:27], v[76:79]\n" \
    "v_lshlrev_b32 v50, 16, v51\n" \
    "ds_read_b128 v[44:47], v108 offset:23552\n" \
    "v_mfma_f32_16x16x32_bf16 v[80:83], a[164:167], v[24:27], v[80:83]\n" \
    "v_lshlrev_b32 v60, 16, v61\n" \
    "v_lshlrev_b32 v62, 16, v63\n" \
    "s_waitcnt lgkmcnt(4)\n" \
    "v_mfma_f32_16x16x32_bf16 v[84:87], a[4:7], v[28:31], v[84:87]\n" \
    "v_and_b32 v49, 0xffff0000, v49\n" \
    "v_and_b32 v51, 0xffff0000, v51\n" \
    "v_mfma_f32_16x16x32_bf16 v[88:91], a[84:87], v[28:31], v[88:91]\n" \
    "v_and_b32 v61, 0xffff0000, v61\n" \
    "v_and_b32 v63, 0xffff0000, v63\n" \
    "v_mfma_f32_16x16x32_bf16 v[92:95], a[164:167], v[28:31], v[92:95]\n" \
    "v_add_f32 v48, v48, v120\n" \
    "v_add_f32 v50, v50, v122\n" \
    "s_waitcnt lgkmcnt(3)\n" \
    "v_mfma_f32_16x16x32_bf16 v[72:75], a[8:11], v[32:35], v[72:75]\n" \
    "v_add_f32 v60, v60, v120\n" \
    "ds_read_b128 v[16:19], v108 offset:4096\n" \
    "v_mfma_f32_16x16x32_bf16 v[76:79], a[88:91], v[32:35], v[76:79]\n" \
    "v_add_f32 v62, v62, v122\n" \
    "ds_read_b128 v[20:23], v108 offset:24576\n" \
    "v_mfma_f32_16x16x32_bf16 v[80:83], a[168:171], v[32:35], v[80:83]\n" \
    "v_add_f32 v49, v49, v121\n" \
    "v_add_f32 v51, v51, v123\n" \
    "s_waitcnt lgkmcnt(4)\n" \
    "v_mfma_f32_16x16x32_bf16 v[84:87], a[8:11], v[36:39], v[84:87]\n" \
    "v_add_f32 v61, v61, v121\n" \
    "v_add_f32 v63, v63, v123\n" \
    "v_mfma_f32_16x16x32_bf16 v[88:91], a[88:91], v[36:39], v[88:91]\n" \
    "v_cvt_pk_bf16_f32 v49, v48, v49\n" \
    "v_cvt_pk_bf16_f32 v51, v50, v51\n" \
    "v_mfma_f32_16x16x32_bf16 v[92:95], a[168:171], v[36:39], v[92:95]\n" \
    "v_cvt_pk_bf16_f32 v61, v60, v61\n" \
    "v_cvt_pk_bf16_f32 v63, v62, v63\n" \
    "s_waitcnt lgkmcnt(3)\n" \
    "v_mfma_f32_16x16x32_bf16 v[72:75], a[12:15], v[40:43], v[72:75]\n" \
    "v_lshlrev_b32 v48, 16, v49\n" \
    "ds_read_b128 v[24:27], v108 offset:5120\n" \
    "v_mfma_f32_16x16x32_bf16 v[76:79], a[92:95], v[40:43], v[76:79]\n" \
    "v_lshlrev_b32 v50, 16, v51\n" \
    "ds_read_b128 v[28:31], v108 offset:25600\n" \
    "v_mfma_f32_16x16x32_bf16 v[80:83], a[172:175], v[40:43], v[80:83]\n" \
    "v_lshlrev_b32 v60, 16, v61\n" \
    "v_lshlrev_b32 v62, 16, v63\n" \
    "s_waitcnt lgkmcnt(4)\n" \
    "v_mfma_f32_16x16x32_bf16 v[84:87], a[12:15], v[44:47], v[84:87]\n" \
    "v_and_b32 v49, 0xffff0000, v49\n" \
    "v_and_b32 v51, 0xffff0000, v51\n" \
    "v_mfma_f32_16x16x32_bf16 v[88:91], a[92:95], v[44:47], v[88:91]\n" \
    "v_and_b32 v61, 0xffff0000, v61\n" \
    "v_and_b32 v63, 0xffff0000, v63\n" \
    "v_mfma_f32_16x16x32_bf16 v[92:95], a[172:175], v[44:47], v[92:95]\n" \
    "v_max_f32 v48, 0, v48\n" \
    "v_max_f32 v50, 0, v50\n" \
    "s_waitcnt vmcnt(0)\n" \
    "s_barrier\n" \
    "s_sub_u32 s73, s60, 1\n" \
    "s_cmp_lg_u32 %[wave], 0\n" \
    "s_cbranch_scc1 Lf_pubd\n" \
    "v_mov_b32 v110, s73\n" \
    "buffer_store_dword v110, %[one], %[ws], %[flag] offen sc1\n" \
    "Lf_pubd:\n" \
    "s_waitcnt lgkmcnt(3)\n" \
    "v_mfma_f32_16x16x32_bf16 v[72:75], a[16:19], v[16:19], v[72:75]\n" \
    "v_max_f32 v60, 0, v60\n" \
    "ds_read_b128 v[32:35], v108 offset:6144\n" \
    "v_mfma_f32_16x16x32_bf16 v[76:79], a[96:99], v[16:19], v[76:79]\n" \
    "v_max_f32 v62, 0, v62\n" \
    "ds_read_b128 v[36:39], v108 offset:26624\n" \
    "v_mfma_f32_16x16x32_bf16 v[80:83], a[176:179], v[16:19], v[80:83]\n" \
    "v_max_f32 v49, 0, v49\n" \
    "s_add_u32 m0, s65, 0\n" \
    "s_nop 0\n" \
    "buffer_load_dwordx4 %[l16], %[ws], s63 offen lds\n" \
    "s_waitcnt lgkmcnt(4)\n" \
    "v_mfma_f32_16x16x32_bf16 v[84:87], a[16:19], v[20:23], v[84:87]\n" \
    "v_max_f32 v51, 0, v51\n" \
    "v_max_f32 v61, 0, v61\n" \
    "v_mfma_f32_16x16x32_bf16 v[88:91], a[96:99], v[20:23], v[88:91]\n" \
    "v_max_f32 v63, 0, v63\n" \
    "buffer_load_dwordx4 %[l16], %[ws], s63 offen offset:1024 lds\n" \
    "v_mfma_f32_16x16x32_bf16 v[92:95], a[176:179], v[20:23], v[92:95]\n" \
    "v_mul_f32 v48, v48, v48\n" \
    "v_mul_f32 v50, v50, v50\n" \
    "s_waitcnt lgkmcnt(3)\n" \
    "v_mfma_f32_16x16x32_bf16 v[72:75], a[20:23], v[24:27], v[72:75]\n" \
    "v_mul_f32 v60, v60, v60\n" \
    "ds_read_b128 v[40:43], v108 offset:7168\n" \
    "v_mfma_f32_16x16x32_bf16 v[76:79], a[100:103], v[24:27], v[76:79]\n" \
    "v_mul_f32 v62, v62, v62\n" \
    "ds_read_b128 v[44:47], v108 offset:27648\n" \
    "v_mfma_f32_16x16x32_bf16 v[80:83], a[180:183], v[24:27], v[80:83]\n" \
    "v_mul_f32 v49, v49, v49\n" \
    "buffer_load_dwordx4 %[l16], %[ws], s63 offen offset:2048 lds\n" \
    "s_waitcnt lgkmcnt(4)\n" \
    "v_mfma_f32_16x16x32_bf16 v[84:87], a[20:23], v[28:31], v[84:87]\n" \
    "v_mul_f32 v51, v51, v51\n" \
    "v_mul_f32 v61, v61, v61\n" \
    "v_mfma_f32_16x16x32_bf16 v[88:91], a[100:103], v[28:31], v[88:91]\n" \
    "v_mul_f32 v63, v63, v63\n" \
    "buffer_load_dwordx4 %[l16], %[ws], s63 offen offset:3072 lds\n" \
    "v_mfma_f32_16x16x32_bf16 v[92:95], a[180:183], v[28:31], v[92:95]\n" \
    "v_cvt_pk_bf16_f32 v96, v48, v49\n" \
    "v_cvt_pk_bf16_f32 v97, v50, v51\n" \
    "s_waitcnt lgkmcnt(3)\n" \
    "v_mfma_f32_16x16x32_bf16 v[72:75], a[24:27], v[32:35], v[72:75]\n" \
    "v_cvt_pk_bf16_f32 v98, v60, v61\n" \
    "ds_read_b128 v[16:19], v108 offset:8192\n" \
    "v_mfma_f32_16x16x32_bf16 v[76:79], a[104:107], v[32:35], v[76:79]\n" \
    "v_cvt_pk_bf16_f32 v99, v62, v63\n" \
    "ds_read_b128 v[20:23], v108 offset:28672\n" \
    "v_mfma_f32_16x16x32_bf16 v[80:83], a[184:187], v[32:35], v[80:83]\n" \
    "buffer_store_dwordx4 v[96:99], %[l16], %[ws], s62 offen sc1\n" \
    "s_add_u32 m0, s65, 4096\n" \
    "s_add_u32 s63, s63, 4096\n" \
    "buffer_load_dwordx4 %[l16], %[ws], s63 offen lds\n" \
    "s_waitcnt lgkmcnt(4)\n" \
    "v_mfma_f32_16x16x32_bf16 v[84:87], a[24:27], v[36:39], v[84:87]\n" \
    "v_cvt_pk_bf16_f32 v53, v52, v53\n" \
    "v_cvt_pk_bf16_f32 v55, v54, v55\n" \
    "v_mfma_f32_16x16x32_bf16 v[88:91], a[104:107], v[36:39], v[88:91]\n" \
    "v_cvt_pk_bf16_f32 v65, v64, v65\n" \
    "buffer_load_dwordx4 %[l16], %[ws], s63 offen offset:1024 lds\n" \
    "v_mfma_f32_16x16x32_bf16 v[92:95], a[184:187], v[36:39], v[92:95]\n" \
    "v_cvt_pk_bf16_f32 v67, v66, v67\n" \
    "v_lshlrev_b32 v52, 16, v53\n" \
    "s_waitcnt lgkmcnt(3)\n" \
    "v_mfma_f32_16x16x32_bf16 v[72:75], a[28:31], v[40:43], v[72:75]\n" \
    "v_lshlrev_b32 v54, 16, v55\n" \
    "ds_read_b128 v[24:27], v108 offset:9216\n" \
    "v_mfma_f32_16x16x32_bf16 v[76:79], a[108:111], v[40:43], v[76:79]\n" \
    "v_lshlrev_b32 v64, 16, v65\n" \
    "ds_read_b128 v[28:31], v108 offset:29696\n" \
    "v_mfma_f32_16x16x32_bf16 v[80:83], a[188:191], v[40:43], v[80:83]\n" \
    "v_lshlrev_b32 v66, 16, v67\n" \
    "buffer_load_dwordx4 %[l16], %[ws], s63 offen offset:2048 lds\n" \
    "s_waitcnt lgkmcnt(4)\n" \
    "v_mfma_f32_16x16x32_bf16 v[84:87], a[28:31], v[44:47], v[84:87]\n" \
    "v_and_b32 v53, 0xffff0000, v53\n" \
    "v_and_b32 v55, 0xffff0000, v55\n" \
    "v_mfma_f32_16x16x32_bf16 v[88:91], a[108:111], v[44:47], v[88:91]\n" \
    "v_and_b32 v65, 0xffff0000, v65\n" \
    "buffer_load_dwordx4 %[l16], %[ws], s63 offen offset:3072 lds\n" \
    "v_mfma_f32_16x16x32_bf16 v[92:95], a[188:191], v[44:47], v[92:95]\n" \
    "v_and_b32 v67, 0xffff0000, v67\n" \
    "v_add_f32 v52, v52, v124\n" \
    "s_waitcnt lgkmcnt(3)\n" \
    "v_mfma_f32_16x16x32_bf16 v[72:75], a[32:35], v[16:19], v[72:75]\n" \
    "v_add_f32 v54, v54, v126\n" \
    "ds_read_b128 v[32:35], v108 offset:10240\n" \
    "v_mfma_f32_16x16x32_bf16 v[76:79], a[112:115], v[16:19], v[76:79]\n" \
    "v_add_f32 v64, v64, v124\n" \
    "ds_read_b128 v[36:39], v108 offset:30720\n" \
    "v_mfma_f32_16x16x32_bf16 v[80:83], a[192:195], v[16:19], v[80:83]\n" \
    "v_add_f32 v66, v66, v126\n" \
    "s_add_u32 m0, s65, 8192\n" \
    "s_add_u32 s63, s63, 4096\n" \
    "buffer_load_dwordx4 %[l16], %[ws], s63 offen lds\n" \
    "s_waitcnt lgkmcnt(4)\n" \
    "v_mfma_f32_16x16x32_bf16 v[84:87], a[32:35], v[20:23], v[84:87]\n" \
    "v_add_f32 v53, v53, v125\n" \
    "v_add_f32 v55, v55, v127\n" \
    "v_mfma_f32_16x16x32_bf16 v[88:91], a[112:115], v[20:23], v[88:91]\n" \
    "v_add_f32 v65, v65, v125\n" \
    "buffer_load_dwordx4 %[l16], %[ws], s63 offen offset:1024 lds\n" \
    "v_mfma_f32_16x16x32_bf16 v[92:95], a[192:195], v[20:23], v[92:95]\n" \
    "v_add_f32 v67, v67, v127\n" \
    "v_cvt_pk_bf16_f32 v53, v52, v53\n" \
    "s_waitcnt lgkmcnt(3)\n" \
    "v_mfma_f32_16x16x32_bf16 v[72:75], a[36:39], v[24:27], v[72:75]\n" \
    "v_cvt_pk_bf16_f32 v55, v54, v55\n" \
    "ds_read_b128 v[40:43], v108 offset:11264\n" \
    "v_mfma_f32_16x16x32_bf16 v[76:79], a[116:119], v[24:27], v[76:79]\n" \
    "v_cvt_pk_bf16_f32 v65, v64, v65\n" \
    "ds_read_b128 v[44:47], v108 offset:31744\n" \
    "v_mfma_f32_16x16x32_bf16 v[80:83], a[196:199], v[24:27], v[80:83]\n" \
    "v_cvt_pk_bf16_f32 v67, v66, v67\n" \
    "v_lshlrev_b32 v52, 16, v53\n" \
    "s_waitcnt lgkmcnt(4)\n" \
    "v_mfma_f32_16x16x32_bf16 v[84:87], a[36:39], v[28:31], v[84:87]\n" \
    "v_lshlrev_b32 v54, 16, v55\n" \
    "v_lshlrev_b32 v64, 16, v65\n" \
    "v_mfma_f32_16x16x32_bf16 v[88:91], a[116:119], v[28:31], v[88:91]\n" \
    "v_lshlrev_b32 v66, 16, v67\n" \
    "v_and_b32 v53, 0xffff0000, v53\n" \
    "v_mfma_f32_16x16x32_bf16 v[92:95], a[196:199], v[28:31], v[92:95]\n" \
    "v_and_b32 v55, 0xffff0000, v55\n" \
    "v_and_b32 v65, 0xffff0000, v65\n" \
    "s_waitcnt lgkmcnt(3)\n" \
    "v_mfma_f32_16x16x32_bf16 v[72:75], a[40:43], v[32:35], v[72:75]\n" \
    "v_and_b32 v67, 0xffff0000, v67\n" \
    "ds_read_b128 v[16:19], v108 offset:12288\n" \
    "v_mfma_f32_16x16x32_bf16 v[76:79], a[120:123], v[32:35], v[76:79]\n" \
    "v_max_f32 v52, 0, v52\n" \
    "ds_read_b128 v[20:23], v108 offset:32768\n" \
    "v_mfma_f32_16x16x32_bf16 v[80:83], a[200:203], v[32:35], v[80:83]\n" \
    "v_max_f32 v54, 0, v54\n" \
    "v_max_f32 v64, 0, v64\n" \
    "s_waitcnt lgkmcnt(4)\n" \
    "v_mfma_f32_16x16x32_bf16 v[84:87], a[40:43], v[36:39], v[84:87]\n" \
    "v_max_f32 v66, 0, v66\n" \
    "v_max_f32 v53, 0, v53\n" \
    "v_mfma_f32_16x16x32_bf16 v[88:91], a[120:123], v[36:39], v[88:91]\n" \
    "v_max_f32 v55, 0, v55\n" \
    "v_max_f32 v65, 0, v65\n" \
    "v_mfma_f32_16x16x32_bf16 v[92:95], a[200:203], v[36:39], v[92:95]\n" \
    "v_max_f32 v67, 0, v67\n" \
    "v_mul_f32 v52, v52, v52\n" \
    "s_waitcnt lgkmcnt(3)\n" \
    "v_mfma_f32_16x16x32_bf16 v[72:75], a[44:47], v[40:43], v[72:75]\n" \
    "v_mul_f32 v54, v54, v54\n" \
    "ds_read_b128 v[24:27], v108 offset:13312\n" \
    "v_mfma_f32_16x16x32_bf16 v[76:79], a[124:127], v[40:43], v[76:79]\n" \
    "v_mul_f32 v64, v64, v64\n" \
    "ds_read_b128 v[28:31], v108 offset:33792\n" \
    "v_mfma_f32_16x16x32_bf16 v[80:83], a[204:207], v[40:43], v[80:83]\n" \
    "v_mul_f32 v66, v66, v66\n" \
    "v_mul_f32 v53, v53, v53\n" \
    "s_waitcnt lgkmcnt(4)\n" \
    "v_mfma_f32_16x16x32_bf16 v[84:87], a[44:47], v[44:47], v[84:87]\n" \
    "v_mul_f32 v55, v55, v55\n" \
    "v_mul_f32 v65, v65, v65\n" \
    "v_mfma_f32_16x16x32_bf16 v[88:91], a[124:127], v[44:47], v[88:91]\n" \
    "v_mul_f32 v67, v67, v67\n" \
    "v_cvt_pk_bf16_f32 v100, v52, v53\n" \
    "v_mfma_f32_16x16x32_bf16 v[92:95], a[204:207], v[44:47], v[92:95]\n" \
    "v_cvt_pk_bf16_f32 v101, v54, v55\n" \
    "v_cvt_pk_bf16_f32 v102, v64, v65\n" \
    "s_waitcnt lgkmcnt(3)\n" \
    "v_mfma_f32_16x16x32_bf16 v[72:75], a[48:51], v[16:19], v[72:75]\n" \
    "v_cvt_pk_bf16_f32 v103, v66, v67\n" \
    "ds_read_b128 v[32:35], v108 offset:14336\n" \
    "v_mfma_f32_16x16x32_bf16 v[76:79], a[128:131], v[16:19], v[76:79]\n" \
    "buffer_store_dwordx4 v[100:103], %[l16], %[ws], s62 offen offset:1024 sc1\n" \
    "ds_read_b128 v[36:39], v108 offset:34816\n" \
    "v_mfma_f32_16x16x32_bf16 v[80:83], a[208:211], v[16:19], v[80:83]\n" \
    "v_cvt_pk_bf16_f32 v57, v56, v57\n" \
    "v_cvt_pk_bf16_f32 v59, v58, v59\n" \
    "s_waitcnt lgkmcnt(4)\n" \
    "v_mfma_f32_16x16x32_bf16 v[84:87], a[48:51], v[20:23], v[84:87]\n" \
    "v_cvt_pk_bf16_f32 v69, v68, v69\n" \
    "v_cvt_pk_bf16_f32 v71, v70, v71\n" \
    "v_mfma_f32_16x16x32_bf16 v[88:91], a[128:131], v[20:23], v[88:91]\n" \
    "v_lshlrev_b32 v56, 16, v57\n" \
    "v_lshlrev_b32 v58, 16, v59\n" \
    "v_mfma_f32_16x16x32_bf16 v[92:95], a[208:211], v[20:23], v[92:95]\n" \
    "v_lshlrev_b32 v68, 16, v69\n" \
    "v_lshlrev_b32 v70, 16, v71\n" \
    "s_waitcnt lgkmcnt(3)\n" \
    "v_mfma_f32_16x16x32_bf16 v[72:75], a[52:55], v[24:27], v[72:75]\n" \
    "v_and_b32 v57, 0xffff0000, v57\n" \
    "ds_read_b128 v[40:43], v108 offset:15360\n" \
    "v_mfma_f32_16x16x32_bf16 v[76:79], a[132:135], v[24:27], v[76:79]\n" \
    "v_and_b32 v59, 0xffff0000, v59\n" \
    "ds_read_b128 v[44:47], v108 offset:35840\n" \
    "v_mfma_f32_16x16x32_bf16 v[80:83], a[212:215], v[24:27], v[80:83]\n" \
    "v_and_b32 v69, 0xffff0000, v69\n" \
    "v_and_b32 v71, 0xffff0000, v71\n" \
    "s_waitcnt lgkmcnt(4)\n" \
    "v_mfma_f32_16x16x32_bf16 v[84:87], a[52:55], v[28:31], v[84:87]\n" \
    "v_add_f32 v56, v56, v128\n" \
    "v_add_f32 v58, v58, v130\n" \
    "v_mfma_f32_16x16x32_bf16 v[88:91], a[132:135], v[28:31], v[88:91]\n" \
    "v_add_f32 v68, v68, v128\n" \
    "v_add_f32 v70, v70, v130\n" \
    "v_mfma_f32_16x16x32_bf16 v[92:95], a[212:215], v[28:31], v[92:95]\n" \
    "v_add_f32 v57, v57, v129\n" \
    "v_add_f32 v59, v59, v131\n" \
    "s_waitcnt lgkmcnt(3)\n" \
    "v_mfma_f32_16x16x32_bf16 v[72:75], a[56:59], v[32:35], v[72:75]\n" \
    "v_add_f32 v69, v69, v129\n" \
    "ds_read_b128 v[16:19], v108 offset:16384\n" \
    "v_mfma_f32_16x16x32_bf16 v[76:79], a[136:139], v[32:35], v[76:79]\n" \
    "v_add_f32 v71, v71, v131\n" \
    "ds_read_b128 v[20:23], v108 offset:36864\n" \
    "v_mfma_f32_16x16x32_bf16 v[80:83], a[216:219], v[32:35], v[80:83]\n" \
    "v_cvt_pk_bf16_f32 v57, v56, v57\n" \
    "v_cvt_pk_bf16_f32 v59, v58, v59\n" \
    "s_waitcnt lgkmcnt(4)\n" \
    "v_mfma_f32_16x16x32_bf16 v[84:87], a[56:59], v[36:39], v[84:87]\n" \
    "v_cvt_pk_bf16_f32 v69, v68, v69\n" \
    "v_cvt_pk_bf16_f32 v71, v70, v71\n" \
    "v_mfma_f32_16x16x32_bf16 v[88:91], a[136:139], v[36:39], v[88:91]\n" \
    "v_lshlrev_b32 v56, 16, v57\n" \
    "v_lshlrev_b32 v58, 16, v59\n" \
    "v_mfma_f32_16x16x32_bf16 v[92:95], a[216:219], v[36:39], v[92:95]\n" \
    "v_lshlrev_b32 v68, 16, v69\n" \
    "v_lshlrev_b32 v70, 16, v71\n" \
    "s_waitcnt lgkmcnt(3)\n" \
    "v_mfma_f32_16x16x32_bf16 v[72:75], a[60:63], v[40:43], v[72:75]\n" \
    "v_and_b32 v57, 0xffff0000, v57\n" \
    "ds_read_b128 v[24:27], v108 offset:17408\n" \
    "v_mfma_f32_16x16x32_bf16 v[76:79], a[140:143], v[40:43], v[76:79]\n" \
    "v_and_b32 v59, 0xffff0000, v59\n" \
    "ds_read_b128 v[28:31], v108 offset:37888\n" \
    "v_mfma_f32_16x16x32_bf16 v[80:83], a[220:223], v[40:43], v[80:83]\n" \
    "v_and_b32 v69, 0xffff0000, v69\n" \
    "v_and_b32 v71, 0xffff0000, v71\n" \
    "s_waitcnt lgkmcnt(4)\n" \
    "v_mfma_f32_16x16x32_bf16 v[84:87], a[60:63], v[44:47], v[84:87]\n" \
    "v_max_f32 v56, 0, v56\n" \
    "v_max_f32 v58, 0, v58\n" \
    "v_mfma_f32_16x16x32_bf16 v[88:91], a[140:143], v[44:47], v[88:91]\n" \
    "v_max_f32 v68, 0, v68\n" \
    "v_max_f32 v70, 0, v70\n" \
    "v_mfma_f32_16x16x32_bf16 v[92:95], a[220:223], v[44:47], v[92:95]\n" \
    "v_max_f32 v57, 0, v57\n" \
    "v_max_f32 v59, 0, v59\n" \
    "s_waitcnt lgkmcnt(3)\n" \
    "v_mfma_f32_16x16x32_bf16 v[72:75], a[64:67], v[16:19], v[72:75]\n" \
    "v_max_f32 v69, 0, v69\n" \
    "ds_read_b128 v[32:35], v108 offset:18432\n" \
    "v_mfma_f32_16x16x32_bf16 v[76:79], a[144:147], v[16:19], v[76:79]\n" \
    "v_max_f32 v71, 0, v71\n" \
    "ds_read_b128 v[36:39], v108 offset:38912\n" \
    "v_mfma_f32_16x16x32_bf16 v[80:83], a[224:227], v[16:19], v[80:83]\n" \
    "v_mul_f32 v56, v56, v56\n" \
    "v_mul_f32 v58, v58, v58\n" \
    "s_waitcnt lgkmcnt(4)\n" \
    "v_mfma_f32_16x16x32_bf16 v[84:87], a[64:67], v[20:23], v[84:87]\n" \
    "v_mul_f32 v68, v68, v68\n" \
    "v_mul_f32 v70, v70, v70\n" \
    "v_mfma_f32_16x16x32_bf16 v[88:91], a[144:147], v[20:23], v[88:91]\n" \
    "v_mul_f32 v57, v57, v57\n" \
    "v_mul_f32 v59, v59, v59\n" \
    "v_mfma_f32_16x16x32_bf16 v[92:95], a[224:227], v[20:23], v[92:95]\n" \
    "v_mul_f32 v69, v69, v69\n" \
    "v_mul_f32 v71, v71, v71\n" \
    "s_waitcnt lgkmcnt(3)\n" \
    "v_mfma_f32_16x16x32_bf16 v[72:75], a[68:71], v[24:27], v[72:75]\n" \
    "v_cvt_pk_bf16_f32 v104, v56, v57\n" \
    "ds_read_b128 v[40:43], v108 offset:19456\n" \
    "v_mfma_f32_16x16x32_bf16 v[76:79], a[148:151], v[24:27], v[76:79]\n" \
    "v_cvt_pk_bf16_f32 v105, v58, v59\n" \
    "ds_read_b128 v[44:47], v108 offset:39936\n" \
    "v_mfma_f32_16x16x32_bf16 v[80:83], a[228:231], v[24:27], v[80:83]\n" \
    "v_cvt_pk_bf16_f32 v106, v68, v69\n" \
    "v_cvt_pk_bf16_f32 v107, v70, v71\n" \
    "s_waitcnt lgkmcnt(4)\n" \
    "v_mfma_f32_16x16x32_bf16 v[84:87], a[68:71], v[28:31], v[84:87]\n" \
    "buffer_store_dwordx4 v[104:107], %[l16], %[ws], s62 offen offset:2048 sc1\n" \
    "v_mfma_f32_16x16x32_bf16 v[88:91], a[148:151], v[28:31], v[88:91]\n" \
    "v_mfma_f32_16x16x32_bf16 v[92:95], a[228:231], v[28:31], v[92:95]\n" \
    "s_waitcnt lgkmcnt(3)\n" \
    "v_mfma_f32_16x16x32_bf16 v[72:75], a[72:75], v[32:35], v[72:75]\n" \
    "ds_read_b128 v[16:19], v109\n" \
    "v_mfma_f32_16x16x32_bf16 v[76:79], a[152:155], v[32:35], v[76:79]\n" \
    "ds_read_b128 v[20:23], v109 offset:20480\n" \
    "v_mfma_f32_16x16x32_bf16 v[80:83], a[232:235], v[32:35], v[80:83]\n" \
    "s_waitcnt lgkmcnt(4)\n" \
    "v_mfma_f32_16x16x32_bf16 v[84:87], a[72:75], v[36:39], v[84:87]\n" \
    "v_mfma_f32_16x16x32_bf16 v[88:91], a[152:155], v[36:39], v[88:91]\n" \
    "v_mfma_f32_16x16x32_bf16 v[92:95], a[232:235], v[36:39], v[92:95]\n" \
    "s_waitcnt lgkmcnt(3)\n" \
    "v_mfma_f32_16x16x32_bf16 v[72:75], a[76:79], v[40:43], v[72:75]\n" \
    "ds_read_b128 v[24:27], v109 offset:1024\n" \
    "v_mfma_f32_16x16x32_bf16 v[76:79], a[156:159], v[40:43], v[76:79]\n" \
    "ds_read_b128 v[28:31], v109 offset:21504\n" \
    "v_mfma_f32_16x16x32_bf16 v[80:83], a[236:239], v[40:43], v[80:83]\n" \
    "s_waitcnt lgkmcnt(4)\n" \
    "v_mfma_f32_16x16x32_bf16 v[84:87], a[76:79], v[44:47], v[84:87]\n" \
    "v_mfma_f32_16x16x32_bf16 v[88:91], a[156:159], v[44:47], v[88:91]\n" \
    "v_mfma_f32_16x16x32_bf16 v[92:95], a[236:239], v[44:47], v[92:95]\n" \
    "s_add_u32 s62, s62, 98304\n" \
    "s_add_u32 s60, s60, 1\n" \
    "s_cmp_lt_u32 s60, 64\n" \
    "s_cbranch_scc1 Lf_loop\n" \
    "v_cvt_pk_bf16_f32 v73, v72, v73\n" \
    "v_cvt_pk_bf16_f32 v75, v74, v75\n" \
    "v_cvt_pk_bf16_f32 v85, v84, v85\n" \
    "v_cvt_pk_bf16_f32 v87, v86, v87\n" \
    "v_lshlrev_b32 v72, 16, v73\n" \
    "v_lshlrev_b32 v74, 16, v75\n" \
    "v_lshlrev_b32 v84, 16, v85\n" \
    "v_lshlrev_b32 v86, 16, v87\n" \
    "v_and_b32 v73, 0xffff0000, v73\n" \
    "v_and_b32 v75, 0xffff0000, v75\n" \
    "v_and_b32 v85, 0xffff0000, v85\n" \
    "v_and_b32 v87, 0xffff0000, v87\n" \
    "v_add_f32 v72, v72, v120\n" \
    "v_add_f32 v74, v74, v122\n" \
    "v_add_f32 v84, v84, v120\n" \
    "v_add_f32 v86, v86, v122\n" \
    "v_add_f32 v73, v73, v121\n" \
    "v_add_f32 v75, v75, v123\n" \
    "v_add_f32 v85, v85, v121\n" \
    "v_add_f32 v87, v87, v123\n" \
    "v_cvt_pk_bf16_f32 v73, v72, v73\n" \
    "v_cvt_pk_bf16_f32 v75, v74, v75\n" \
    "v_cvt_pk_bf16_f32 v85, v84, v85\n" \
    "v_cvt_pk_bf16_f32 v87, v86, v87\n" \
    "v_lshlrev_b32 v72, 16, v73\n" \
    "v_lshlrev_b32 v74, 16, v75\n" \
    "v_lshlrev_b32 v84, 16, v85\n" \
    "v_lshlrev_b32 v86, 16, v87\n" \
    "v_and_b32 v73, 0xffff0000, v73\n" \
    "v_and_b32 v75, 0xffff0000, v75\n" \
    "v_and_b32 v85, 0xffff0000, v85\n" \
    "v_and_b32 v87, 0xffff0000, v87\n" \
    "v_max_f32 v72, 0, v72\n" \
    "v_max_f32 v74, 0, v74\n" \
    "v_max_f32 v84, 0, v84\n" \
    "v_max_f32 v86, 0, v86\n" \
    "v_max_f32 v73, 0, v73\n" \
    "v_max_f32 v75, 0, v75\n" \
    "v_max_f32 v85, 0, v85\n" \
    "v_max_f32 v87, 0, v87\n" \
    "v_mul_f32 v72, v72, v72\n" \
    "v_mul_f32 v74, v74, v74\n" \
    "v_mul_f32 v84, v84, v84\n" \
    "v_mul_f32 v86, v86, v86\n" \
    "v_mul_f32 v73, v73, v73\n" \
    "v_mul_f32 v75, v75, v75\n" \
    "v_mul_f32 v85, v85, v85\n" \
    "v_mul_f32 v87, v87, v87\n" \
    "v_cvt_pk_bf16_f32 v96, v72, v73\n" \
    "v_cvt_pk_bf16_f32 v97, v74, v75\n" \
    "v_cvt_pk_bf16_f32 v98, v84, v85\n" \
    "v_cvt_pk_bf16_f32 v99, v86, v87\n" \
    "buffer_store_dwordx4 v[96:99], %[l16], %[ws], s62 offen sc1\n" \
    "v_cvt_pk_bf16_f32 v77, v76, v77\n" \
    "v_cvt_pk_bf16_f32 v79, v78, v79\n" \
    "v_cvt_pk_bf16_f32 v89, v88, v89\n" \
    "v_cvt_pk_bf16_f32 v91, v90, v91\n" \
    "v_lshlrev_b32 v76, 16, v77\n" \
    "v_lshlrev_b32 v78, 16, v79\n" \
    "v_lshlrev_b32 v88, 16, v89\n" \
    "v_lshlrev_b32 v90, 16, v91\n" \
    "v_and_b32 v77, 0xffff0000, v77\n" \
    "v_and_b32 v79, 0xffff0000, v79\n" \
    "v_and_b32 v89, 0xffff0000, v89\n" \
    "v_and_b32 v91, 0xffff0000, v91\n" \
    "v_add_f32 v76, v76, v124\n" \
    "v_add_f32 v78, v78, v126\n" \
    "v_add_f32 v88, v88, v124\n" \
    "v_add_f32 v90, v90, v126\n" \
    "v_add_f32 v77, v77, v125\n" \
    "v_add_f32 v79, v79, v127\n" \
    "v_add_f32 v89, v89, v125\n" \
    "v_add_f32 v91, v91, v127\n" \
    "v_cvt_pk_bf16_f32 v77, v76, v77\n" \
    "v_cvt_pk_bf16_f32 v79, v78, v79\n" \
    "v_cvt_pk_bf16_f32 v89, v88, v89\n" \
    "v_cvt_pk_bf16_f32 v91, v90, v91\n" \
    "v_lshlrev_b32 v76, 16, v77\n" \
    "v_lshlrev_b32 v78, 16, v79\n" \
    "v_lshlrev_b32 v88, 16, v89\n" \
    "v_lshlrev_b32 v90, 16, v91\n" \
    "v_and_b32 v77, 0xffff0000, v77\n" \
    "v_and_b32 v79, 0xffff0000, v79\n" \
    "v_and_b32 v89, 0xffff0000, v89\n" \
    "v_and_b32 v91, 0xffff0000, v91\n" \
    "v_max_f32 v76, 0, v76\n" \
    "v_max_f32 v78, 0, v78\n" \
    "v_max_f32 v88, 0, v88\n" \
    "v_max_f32 v90, 0, v90\n" \
    "v_max_f32 v77, 0, v77\n" \
    "v_max_f32 v79, 0, v79\n" \
    "v_max_f32 v89, 0, v89\n" \
    "v_max_f32 v91, 0, v91\n" \
    "v_mul_f32 v76, v76, v76\n" \
    "v_mul_f32 v78, v78, v78\n" \
    "v_mul_f32 v88, v88, v88\n" \
    "v_mul_f32 v90, v90, v90\n" \
    "v_mul_f32 v77, v77, v77\n" \
    "v_mul_f32 v79, v79, v79\n" \
    "v_mul_f32 v89, v89, v89\n" \
    "v_mul_f32 v91, v91, v91\n" \
    "v_cvt_pk_bf16_f32 v100, v76, v77\n" \
    "v_cvt_pk_bf16_f32 v101, v78, v79\n" \
    "v_cvt_pk_bf16_f32 v102, v88, v89\n" \
    "v_cvt_pk_bf16_f32 v103, v90, v91\n" \
    "buffer_store_dwordx4 v[100:103], %[l16], %[ws], s62 offen offset:1024 sc1\n" \
    "v_cvt_pk_bf16_f32 v81, v80, v81\n" \
    "v_cvt_pk_bf16_f32 v83, v82, v83\n" \
    "v_cvt_pk_bf16_f32 v93, v92, v93\n" \
    "v_cvt_pk_bf16_f32 v95, v94, v95\n" \
    "v_lshlrev_b32 v80, 16, v81\n" \
    "v_lshlrev_b32 v82, 16, v83\n" \
    "v_lshlrev_b32 v92, 16, v93\n" \
    "v_lshlrev_b32 v94, 16, v95\n" \
    "v_and_b32 v81, 0xffff0000, v81\n" \
    "v_and_b32 v83, 0xffff0000, v83\n" \
    "v_and_b32 v93, 0xffff0000, v93\n" \
    "v_and_b32 v95, 0xffff0000, v95\n" \
    "v_add_f32 v80, v80, v128\n" \
    "v_add_f32 v82, v82, v130\n" \
    "v_add_f32 v92, v92, v128\n" \
    "v_add_f32 v94, v94, v130\n" \
    "v_add_f32 v81, v81, v129\n" \
    "v_add_f32 v83, v83, v131\n" \
    "v_add_f32 v93, v93, v129\n" \
    "v_add_f32 v95, v95, v131\n" \
    "v_cvt_pk_bf16_f32 v81, v80, v81\n" \
    "v_cvt_pk_bf16_f32 v83, v82, v83\n" \
    "v_cvt_pk_bf16_f32 v93, v92, v93\n" \
    "v_cvt_pk_bf16_f32 v95, v94, v95\n" \
    "v_lshlrev_b32 v80, 16, v81\n" \
    "v_lshlrev_b32 v82, 16, v83\n" \
    "v_lshlrev_b32 v92, 16, v93\n" \
    "v_lshlrev_b32 v94, 16, v95\n" \
    "v_and_b32 v81, 0xffff0000, v81\n" \
    "v_and_b32 v83, 0xffff0000, v83\n" \
    "v_and_b32 v93, 0xffff0000, v93\n" \
    "v_and_b32 v95, 0xffff0000, v95\n" \
    "v_max_f32 v80, 0, v80\n" \
    "v_max_f32 v82, 0, v82\n" \
    "v_max_f32 v92, 0, v92\n" \
    "v_max_f32 v94, 0, v94\n" \
    "v_max_f32 v81, 0, v81\n" \
    "v_max_f32 v83, 0, v83\n" \
    "v_max_f32 v93, 0, v93\n" \
    "v_max_f32 v95, 0, v95\n" \
    "v_mul_f32 v80, v80, v80\n" \
    "v_mul_f32 v82, v82, v82\n" \
    "v_mul_f32 v92, v92, v92\n" \
    "v_mul_f32 v94, v94, v94\n" \
    "v_mul_f32 v81, v81, v81\n" \
    "v_mul_f32 v83, v83, v83\n" \
    "v_mul_f32 v93, v93, v93\n" \
    "v_mul_f32 v95, v95, v95\n" \
    "v_cvt_pk_bf16_f32 v104, v80, v81\n" \
    "v_cvt_pk_bf16_f32 v105, v82, v83\n" \
    "v_cvt_pk_bf16_f32 v106, v92, v93\n" \
    "v_cvt_pk_bf16_f32 v107, v94, v95\n" \
    "buffer_store_dwordx4 v[104:107], %[l16], %[ws], s62 offen offset:2048 sc1\n" \
    "s_waitcnt vmcnt(0)\n" \
    "s_barrier\n" \
    "s_cmp_lg_u32 %[wave], 0\n" \
    "s_cbranch_scc1 Lf_fle\n" \
    "v_mov_b32 v110, s60\n" \
    "buffer_store_dword v110, %[one], %[ws], %[flag] offen sc1\n" \
    "Lf_fle:\n" \
    "s_add_u32 s62, s62, 98304\n" \
    "s_waitcnt lgkmcnt(0)\n" \
    "s_waitcnt vmcnt(0)\n" \
    "s_endpgm\n"
#define CLOB_MID "v16", "v17", "v18", "v19", "v20", "v21", "v22", "v23", "v24", "v25", "v26", "v27", "v28", "v29", "v30", "v31", "v32", "v33", "v34", "v35", "v36", "v37", "v38", "v39", "v40", "v41", "v42", "v43", "v44", "v45", "v46", "v47", "v48", "v49", "v50", "v51", "v52", "v53", "v54", "v55", "v56", "v57", "v58", "v59", "v60", "v61", "v62", "v63", "v64", "v65", "v66", "v67", "v68", "v69", "v70", "v71", "v72", "v73", "v74", "v75", "v76", "v77", "v78", "v79", "v80", "v81", "v82", "v83", "v84", "v85", "v86", "v87", "v88", "v89", "v90", "v91", "v92", "v93", "v94", "v95", "v96", "v97", "v98", "v99", "v100", "v101", "v102", "v103", "v104", "v105", "v106", "v107", "v108", "v109", "v110", "v111", "v112", "v113", "v114", "v115", "v116", "v117", "v118", "v119", "v120", "v121", "v122", "v123", "v124", "v125", "v126", "v127", "v128", "v129", "v130", "v131", "v132", "v133", "v134", "v135", "v136", "v137", "v138", "v139", "v140", "v141", "v142", "v143", "v144", "v145", "v146", "v147", "v148", "v149", "v150", "v151", "v152", "v153", "v154", "v155", "v156", "v157", "v158", "v159", "v160", "v161", "v162", "v163", "v164", "v165", "v166", "v167", "v168", "v169", "v170", "v171", "v172", "v173", "v174", "v175", "v176", "v177", "v178", "v179", "v180", "v181", "v182", "v183", "v184", "v185", "v186", "v187", "v188", "v189", "v190", "v191", "a0", "a1", "a2", "a3", "a4", "a5", "a6", "a7", "a8", "a9", "a10", "a11", "a12", "a13", "a14", "a15", "a16", "a17", "a18", "a19", "a20", "a21", "a22", "a23", "a24", "a25", "a26", "a27", "a28", "a29", "a30", "a31", "a32", "a33", "a34", "a35", "a36", "a37", "a38", "a39", "a40", "a41", "a42", "a43", "a44", "a45", "a46", "a47", "a48", "a49", "a50", "a51", "a52", "a53", "a54", "a55", "a56", "a57", "a58", "a59", "a60", "a61", "a62", "a63", "a64", "a65", "a66", "a67", "a68", "a69", "a70", "a71", "a72", "a73", "a74", "a75", "a76", "a77", "a78", "a79", "a80", "a81", "a82", "a83", "a84", "a85", "a86", "a87", "a88", "a89", "a90", "a91", "a92", "a93", "a94", "a95", "a96", "a97", "a98", "a99", "a100", "a101", "a102", "a103", "a104", "a105", "a106", "a107", "a108", "a109", "a110", "a111", "a112", "a113", "a114", "a115", "a116", "a117", "a118", "a119", "a120", "a121", "a122", "a123", "a124", "a125", "a126", "a127", "a128", "a129", "a130", "a131", "a132", "a133", "a134", "a135", "a136", "a137", "a138", "a139", "a140", "a141", "a142", "a143", "a144", "a145", "a146", "a147", "a148", "a149", "a150", "a151", "a152", "a153", "a154", "a155", "a156", "a157", "a158", "a159", "a160", "a161", "a162", "a163", "a164", "a165", "a166", "a167", "a168", "a169", "a170", "a171", "a172", "a173", "a174", "a175", "a176", "a177", "a178", "a179", "a180", "a181", "a182", "a183", "a184", "a185", "a186", "a187", "a188", "a189", "a190", "a191", "a192", "a193", "a194", "a195", "a196", "a197", "a198", "a199", "a200", "a201", "a202", "a203", "a204", "a205", "a206", "a207", "a208", "a209", "a210", "a211", "a212", "a213", "a214", "a215", "a216", "a217", "a218", "a219", "a220", "a221", "a222", "a223", "a224", "a225", "a226", "a227", "a228", "a229", "a230", "a231", "a232", "a233", "a234", "a235", "a236", "a237", "a238", "a239", "s60", "s61", "s62", "s63", "s64", "s65", "s66", "s67", "s68", "s69", "s70", "s71", "s72", "s73", "s74", "s75", "s76", "s77", "s78", "s79", "s80", "s81", "s82", "s83", "s84", "s85", "vcc", "scc", "m0", "memory"
#define ULIN 0
#define LFLAG 0
#define CLOB_BIG "v2", "v3", "v4", "v5", "v6", "v7", "v8", "v9", "v10", "v11", "v12", "v13", "v14", "v15", "v16", "v17", "v18", "v19", "v20", "v21", "v22", "v23", "v24", "v25", "v26", "v27", "v28", "v29", "v30", "v31", "v32", "v33", "v34", "v35", "v36", "v37", "v38", "v39", "v40", "v41", "v42", "v43", "v44", "v45", "v46", "v47", "v48", "v49", "v50", "v51", "v52", "v53", "v54", "v55", "v56", "v57", "v58", "v59", "v60", "v61", "v62", "v63", "v64", "v65", "v66", "v67", "v68", "v69", "v70", "v71", "v72", "v73", "v74", "v75", "v76", "v77", "v78", "v79", "v80", "v81", "v82", "v83", "v84", "v85", "v86", "v87", "v88", "v89", "v90", "v91", "v92", "v93", "v94", "v95", "v96", "v97", "v98", "v99", "v100", "v101", "v102", "v103", "v104", "v105", "v106", "v107", "v108", "v109", "v110", "v111", "v112", "v113", "v114", "v115", "v116", "v117", "v118", "v119", "v120", "v121", "v122", "v123", "v124", "v125", "v126", "v127", "v128", "v129", "v130", "v131", "v132", "v133", "v134", "v135", "v136", "v137", "v138", "v139", "v140", "v141", "v142", "v143", "v144", "v145", "v146", "v147", "v148", "v149", "v150", "v151", "v152", "v153", "v154", "v155", "v156", "v157", "v158", "v159", "v160", "v161", "v162", "v163", "v164", "v165", "v166", "v167", "v168", "v169", "v170", "v171", "v172", "v173", "v174", "v175", "v176", "v177", "v178", "v179", "v180", "v181", "v182", "v183", "v184", "v185", "v186", "v187", "v188", "v189", "v190", "v191", "v192", "v193", "v194", "v195", "v196", "v197", "v198", "v199", "v200", "v201", "v202", "v203", "v204", "v205", "v206", "v207", "v208", "v209", "v210", "v211", "v212", "v213", "v214", "v215", "v216", "v217", "v218", "v219", "v220", "v221", "v222", "v223", "v224", "v225", "v226", "v227", "v228", "v229", "v230", "v231", "v232", "v233", "v234", "v235", "v236", "v237", "v238", "v239", "v240", "v241", "v242", "v243", "v244", "v245", "v246", "v247", "v248", "v249", "v250", "v251", "v252", "v253", "v254", "v255", "a0", "a1", "a2", "a3", "a4", "a5", "a6", "a7", "a8", "a9", "a10", "a11", "a12", "a13", "a14", "a15", "a16", "a17", "a18", "a19", "a20", "a21", "a22", "a23", "a24", "a25", "a26", "a27", "a28", "a29", "a30", "a31", "a32", "a33", "a34", "a35", "a36", "a37", "a38", "a39", "a40", "a41", "a42", "a43", "a44", "a45", "a46", "a47", "a48", "a49", "a50", "a51", "a52", "a53", "a54", "a55", "a56", "a57", "a58", "a59", "a60", "a61", "a62", "a63", "a64", "a65", "a66", "a67", "a68", "a69", "a70", "a71", "a72", "a73", "a74", "a75", "a76", "a77", "a78", "a79", "a80", "a81", "a82", "a83", "a84", "a85", "a86", "a87", "a88", "a89", "a90", "a91", "a92", "a93", "a94", "a95", "a96", "a97", "a98", "a99", "a100", "a101", "a102", "a103", "a104", "a105", "a106", "a107", "a108", "a109", "a110", "a111", "a112", "a113", "a114", "a115", "a116", "a117", "a118", "a119", "a120", "a121", "a122", "a123", "a124", "a125", "a126", "a127", "a128", "a129", "a130", "a131", "a132", "a133", "a134", "a135", "a136", "a137", "a138", "a139", "a140", "a141", "a142", "a143", "a144", "a145", "a146", "a147", "a148", "a149", "a150", "a151", "a152", "a153", "a154", "a155", "a156", "a157", "a158", "a159", "a160", "a161", "a162", "a163", "a164", "a165", "a166", "a167", "a168", "a169", "a170", "a171", "a172", "a173", "a174", "a175", "a176", "a177", "a178", "a179", "a180", "a181", "a182", "a183", "a184", "a185", "a186", "a187", "a188", "a189", "a190", "a191", "a192", "a193", "a194", "a195", "a196", "a197", "a198", "a199", "a200", "a201", "a202", "a203", "a204", "a205", "a206", "a207", "a208", "a209", "a210", "a211", "a212", "a213", "a214", "a215", "a216", "a217", "a218", "a219", "a220", "a221", "a222", "a223", "a224", "a225", "a226", "a227", "a228", "a229", "a230", "a231", "a232", "a233", "a234", "a235", "a236", "a237", "a238", "a239", "a240", "a241", "a242", "a243", "a244", "a245", "a246", "a247", "a248", "a249", "a250", "a251", "a252", "a253", "a254", "a255", "s44", "s45", "s46", "s47", "s48", "s49", "s50", "s51", "s52", "s53", "s54", "s55", "s56", "s57", "s58", "s59", "s60", "s61", "s62", "s63", "s64", "s65", "s66", "s67", "s68", "s69", "s70", "s71", "s72", "s73", "s74", "s75", "s76", "s77", "s78", "s79", "s80", "s81", "s82", "s83", "s84", "s85", "s86", "s87", "s88", "s89", "s90", "s91", "s92", "s93", "s94", "s95", "s96", "s97", "s98", "s99", "vcc", "scc", "m0", "memory"
#define CLOB_R "v16", "v17", "v18", "v19", "v20", "v21", "v22", "v23", "v24", "v25", "v26", "v27", "v28", "v29", "v30", "v31", "v32", "v33", "v34", "v35", "v36", "v37", "v38", "v39", "v40", "v41", "v42", "v43", "v44", "v45", "v46", "v47", "v48", "v49", "v50", "v51", "v52", "v53", "v54", "v55", "v56", "v57", "v58", "v59", "v60", "v61", "v62", "v63", "v64", "v65", "v66", "v67", "v68", "v69", "v70", "v71", "v72", "v73", "v74", "v75", "v76", "v77", "v78", "v79", "v80", "v81", "v82", "v83", "v84", "v85", "v86", "v87", "v88", "v89", "v90", "v91", "v92", "v93", "v94", "v95", "v96", "v97", "v98", "v99", "v100", "v101", "v102", "v103", "v104", "v105", "v106", "v107", "v108", "v109", "v110", "v111", "v112", "v113", "v114", "v115", "v116", "v117", "v118", "v119", "v120", "v121", "v122", "v123", "v124", "v125", "v126", "v127", "v128", "v129", "v130", "v131", "v132", "v133", "v134", "v135", "v136", "v137", "v138", "v139", "v140", "v141", "v142", "v143", "v144", "v145", "v146", "v147", "v148", "v149", "v150", "v151", "v152", "v153", "v154", "v155", "v156", "v157", "v158", "v159", "a0", "a1", "a2", "a3", "a4", "a5", "a6", "a7", "a8", "a9", "a10", "a11", "a12", "a13", "a14", "a15", "a16", "a17", "a18", "a19", "a20", "a21", "a22", "a23", "a24", "a25", "a26", "a27", "a28", "a29", "a30", "a31", "a32", "a33", "a34", "a35", "a36", "a37", "a38", "a39", "a40", "a41", "a42", "a43", "a44", "a45", "a46", "a47", "a48", "a49", "a50", "a51", "a52", "a53", "a54", "a55", "a56", "a57", "a58", "a59", "a60", "a61", "a62", "a63", "a64", "a65", "a66", "a67", "a68", "a69", "a70", "a71", "a72", "a73", "a74", "a75", "a76", "a77", "a78", "a79", "a80", "a81", "a82", "a83", "a84", "a85", "a86", "a87", "a88", "a89", "a90", "a91", "a92", "a93", "a94", "a95", "a96", "a97", "a98", "a99", "a100", "a101", "a102", "a103", "a104", "a105", "a106", "a107", "a108", "a109", "a110", "a111", "a112", "a113", "a114", "a115", "a116", "a117", "a118", "a119", "a120", "a121", "a122", "a123", "a124", "a125", "a126", "a127", "a128", "a129", "a130", "a131", "a132", "a133", "a134", "a135", "a136", "a137", "a138", "a139", "a140", "a141", "a142", "a143", "a144", "a145", "a146", "a147", "a148", "a149", "a150", "a151", "a152", "a153", "a154", "a155", "a156", "a157", "a158", "a159", "a160", "a161", "a162", "a163", "a164", "a165", "a166", "a167", "a168", "a169", "a170", "a171", "a172", "a173", "a174", "a175", "a176", "a177", "a178", "a179", "a180", "a181", "a182", "a183", "a184", "a185", "a186", "a187", "a188", "a189", "a190", "a191", "a192", "a193", "a194", "a195", "a196", "a197", "a198", "a199", "a200", "a201", "a202", "a203", "a204", "a205", "a206", "a207", "a208", "a209", "a210", "a211", "a212", "a213", "a214", "a215", "a216", "a217", "a218", "a219", "a220", "a221", "a222", "a223", "a224", "a225", "a226", "a227", "a228", "a229", "a230", "a231", "a232", "a233", "a234", "a235", "a236", "a237", "a238", "a239", "a240", "a241", "a242", "a243", "a244", "a245", "a246", "a247", "a248", "a249", "a250", "a251", "a252", "a253", "a254", "a255", "s44", "s45", "s46", "s47", "s48", "s49", "s50", "s51", "s52", "s53", "s54", "s55", "s56", "s57", "s58", "s59", "s60", "s61", "s62", "s63", "s64", "s65", "s66", "s67", "s68", "s69", "s70", "s71", "s72", "s73", "s74", "s75", "s76", "s77", "s78", "s79", "s80", "s81", "s82", "s83", "s84", "s85", "s86", "s87", "s88", "s89", "s90", "s91", "s92", "s93", "s94", "s95", "s96", "s97", "s98", "s99", "vcc", "scc", "m0", "memory"

DI unsigned kx_off(int h, int w) { return (h * 2 + (w >> 1)) * 16384u + (w & 1) * 3072u; }
DI unsigned km_off(int h, int w) { return (h >> 1) * 16384u + ((h & 1) * 4 + w) * 1024u; }
DI unsigned ucell(unsigned c, unsigned b, unsigned par) { return ULIN ? (unsigned)(72 * MiB) + c * 32768u + b * 8192u : (unsigned)OFF_U + nearblk(c * 65536u, par ^ 1u) + b * 16384u; }
DI unsigned ubase(unsigned c, unsigned b) { return ULIN ? (unsigned)(72 * MiB) + c * 32768u + b * 8192u : (unsigned)OFF_U + c * 65536u + b * 16384u; }
template <int MODE> DI int wswz(int r) { return MODE == 0 ? (((r >> 1) & 1) | (((r >> 3) & 1) << 1)) : MODE == 3 ? (((r & 1) << 1) | (r & 4)) : (((r >> 2) & 1) | ((MODE - 1) << 1)); }
template <int UNITS, int MODE> DI unsigned wsrc(int i, int w, int lane, unsigned ldb) { const int c = i * 64 + lane, r = 8 * w + c / (2 * UNITS), pc = c % (2 * UNITS); return (unsigned)r * ldb + (unsigned)(((pc >> 1) ^ wswz<MODE>(r)) * 32 + (pc & 1) * 16); }
DI unsigned wtr0(int u, int lane) { const int r0 = 8 * (lane >> 4) + ((lane >> 2) & 3); return (unsigned)(r0 * 384 + ((u ^ wswz<0>(r0)) * 32) + (lane & 3) * 8); }
DI unsigned wtr_qk(int u, int lane) { const int q = (lane >> 2) & 3, r0 = 4 * (lane >> 4) + (q & 1), m = q >> 1; return (unsigned)(m * 12288 + r0 * 384 + ((u ^ (m ? wswz<2>(r0) : wswz<1>(r0))) * 32) + (lane & 3) * 8); }
DI unsigned wtr_o(int u2, int lane) { const int q = (lane >> 2) & 3, r0 = 4 * (lane >> 4) + (q & 1); return (unsigned)(r0 * 1280 + (((u2 + (q >> 1)) ^ wswz<3>(r0)) * 32) + (lane & 3) * 8); }
DI unsigned afrag_split(int lane) { const int g = lane >> 4, q = (lane >> 2) & 3, p = lane & 3; return g * 256 + (p & 1) * 128 + ((q & 1) + 4 * (q >> 1) + 2 * (p >> 1)) * 8; }
DI unsigned afrag_dup(int lane) { const int g = lane >> 4; return (g >> 1) * 256 + (lane & 1) * 128 + ((g & 1) * 8 + ((lane >> 2) & 3)) * 8; }
DI unsigned vfrag_dup(int lane) { return (lane >> 4) * 128 + ((lane >> 2) & 3) * 16 + (lane & 1) * 8; }
template <bool SPLIT> DI u32x4 poll_consts(int w, int lane) {
  const int t0 = w < 2 ? 3 * w : 2 * w + 2;
  u32x4 v;
  v.x = (t0 < 8 ? 3u * 16384u + t0 * 1024u : 2u * 16384u + 4096u) + lane * 16u;
  v.y = L_MEM + t0 * 1024 + lane * 16;
  v.z = L_MEM + (SPLIT ? afrag_split(lane) : afrag_dup(lane));
  v.w = (lane & 8) ? 0xEEEEu : 0x4444u;
  return v;
}

#define D_QK 1380
#define D_VO 1380
#define D_R0 -100
#define D_R1 4
#define D_R2 3000
DI unsigned ldc(const Params& p, int i) { return i == 2 ? D_QK : i == 3 ? D_VO : i == 4 ? D_R0 : i == 5 ? D_R1 : D_R2; }
#define DBP ((lane & 31) * 128u)
DI void role_qk(const Params& p, int h, unsigned par, LAS char* lds, int w) {
  const int lane = lane_id(), tid = w * 64 + lane, g = lane >> 4, i16 = lane & 15;
  const unsigned vres = L_RES + w * 20480 + lane * 16;
  LAS u32x4* tb = (LAS u32x4*)(lds + vres);
  tb[0] = poll_consts<false>(w, lane);
  u32x4 c6, c1, c2, c3, c4, c5;
  c1.x = (4 * g + 0 == i16 ? 0x3F80u : 0u) | (4 * g + 1 == i16 ? 0x3F800000u : 0u);
  c1.y = (4 * g + 2 == i16 ? 0x3F80u : 0u) | (4 * g + 3 == i16 ? 0x3F800000u : 0u);
  c1.z = kx_off(h, w) + lane * 16;
  c1.w = i16 >= 8 ? km_off(h, w) + (g * 8 + i16 - 8) * 16 : OOB;
  const unsigned dump = LDS_DROP;
  c2.x = (i16 < 8 && g >= 2) ? L_SX + (w * 8 + i16) * SXROW + 128 + (g - 2) * 16 : dump;
  c2.y = (i16 < 8) ? L_SX + (w * 8 + i16) * SXROW + (4 * (g >> 1) + (g & 1)) * 16 : dump;
  c2.z = L_SX + (lane >> 3) * SXROW + (lane & 7) * 16;
  c2.w = L_SX + (lane >> 3) * SXROW + 128 + (lane & 7) * 4;
  c3.x = (h >> 4) * 16384u + (h & 15) * 512u + lane * 8u;
  c3.y = (lane & 1) ? OOB : 2u * 16384u + h * 128u + lane * 2u;
  c3.z = DBP; c3.w = tid == 64 ? h * 128u : OOB;
#pragma unroll
  for (int i = 0; i < 3; ++i) { c4[i] = wsrc<12, 1>(i, w, lane, HD * 2) + h * DH * 2; c5[i] = wsrc<12, 2>(i, w, lane, HD * 2) + h * DH * 2; c6[i] = wtr_qk(3 * w + i, lane); }
  c4.w = c5.w = c6.w = 0;
  tb[64] = c1; tb[128] = c2; tb[192] = c3; tb[256] = c4; tb[320] = c5; tb[384] = c6;
  const rsrc_t rws = mkrsrc(p.ws, WS_SPAN), r0 = mkrsrc(p.wq, (size_t)DM * HD * 2), r1 = mkrsrc(p.wk, (size_t)DM * HD * 2);
  const unsigned flag = (unsigned)OFF_CTL + 4 * (C_KP + h * FS), stb = (unsigned)OFF_STAMP + h * 64 * 32;
  asm volatile("s_waitcnt lgkmcnt(0)\n s_nop 4\n" ASM_QK :: [vres] "v"(vres), [ws] "s"(rws), [w0] "s"(r0), [w1] "s"(r1), [wave] "s"(w), [par] "s"(par << 13), [flag] "s"(flag), [stb] "s"(stb), [d0] "s"(ldc(p, 2)) : CLOB_BIG);
}
DI void role_vo(const Params& p, int h, unsigned par, LAS char* lds, int w) {
  const int lane = lane_id(), tid = w * 64 + lane, g = lane >> 4, i16 = lane & 15, hi = lane >> 5;
  const unsigned vres = L_RES + w * 20480 + lane * 16;
  LAS u32x4* tb = (LAS u32x4*)(lds + vres);
  tb[0] = poll_consts<true>(w, lane);
  u32x4 c;
  c.x = L_SX + (48 * w + 16 * hi + i16) * 16 + (g & 1) * 8; c.y = L_SX + (48 * w + 32 + i16) * 16 + (g & 1) * 8 + hi * 4;
  c.z = (unsigned)(h * 256 + i16 * 16 + (((g & 1) << 1) | (g >> 1)) * 16384); c.w = (unsigned)(h * 256 + i16 * 16 + (g & 1) * 8 + hi * 16384); tb[64] = c;
#pragma unroll
  for (int i = 0; i < 3; ++i) c[i] = wsrc<12, 0>(i, w, lane, HD * 2) + h * DH * 2;
  c.w = L_SX + vfrag_dup(lane); tb[128] = c;
#pragma unroll
  for (int i = 0; i < 3; ++i) c[i] = wtr0(3 * w + i, lane);
  c.w = DBP; tb[192] = c;
#pragma unroll
  for (int k = 0; k < 3; ++k) {
    u32x4 a, b;
#pragma unroll
    for (int e = 0; e < 4; ++e) { const int i = 4 * k + e < 10 ? 4 * k + e : 9; a[e] = wsrc<40, 3>(i, w, lane, DM * 2) + h * DH * DM * 2; b[e] = wtr_o(10 * w + 2 * (i < 5 ? i : 4), lane); }
    tb[256 + 64 * k] = a; if (k < 2) tb[448 + 64 * k] = b;
  }
  const rsrc_t rws = mkrsrc(p.ws, WS_SPAN), r0 = mkrsrc(p.wv, (size_t)DM * HD * 2), r1 = mkrsrc(p.wo, (size_t)DM * HD * 2);
  const unsigned stb = (unsigned)OFF_STAMP + (32 + h) * 64 * 32;
  asm volatile("s_waitcnt lgkmcnt(0)\n s_nop 4\n" ASM_VO :: [vres] "v"(vres), [ws] "s"(rws), [w0] "s"(r0), [w1] "s"(r1), [wave] "s"(w), [par] "s"(par << 13), [kmb] "s"((unsigned)OFF_KM + km_off(h, w)), [stb] "s"(stb), [d0] "s"(ldc(p, 3)) : CLOB_BIG);
}
DI void role_r(const Params& p, int j, unsigned par, LAS char* lds, int w) {
  const int lane = lane_id(), tid = w * 64 + lane, g = lane >> 4, i16 = lane & 15, hi = lane >> 5;
  const rsrc_t rws = mkrsrc(p.ws, WS_SPAN);
  if (w >= 3) return;
  const int col = 16 * j + i16;
  const unsigned mblk = j < 32 ? 3u * 16384u + j * 256u : 2u * 16384u + 4096u + (j - 32) * 256u;
  const unsigned moff = (g & 1) ? OOB : mblk + hi * 128 + i16 * 8;
  { const int r = (g & 1) * 4; u32x2 v;
    v.x = nosent((unsigned)p.mem0[r * DM + col] | ((unsigned)p.mem0[(r + 1) * DM + col] << 16));
    v.y = nosent((unsigned)p.mem0[(r + 2) * DM + col] | ((unsigned)p.mem0[(r + 3) * DM + col] << 16));
    st64(v, rws, tid < 32 ? mblk + lane * 8 : OOB, par << 13); }
  const int pc = j * 48 + lane;
  const unsigned po = lane >= 48 ? OOB : pc < 1408 ? (pc >> 9) * 16384u + (pc & 511) * 16u : 3u * 16384u + (pc - 1408) * 16u;
  const unsigned mx = (unsigned)((g >> 1) * 512 + (i16 >> 1) * 64 + (g & 1) * 32 + (i16 & 1) * 16), mm = g * 256 + i16 * 8;
  const unsigned idx = (lane & 1) ? 0xEEEEu : 0x4444u, stb = (unsigned)OFF_STAMP + (64 + j) * 64 * 32;
  asm volatile("s_nop 4\n" ASM_R :: [l16] "v"(lane * 16), [mx] "v"(mx), [mm] "v"(mm), [dbp] "v"(DBP), [po] "v"(po), [mo] "v"(moff), [idx] "v"(idx), [fl] "v"((lane & 31) * FS * 4),
               [ws] "s"(rws), [wave] "s"(w), [par] "s"(par << 13), [ub] "s"(ubase(j, 0)), [yb] "s"((unsigned)OFF_YM + j * 16384), [stb] "s"(stb), [d0] "s"(ldc(p, 4)), [d1] "s"(ldc(p, 5)), [d2] "s"(ldc(p, 6)) : CLOB_R);
}

#define BST(k)
#define BSC(k)
#define TW_V0(acc) WAIT_V0()
#define TW_BAR(acc) BAR()
#define BSV(k, v)
#define BSLOT
#define BARG
#define BARG2
template <int V> struct IC { static constexpr int value = V; constexpr operator int() const { return V; } };
template <int N, int I = 0, class F> DI void static_for(F f) { if constexpr (I < N) { f(IC<I>{}); static_for<N, I + 1>(f); } }
DI bool wait_ge(gu32* w, unsigned want) {
  int cnt = SPIN_CAP;
  while (__builtin_amdgcn_readfirstlane(__hip_atomic_load(w, RLX_AGENT)) < want) { if (--cnt <= 0) return false; __builtin_amdgcn_s_sleep(4); }
  __builtin_amdgcn_fence(__ATOMIC_ACQUIRE, "wavefront");
  return true;
}
template <int N> DI bool wait_all_ge(gu32* w, unsigned want, int lane) {
  int cnt = SPIN_CAP;
  while (!__all(__hip_atomic_load(w + (lane & (N - 1)) * FS, RLX_AGENT) >= want)) { if (--cnt <= 0) return false; __builtin_amdgcn_s_sleep(4); }
  __builtin_amdgcn_fence(__ATOMIC_ACQUIRE, "wavefront");
  return true;
}
DI void give_up(const Params& p, unsigned code) { __hip_atomic_store((gu32*)(p.ws + OFF_CTL) + C_TMO, code, RLX_AGENT); }
DI void dma16(rsrc_t r, LAS char* dst, unsigned v, unsigned s) { __builtin_amdgcn_raw_ptr_buffer_load_lds(r, (LAS void*)dst, 16, v, s, 0, 0); }
DI float bf_lo(unsigned x) { return __builtin_bit_cast(float, x << 16); }
DI float bf_hi(unsigned x) { return __builtin_bit_cast(float, x & 0xFFFF0000u); }
DI float rbf(float x) { return bf_lo(cvtpk(x, x) & 0xFFFFu); }
DI bf16x8 as8(u32x4 v) { return __builtin_bit_cast(bf16x8, v); }
DI bf16x8 pack8(unsigned a, unsigned b, unsigned c, unsigned d) { u32x4 v = {a, b, c, d}; return as8(v); }

#define STB 0u
DI void role_kv(const Params& p, rsrc_t rws, bool isk, int h, unsigned par, int w BSLOT) {
  const uint16_t* W = isk ? p.wk : p.wv;
  const int flag = (isk ? C_KP : C_VP) + h * FS;
  const int lane = lane_id();
  const rsrc_t rw = mkrsrc(W, (size_t)DM * HD * 2);
  const unsigned cb = h * DH * 2;
  asm volatile("s_nop 4\n" ASM_PROJ :: [l16] "v"(lane * 16), [one] "v"(lane ? OOB : 0u), [src0] "v"(wsrc<12, 0>(0, w, lane, HD * 2) + cb), [src1] "v"(wsrc<12, 0>(1, w, lane, HD * 2) + cb), [src2] "v"(wsrc<12, 0>(2, w, lane, HD * 2) + cb),
               [tr0] "v"(wtr0(3 * w, lane)), [tr1] "v"(wtr0(3 * w + 1, lane)), [tr2] "v"(wtr0(3 * w + 2, lane)),
               [ws] "s"(rws), [w0] "s"(rw), [wave] "s"(w), [ob] "s"(isk ? (unsigned)OFF_KX + kx_off(h, w) : (unsigned)OFF_VX + h * 12288 + w * 3072), [ost] "s"(isk ? (unsigned)MiB : NH * 12288u), [msk] "s"(isk ? 0x2000u : 0u), [par] "s"(par << 13), [flag] "s"((unsigned)OFF_CTL + 4 * flag), [stb] "s"(STB) : CLOB_MID);
}
DI void role_f1(const Params& p, rsrc_t rws, int c, int w BSLOT) {
  const int lane = lane_id();
  const rsrc_t rw = mkrsrc(p.w1, (size_t)DM * FF * 2);
  const unsigned cb = c * DH * 2;
  const uint16_t* bp = p.b1 + c * DH + 48 * w + 4 * (lane >> 4);
  const u32x2 b0 = *(const u32x2*)bp, b1 = *(const u32x2*)(bp + 16), b2 = *(const u32x2*)(bp + 32);
  asm volatile("s_nop 4\n" ASM_F1 :: [l16] "v"(lane * 16), [one] "v"(lane ? OOB : 0u), [src0] "v"(wsrc<12, 0>(0, w, lane, FF * 2) + cb), [src1] "v"(wsrc<12, 0>(1, w, lane, FF * 2) + cb), [src2] "v"(wsrc<12, 0>(2, w, lane, FF * 2) + cb),
               [tr0] "v"(wtr0(3 * w, lane)), [tr1] "v"(wtr0(3 * w + 1, lane)), [tr2] "v"(wtr0(3 * w + 2, lane)),
               [b0] "v"(b0.x), [b1] "v"(b0.y), [b2] "v"(b1.x), [b3] "v"(b1.y), [b4] "v"(b2.x), [b5] "v"(b2.y),
               [ws] "s"(rws), [w0] "s"(rw), [wave] "s"(w), [ob] "s"((unsigned)OFF_H1 + c * 12288 + w * 3072), [flag] "s"((unsigned)OFF_CTL + 4 * (C_F1P + c * FS)), [stb] "s"(STB) : CLOB_MID);
}
DI void role_o(const Params& p, rsrc_t rws, int h, unsigned par, LAS char* lds, int w BSLOT) {
  const int lane = lane_id();
  const rsrc_t rw = mkrsrc(p.wo, (size_t)DM * HD * 2);
  const unsigned tab = w * 8192 + lane * 16;
  const int r0 = 4 * (lane >> 4) + ((lane >> 2) & 3);
#pragma unroll
  for (int k = 0; k < 3; ++k) {
    u32x4 a, b;
#pragma unroll
    for (int e = 0; e < 4; ++e) {
      const int i = 4 * k + e < 10 ? 4 * k + e : 9, c = i * 64 + lane, r = 8 * w + c / 80, pc = c % 80;
      a[e] = (unsigned)(r * 1280 + (((pc >> 1) ^ (r & 7)) * 32) + (pc & 1) * 16 + h * DH * DM * 2);
      b[e] = (unsigned)(r0 * 1280 + (((10 * w + i) ^ (r0 & 7)) * 32) + (lane & 3) * 8);
    }
    *(LAS u32x4*)(lds + tab + k * 1024) = a; *(LAS u32x4*)(lds + tab + 3072 + k * 1024) = b;
  }
  asm volatile("s_waitcnt lgkmcnt(0)\n s_nop 4\n" ASM_O :: [l16] "v"(lane * 16), [one] "v"(lane ? OOB : 0u), [tab] "v"(tab), [pk] "v"(w * 3072 + lane * 16),
               [ws] "s"(rws), [w0] "s"(rw), [wave] "s"(w), [ob] "s"(ubase(10 * w, h >> 3) + (h & 7) * 1024), [par] "s"(par << 13), [vb] "s"((unsigned)OFF_VX + h * 12288 + w * 3072),
               [flag] "s"((unsigned)OFF_CTL + 4 * (C_UP + h * FS)), [vflag] "s"((unsigned)OFF_CTL + 4 * (C_VP + h * FS)), [stb] "s"(STB) : CLOB_MID);
}

DI bool role_f2(const Params& p, rsrc_t rws, int c, int rr, LAS char* lds, int w BSLOT) {
  const int lane = lane_id(), tid = w * 64 + lane, g = lane >> 4;
  gu32* ctl = (gu32*)(p.ws + OFF_CTL);
  bf16x8 wf[5][12];
  const u32x2 bia = *(const u32x2*)(p.b2 + c * 80 + w * 16 + 4 * g), bib = *(const u32x2*)(p.b2 + c * 80 + 64 + 4 * g);
  {
    const rsrc_t rw = mkrsrc(p.w2, (size_t)FF * DM * 2);
    unsigned src[5];
#pragma unroll
    for (int i = 0; i < 5; ++i) { const int ch = i * 64 + lane, r = ch / 10, pc = ch % 10; src[i] = (unsigned)((384 * w + r) * 1280 + c * 160 + pc * 16); }
    LAS char* my = lds + w * 30720;
    const int r0 = 4 * g + ((lane >> 2) & 3);
#pragma unroll
    for (int half = 0; half < 2; ++half) {
#pragma unroll
      for (int st = 0; st < 6; ++st)
#pragma unroll
        for (int i = 0; i < 5; ++i) dma16(rw, my + st * 5120 + i * 1024, src[i], (unsigned)((half * 6 + st) * 32 * 1280));
      WAIT_V0();
#pragma unroll
      for (int st = 0; st < 6; ++st)
#pragma unroll
        for (int T = 0; T < 5; ++T) { LAS char* b = my + st * 5120 + r0 * 160 + T * 32 + (lane & 3) * 8; wf[T][half * 6 + st] = cat8(trread(b), trread(b + 16 * 160)); }
      WAIT_L0();
      SCHED();
    }
  }
  BAR();
  BST(1);
  u32x4 hb[2][12];
  unsigned fprog = 0;
  auto fetch = [&](int s, int hf) -> bool {
    if (fprog <= (unsigned)s) {
      if (!wait_all_ge<2>(ctl + C_F1P + 2 * w * FS, s + 1, lane)) return false;
      fprog = __all(__hip_atomic_load(ctl + C_F1P + (2 * w + (lane & 1)) * FS, RLX_AGENT) >= (unsigned)s + 7u) ? s + 7 : s + 1;
    }
#pragma unroll
    for (int i = 0; i < 12; ++i) hb[hf][i] = ld128(rws, lane * 16 + (i & 3) * 1024, (unsigned)OFF_H1 + s * 98304 + (2 * w + hf) * 12288 + (i >> 2) * 4096);
    return true;
  };
  if (!fetch(rr, 0) || !fetch(rr, 1)) { give_up(p, 0x800); return false; }
  for (int it = 0; it < 32; ++it) {
    const int s = rr + 2 * it;
    f32x4 acc[2][5];
#pragma unroll
    for (int hf = 0; hf < 2; ++hf) {
#pragma unroll
      for (int ks = 0; ks < 6; ++ks) {
        const bf16x8 b0 = pack8(hb[hf][2 * ks].x, hb[hf][2 * ks].y, hb[hf][2 * ks + 1].x, hb[hf][2 * ks + 1].y), b1 = pack8(hb[hf][2 * ks].z, hb[hf][2 * ks].w, hb[hf][2 * ks + 1].z, hb[hf][2 * ks + 1].w);
#pragma unroll
        for (int T = 0; T < 5; ++T) { const f32x4 z = {0, 0, 0, 0}; const bool f = hf == 0 && ks == 0; acc[0][T] = mf32(wf[T][hf * 6 + ks], b0, f ? z : acc[0][T]); acc[1][T] = mf32(wf[T][hf * 6 + ks], b1, f ? z : acc[1][T]); }
      }
      SCHED();
      if (hf == 0) { WAIT_V0(); BAR(); if (tid == 0) __hip_atomic_store(ctl + C_GP + (rr * 8 + c) * FS, (unsigned)it, RLX_AGENT); }
      if (it + 1 < 32) { if (!fetch(s + 2, hf)) { give_up(p, 0x800 + s); return false; } }
      SCHED();
    }
    LAS char* ex = lds + (it & 1) * 40960;
#pragma unroll
    for (int tt = 0; tt < 2; ++tt)
#pragma unroll
      for (int T = 0; T < 5; ++T) *(LAS f32x4*)(ex + ((w * 5 + T) * 2 + tt) * 1024 + lane * 16) = acc[tt][T];
    WAIT_L0(); BAR();
    for (int T = w; T < 5; T += 4) {
      const u32x2 bb = T == w ? bia : bib;
      const float bv[4] = {bf_lo(bb.x), bf_hi(bb.x), bf_lo(bb.y), bf_hi(bb.y)};
      u32x4 o;
#pragma unroll
      for (int tt = 0; tt < 2; ++tt) {
        LAS char* q = ex + (T * 2 + tt) * 1024 + lane * 16;
        const f32x4 v = (*(LAS f32x4*)q + *(LAS f32x4*)(q + 10240)) + (*(LAS f32x4*)(q + 20480) + *(LAS f32x4*)(q + 30720));
        float gq[4];
#pragma unroll
        for (int e = 0; e < 4; ++e) { const float t = rbf(rbf(v[e]) + bv[e]); gq[e] = t / (1.f + __expf(-t)); }
        o[2 * tt] = cvtpk(gq[0], gq[1]); o[2 * tt + 1] = cvtpk(gq[2], gq[3]);
      }
      st128(o, rws, lane * 16, (unsigned)OFF_G + (s * 40 + c * 5 + T) * 1024);
    }
    BST(16 + it);
  }
  WAIT_V0(); BAR();
  if (tid == 0) __hip_atomic_store(ctl + C_GP + (rr * 8 + c) * FS, 32u, RLX_AGENT);
  BST(2);
  return true;
}

DI void role_q(const Params& p, rsrc_t rws, int h, unsigned par, int w BSLOT) {
  const int lane = lane_id(), g = lane >> 4, i16 = lane & 15;
  const rsrc_t rw = mkrsrc(p.wq, (size_t)DM * HD * 2);
  const unsigned cb = h * DH * 2;
  constexpr int XO = 122880, ROW = 176;
  const unsigned km = i16 < 8 ? km_off(h, w) + (g * 8 + i16) * 16 : OOB;
  const int qi = 8 * w + (lane >> 3), kg = lane & 7;
  const int k0 = 4 * (kg >> 1) + 2 * (kg & 1);
  const int mk = (kg & 1) | ((kg & 2) << 1) | ((kg & 4) >> 1);
  const unsigned xw = XO + (w * 32 + i16) * ROW + g * 16, xm = g < 2 ? xw + 128 : LDS_DROP;
  const unsigned long long m0 = __ballot(k0 <= qi), m1 = __ballot(k0 + 1 <= qi), m2 = __ballot(k0 + 16 <= qi), m3 = __ballot(k0 + 17 <= qi);
  asm volatile("s_nop 4\n" ASM_Q :: [l16] "v"(lane * 16), [one] "v"(lane ? OOB : 0u), [src0] "v"(wsrc<12, 0>(0, w, lane, HD * 2) + cb), [src1] "v"(wsrc<12, 0>(1, w, lane, HD * 2) + cb), [src2] "v"(wsrc<12, 0>(2, w, lane, HD * 2) + cb),
               [tr0] "v"(wtr0(3 * w, lane)), [tr1] "v"(wtr0(3 * w + 1, lane)), [tr2] "v"(wtr0(3 * w + 2, lane)), [km] "v"(km), [xw] "v"(xw), [xm] "v"(xm),
               [xr] "v"(XO + qi * ROW + k0 * 4), [xr2] "v"(XO + qi * ROW + 128 + mk * 4),
               [pt] "v"((h * 2 + (qi >> 4)) * 1024 + (kg >> 1) * 256 + (qi & 15) * 16 + (kg & 1) * 8), [ptm] "v"((kg & 1) ? OOB : 65536u + ((qi >> 4) * 32 + h) * 256 + (qi & 15) * 16 + kg * 2),
               [m0] "s"(m0), [m1] "s"(m1), [m2] "s"(m2), [m3] "s"(m3),
               [ws] "s"(rws), [w0] "s"(rw), [wave] "s"(w), [ob] "s"((unsigned)OFF_KX + kx_off(h, w)), [par] "s"(par << 13), [flag] "s"((unsigned)OFF_CTL + 4 * (C_KP + h * FS)), [qflag] "s"((unsigned)OFF_CTL + 4 * (C_QP + h * FS)), [stb] "s"(STB) : CLOB_MID);
}

#define OUTST(ptr, v) (*(u32x2*)(ptr) = (v))
DI u32x2 out_fin(f32x4 c, unsigned ga, unsigned gb, u32x2 xx) {
  const unsigned sa = cvtpk(c[0], c[1]), sb = cvtpk(c[2], c[3]);
  const unsigned ma = cvtpk(bf_lo(sa) * bf_lo(ga), bf_hi(sa) * bf_hi(ga)), mb = cvtpk(bf_lo(sb) * bf_lo(gb), bf_hi(sb) * bf_hi(gb));
  u32x2 o; o.x = cvtpk(bf_lo(xx.x) + bf_lo(ma), bf_hi(xx.x) + bf_hi(ma)); o.y = cvtpk(bf_lo(xx.y) + bf_lo(mb), bf_hi(xx.y) + bf_hi(mb)); return o;
}
DI void out_last(const Params& p, rsrc_t rws, unsigned par, LAS char* lds, int w, int f BSLOT) {
  const int lane = lane_id(), g = lane >> 4, i16 = lane & 15;
  gu32* ctl = (gu32*)(p.ws + OFF_CTL);
  const int s = NS - 2 + (f >= 40), tt = f & 1, j0 = ((f % 40) >> 1) * 2, jm = j0 + (w & 1);
  if (!wait_all_ge<32>(ctl + C_UP, NS, lane) || !wait_ge(ctl + C_GP + ((s & 1) * 8 + jm / 5) * FS, 32)) { give_up(p, 0xF00); return; }
  u32x4 ub[2][8], yb[2][2], pt[10];
#pragma unroll
  for (int t = 0; t < 2; ++t)
#pragma unroll
    for (int i = 0; i < 8; ++i) ub[t][i] = ld128(rws, lane * 16 + (i & 3) * 1024, ucell(s * 40 + j0 + t, w, par) + (i >> 2) * 4096);
  const size_t xo = (size_t)(s * 32 + tt * 16 + i16) * DM + jm * 16 + 4 * g;
  const u32x2 xv = *(const u32x2*)(p.x + xo);
  const u32x2 gt = ld64(rws, lane * 16 + tt * 8, (unsigned)OFF_G + (s * 40 + jm) * 1024);
  {
    int cnt = SPIN_CAP;
    while (!__all(__hip_atomic_load(ctl + C_QP + (8 * w + (lane & 7)) * FS, RLX_AGENT) >= (unsigned)(NS - 1))) { if (--cnt <= 0) { give_up(p, 0xF01); return; } __builtin_amdgcn_s_sleep(8); }
  }
#define LST(k)
  LST(0);
  const unsigned yo = (unsigned)OFF_YM + s * (unsigned)MiB + ((((s >> 1) & 1) ^ par) << 13) + j0 * 16384 + w * 2048;
  int cnt = SPIN_CAP; bool bad;
  do {
#pragma unroll
    for (int t = 0; t < 2; ++t) { yb[t][0] = ld128(rws, lane * 16, yo + t * 16384); yb[t][1] = ld128(rws, lane * 16 + 1024, yo + t * 16384); }
    unsigned mx = umax4(umax4(umax4(umax4(0u, yb[0][0]), yb[0][1]), yb[1][0]), yb[1][1]);
    asm volatile("" : "+v"(mx)::"memory");
    bad = __any(mx == SENT); --cnt;
  } while (bad && cnt > 0);
  LST(1);
  if (LFLAG) { while (!__all(__hip_atomic_load(ctl + C_QP + (8 * w + (lane & 7)) * FS, RLX_AGENT) >= (unsigned)s + 2u)) { if (--cnt <= 0) { give_up(p, 0xF01); return; } __builtin_amdgcn_s_sleep(1); } }
  LST(2);
  if (!bad) do {
#pragma unroll
    for (int i = 0; i < 8; ++i) pt[i] = ld128(rws, lane * 16 + (i & 1) * 2048, (unsigned)OFF_PT + s * 81920 + (16 * w + tt) * 1024 + (i >> 1) * 4096);
#pragma unroll
    for (int i = 0; i < 2; ++i) pt[8 + i] = ld128(rws, lane * 16 + i * 1024, (unsigned)OFF_PT + s * 81920 + 65536 + tt * 8192 + w * 2048);
    unsigned mx = 0;
#pragma unroll
    for (int i = 0; i < 10; ++i) mx = umax4(mx, pt[i]);
    asm volatile("" : "+v"(mx)::"memory");
    bad = __any(mx == SENT); --cnt;
  } while (bad && cnt > 0);
  if (bad) { give_up(p, 0xF02); return; }
  LST(3);
  f32x4 c[2] = {{0, 0, 0, 0}, {0, 0, 0, 0}};
#pragma unroll
  for (int i = 0; i < 10; ++i)
#pragma unroll
    for (int t = 0; t < 2; ++t) c[t] = mf32(as8(i < 8 ? ub[t][i < 8 ? i : 0] : yb[t][i < 8 ? 0 : i - 8]), as8(pt[i]), c[t]);
  *(LAS f32x4*)(lds + (w * 2) * 1024 + lane * 16) = c[0]; *(LAS f32x4*)(lds + (w * 2 + 1) * 1024 + lane * 16) = c[1];
  WAIT_L0(); BAR();
  if (w < 2) {
    LAS char* q = lds + w * 1024 + lane * 16;
    const f32x4 v = (*(LAS f32x4*)q + *(LAS f32x4*)(q + 2048)) + (*(LAS f32x4*)(q + 4096) + *(LAS f32x4*)(q + 6144));
    OUTST(p.out + xo, out_fin(v, gt.x, gt.y, xv));
  }
  LST(4);
}
DI unsigned wave_min(unsigned v) {
  v = min(v, (unsigned)__builtin_amdgcn_update_dpp(0, (int)v, 0x128, 0xf, 0xf, false)); v = min(v, (unsigned)__builtin_amdgcn_update_dpp(0, (int)v, 0x124, 0xf, 0xf, false));
  v = min(v, (unsigned)__builtin_amdgcn_update_dpp(0, (int)v, 0x122, 0xf, 0xf, false)); v = min(v, (unsigned)__builtin_amdgcn_update_dpp(0, (int)v, 0x121, 0xf, 0xf, false));
  return min(min((unsigned)__builtin_amdgcn_readlane(v, 0), (unsigned)__builtin_amdgcn_readlane(v, 16)), min((unsigned)__builtin_amdgcn_readlane(v, 32), (unsigned)__builtin_amdgcn_readlane(v, 48)));
}
DI void role_out(const Params& p, rsrc_t rws, unsigned par, int grp, LAS char* lds, int w BSLOT) {
  const int lane = lane_id(), tid = w * 64 + lane, g = lane >> 4, i16 = lane & 15;
  gu32* ctl = (gu32*)(p.ws + OFF_CTL);
  constexpr int TK = 81920;
  auto ticket = [&]() { return (unsigned)__builtin_amdgcn_raw_ptr_buffer_atomic_add_i32(1, rws, tid == 0 ? 0u : OOB, (unsigned)OFF_CTL + 4 * (C_QHEAD + grp * FS), 0); };
  unsigned tk = ticket();
  unsigned uok = 0, ok = 0;
  auto look = [&]() {
    const unsigned v = __hip_atomic_load(lane < 32 ? ctl + C_QP + lane * FS : ctl + C_GP + (grp & 1) * 8 * FS + (lane & 7) * FS, RLX_AGENT);
    ok = wave_min(lane < 32 ? (v ? v - 1 : 0) : 2 * v);
    __builtin_amdgcn_fence(__ATOMIC_ACQUIRE, "wavefront");
  };
  for (int it = 0;; ++it) {
    if (tid == 0) *(LAS unsigned*)(lds + TK + (it & 1) * 16) = tk;
    WAIT_L0(); BAR();
    const int q = __builtin_amdgcn_readfirstlane(*(volatile LAS unsigned*)(lds + TK + (it & 1) * 16)), nq = grp < 2 ? 160 : 150;
    if (q >= nq) {
      const int n = grp == 0 ? 9 : grp == 1 ? 7 : 12, o = grp == 0 ? 0 : grp == 1 ? 9 : grp == 2 ? 16 : 28, e = q - nq;
      if (e < 2 * n) out_last(p, rws, par, lds, w, e < n ? o + e : 40 + o + e - n BARG2);
      BST(5); return;
    }
    const int u = ((q / 10) * 4 + grp) * 10 + q % 10;
#define UST(k)
    UST(0);
    const int s = u / 10, j = (u % 10) * 4 + w;
    for (int cnt = SPIN_CAP; uok <= (unsigned)s;) {
      uok = wave_min(__hip_atomic_load(ctl + C_UP + (lane & 31) * FS, RLX_AGENT));
      __builtin_amdgcn_fence(__ATOMIC_ACQUIRE, "wavefront");
      if (uok <= (unsigned)s) { if (--cnt <= 0) { give_up(p, 0xB00 + s); return; } __builtin_amdgcn_s_sleep(4); }
    }
    if (ok <= (unsigned)s) look();
    const size_t xo = (size_t)(s * 32 + i16) * DM + j * 16 + 4 * g;
    const unsigned yo = (unsigned)OFF_YM + s * (unsigned)MiB + ((((s >> 1) & 1) ^ par) << 13) + j * 16384;
    auto body = [&](auto WAITING) -> bool {
      u32x4 ub[32], pt[20], yb[8];
      auto uload = [&]() {
#pragma unroll
        for (int i = 0; i < 32; ++i) ub[i] = ld128(rws, lane * 16 + (i & 3) * 1024, ucell(s * 40 + j, i >> 3, par) + ((i >> 2) & 1) * 4096);
      };
      auto yload = [&]() {
#pragma unroll
        for (int i = 0; i < 8; ++i) yb[i] = ld128(rws, lane * 16 + (i & 3) * 1024, yo + (i >> 2) * 4096);
      };
      if (WAITING) {
        uload();
        for (int cnt = SPIN_CAP; ok <= (unsigned)s;) {
          if (--cnt <= 0) { give_up(p, 0xD00 + s); return false; }
          if (ok == (unsigned)s) __builtin_amdgcn_s_sleep(2); else __builtin_amdgcn_s_sleep(32);
          look();
        }
      }
      UST(2);
      tk = ticket();
#pragma unroll
      for (int i = 0; i < 20; ++i) { pt[i] = ld128(rws, lane * 16 + (i & 3) * 1024, (unsigned)OFF_PT + s * 81920 + w * 20480 + (i >> 2) * 4096); SCHED(); }
      yload();
      SCHED();
      if (!WAITING) {
#pragma unroll
        for (int i = 0; i < 20; ++i) {
          ub[i] = ld128(rws, lane * 16 + (i & 3) * 1024, ucell(s * 40 + j, i >> 3, par) + ((i >> 2) & 1) * 4096);
          *(LAS u32x4*)(lds + (w * 20 + i) * 1024 + lane * 16) = pt[i];
          SCHED();
        }
#pragma unroll
        for (int i = 20; i < 32; ++i) { ub[i] = ld128(rws, lane * 16 + (i & 3) * 1024, ucell(s * 40 + j, i >> 3, par) + ((i >> 2) & 1) * 4096); SCHED(); }
      } else {
#pragma unroll
        for (int i = 0; i < 20; ++i) *(LAS u32x4*)(lds + (w * 20 + i) * 1024 + lane * 16) = pt[i];
      }
      const u32x4 gt = ld128(rws, lane * 16, (unsigned)OFF_G + (s * 40 + j) * 1024);
      const u32x2 x0 = *(const u32x2*)(p.x + xo), x1 = *(const u32x2*)(p.x + xo + 16 * DM);
      WAIT_L0(); BAR();
      LAS char* pb = lds + lane * 16;
      f32x4 cu[2], cy[2];
      auto prod = [&](f32x4 (&c)[2], auto N, auto BASE, auto STEP, auto OFF, const u32x4* a) {
        constexpr int n = N;
        bf16x8 f[4][2];
        auto rdp = [&](int k) { f[k & 3][0] = *(LAS bf16x8*)(pb + (BASE + STEP * k) * 1024); f[k & 3][1] = *(LAS bf16x8*)(pb + (BASE + STEP * k + OFF) * 1024); };
        rdp(0); rdp(1); rdp(2);
        c[0] = c[1] = (f32x4){0, 0, 0, 0};
        static_for<n>([&](auto K) {
          constexpr int k = K;
          if (k + 3 < n) rdp(k + 3);
          c[0] = mf32(as8(a[k]), f[k & 3][0], c[0]); c[1] = mf32(as8(a[k]), f[k & 3][1], c[1]);
          SCHED();
        });
      };
      prod(cy, IC<8>{}, IC<64>{}, IC<1>{}, IC<8>{}, yb);
      prod(cu, IC<32>{}, IC<0>{}, IC<2>{}, IC<1>{}, ub);
      const float probe = (cy[0][0] + cy[0][1]) + (cy[0][2] + cy[0][3]);
      if (__any(probe != probe)) {
        for (int cnt = SPIN_CAP;;) {
          unsigned mx = 0;
#pragma unroll
          for (int i = 0; i < 8; ++i) mx = umax4(mx, yb[i]);
          asm volatile("" : "+v"(mx)::"memory");
          if (!__any(mx == SENT)) break;
          if (--cnt <= 0) { give_up(p, 0xE00 + s); return false; }
          yload();
        }
        prod(cy, IC<8>{}, IC<64>{}, IC<1>{}, IC<8>{}, yb);
      }
      OUTST(p.out + xo, out_fin(cy[0] + cu[0], gt.x, gt.y, x0));
      OUTST(p.out + xo + 16 * DM, out_fin(cy[1] + cu[1], gt.z, gt.w, x1));
      UST(4);
      return true;
    };
    if (!(ok > (unsigned)s ? body(IC<0>{}) : body(IC<1>{}))) return;
  }
}

#define LAYOUT 5
__host__ __device__ inline void slot_role(int slot, int& kind, int& h) {
  const int grp = slot >> 5, t = slot & 31;
  if (grp >= 4) { kind = t >> 3; h = (grp - 4) * 8 + (t & 7); return; }
  int f = -1;
  if (LAYOUT == 0) {
    if (t < 16) { kind = grp < 2 ? 6 : 7; h = (grp & 1) * 16 + t; } else if (t < 26) { kind = 8; h = grp * 10 + t - 16; } else f = (t - 26) * 4 + grp;
  } else if (LAYOUT == 1) {
    if (t < 16) { kind = grp < 2 ? 6 : 7; h = (grp & 1) * 16 + t; } else if (grp < 2) { kind = 8; h = grp * 16 + t - 16; } else if (t < 20) { kind = 8; h = 32 + (grp - 2) * 4 + t - 16; } else f = (t - 20) * 2 + (grp - 2);
  } else if (LAYOUT == 2) {
    if (grp < 2) { kind = 6 + grp; h = t; } else if (grp == 2) { kind = 8; h = t; } else if (t < 8) { kind = 8; h = 32 + t; } else f = t - 8;
  } else if (LAYOUT == 3) {
    if (t < 16) { kind = 6 + (t >> 3); h = grp * 8 + (t & 7); } else if (t < 26) { kind = 8; h = grp * 10 + t - 16; } else f = (t - 26) * 4 + grp;
  } else if (LAYOUT == 5) {
    const int nq = grp < 2 ? 11 : 10;
    if (t < 8) { kind = 7; h = grp * 8 + t; } else if (grp == 3) f = t - 8; else if (t < 8 + nq) { kind = 6; h = grp * 11 + t - 8; } else { kind = 8; h = grp * 13 + t - 8 - nq; }
  } else {
    if (t < 16) { kind = grp < 2 ? 6 : 7; h = (grp & 1) * 16 + t; } else if (grp >= 2) { kind = 8; h = (grp - 2) * 16 + t - 16; } else if (t < 20) { kind = 8; h = 32 + grp * 4 + t - 16; } else f = (t - 20) * 2 + grp;
  }
  if (f >= 0) { kind = f < 8 ? 4 : 5; h = f < 8 ? f : f - 8; }
}
DI char* ws_align(char* ws) { return ws + ((0 - (size_t)ws) & (4 * MiB - 1)); }

__global__ void __launch_bounds__(256, 1) k_main(Params p) {
  extern __shared__ __attribute__((aligned(16))) char smem[];
  LAS char* lds = (LAS char*)smem;
  p.ws = ws_align(p.ws);
  gu32* ctl = (gu32*)(p.ws + OFF_CTL);
  if (threadIdx.x == 0) {
    unsigned gsel = xcc_id() & 7u, slot = 0;
    for (int i = 0; i < 8; ++i) { const unsigned t = __hip_atomic_fetch_add(ctl + C_TICKET + gsel * FS, 1u, RLX_AGENT); if (t < 32u) { slot = gsel * 32 + t; break; } gsel = (gsel + 1) & 7u; }
    *(LAS unsigned*)lds = slot;
  }
  __syncthreads();
  const int slot = __builtin_amdgcn_readfirstlane(*(LAS unsigned*)lds);
  const unsigned par = __builtin_popcountll(__ballot(ctl[C_PARITY + (threadIdx.x & 15)] != 0u) & 0x7FFFull) >= 8 ? 1u : 0u;
  __syncthreads();
  const int w = __builtin_amdgcn_readfirstlane(threadIdx.x >> 6);
  int kind, h;
  slot_role(slot, kind, h);
  auto pause = [](unsigned ticks) { const unsigned long long t0 = __builtin_amdgcn_s_memrealtime(); while (__builtin_amdgcn_s_memrealtime() - t0 < ticks) __builtin_amdgcn_s_sleep(8); };
  if (kind == 0) pause(PAUSE_K);
  if (kind == 8 || (kind >= 3 && kind <= 5)) {
    if (kind != 3) pause(PAUSE_H);
    const int g = (slot >> 5) & 3, tx = threadIdx.x;
    unsigned acc = 0;
    if (kind == 8) {
      const rsrc_t rw = mkrsrc(p.wq, (size_t)DM * HD * 2);
#pragma unroll
      for (int k = 0; k < 7; ++k) { const int i = (k * 13 + h - g * 13) * 256 + tx; acc ^= __builtin_amdgcn_raw_buffer_load_b32(rw, (HROW + i / 33) * 12288 + (g * 33 + i % 33) * 128, 0, 0); }
    } else {
      const int n = kind == 3 ? 8 : 24, r = kind == 3 ? h & 7 : (slot & 31) - 8;
      const rsrc_t rv = mkrsrc(p.wv, (size_t)DM * HD * 2), ro = mkrsrc(p.wo, (size_t)DM * HD * 2);
#pragma unroll
      for (int k = 0; k < 6; ++k) { const int i = (k * n + r) * 256 + tx; acc ^= __builtin_amdgcn_raw_buffer_load_b32(rv, (192 + i / 24) * 12288 + (g * 24 + i % 24) * 128, 0, 0); }
#pragma unroll
      for (int k = 0; k < HWO; ++k) { const int i = k * 256 + tx; acc ^= __builtin_amdgcn_raw_buffer_load_b32(ro, r < 8 ? (g * 8 + r + 1) * 245760 - (i + 1) * 128 : OOB, 0, 0); }
    }
    asm volatile("" :: "v"(acc));
  }
  if (kind >= 6) {
    if (kind == 6) role_qk(p, h, par, lds, w); else if (kind == 7) role_vo(p, h, par, lds, w); else role_r(p, h, par, lds, w);
    return;
  }
  const rsrc_t rws = mkrsrc(p.ws, WS_SPAN);
  if (kind >= 3) pause(DEFER_TICKS);
  if (kind == 3) {
    unsigned acc = 0;
#pragma unroll
    for (int i = 0; i < 10; ++i) acc ^= __builtin_amdgcn_raw_buffer_load_b32(rws, ((i * 8 + (h & 7)) * 256 + threadIdx.x) * 128, (unsigned)OFF_XF, 0);
    asm volatile("" :: "v"(acc));
  }
  if (kind == 3) { role_q(p, rws, h, par, w BARG); return; }
  if (kind == 5) { role_f2(p, rws, h & 7, h >> 3, lds, w BARG); return; }
  if (kind == 4) { role_f1(p, rws, h, w BARG); return; }
  if (kind == 2) role_o(p, rws, h, par, lds, w BARG);
  else role_kv(p, rws, kind == 0, h, par, w BARG);
  role_out(p, rws, par, (slot >> 5) & 3, lds, w BARG);
}

__global__ void __launch_bounds__(256) k_pre(Params p) {
  p.ws = ws_align(p.ws);
  const int b = blockIdx.x, tid = threadIdx.x, lane = tid & 63;
  if (b < 128) {
    const int tok = (tid >> 6) * 4 + (lane >> 4), c = lane & 15;
    const uint16_t* xr = p.x + (size_t)(b * 16 + tok) * DM;
    u32x4 v[5]; float sum = 0.f;
#pragma unroll
    for (int i = 0; i < 5; ++i) { v[i] = *(const u32x4*)(xr + (c + 16 * i) * 8);
#pragma unroll
      for (int e = 0; e < 4; ++e) sum += bf_lo(v[i][e]) + bf_hi(v[i][e]); }
    sum += dppf<0x128>(sum); sum += dppf<0x124>(sum); sum += dppf<0x122>(sum); sum += dppf<0x121>(sum);
    const float mean = sum * (1.f / DM); float sq = 0.f;
#pragma unroll
    for (int i = 0; i < 5; ++i)
#pragma unroll
      for (int e = 0; e < 4; ++e) { const float a = bf_lo(v[i][e]) - mean, d = bf_hi(v[i][e]) - mean; sq += a * a + d * d; }
    sq += dppf<0x128>(sq); sq += dppf<0x124>(sq); sq += dppf<0x122>(sq); sq += dppf<0x121>(sq);
    const float mb = rbf(mean), sd = rbf(sqrtf(rbf(rbf(sq * (1.f / DM)) + 1e-5f))), rs = 1.f / sd;
    auto dv = [&](float d) { const float q = d * rs; return fmaf(fmaf(-q, sd, d), rs, q); };
#pragma unroll
    for (int i = 0; i < 5; ++i) {
      u32x4 o;
#pragma unroll
      for (int e = 0; e < 4; ++e) { const unsigned d = cvtpk(bf_lo(v[i][e]) - mb, bf_hi(v[i][e]) - mb); o[e] = cvtpk(dv(bf_lo(d)), dv(bf_hi(d))); }
      const int ch = c + 16 * i;
      *(u32x4*)(p.ws + OFF_XF + ((size_t)(b * 20 + (ch >> 2)) * 64 + (ch & 3) * 16 + tok) * 16) = o;
    }
  } else if (b < 241) {
    const rsrc_t rws = mkrsrc(p.ws, WS_SPAN);
    const u32x4 z = {SENT, SENT, SENT, SENT}, z0 = {0, 0, 0, 0};
    constexpr int N0 = 5 * 4096, N1 = N0 + 4 * 40960, N2 = N1 + 4 * 16384, N3 = N2 + 10240, N4 = N3 + 2048, N5 = N4 + C_WORDS / 4;
#pragma unroll
    for (int k = 0; k < (N5 + 113 * 256 - 1) / (113 * 256); ++k) {
      const int i = (k * 113 + b - 128) * 256 + tid, y = (i - N0) / 40960;
      const unsigned o = i < N0 ? OFF_SLOT + i * 16u : i < N1 ? OFF_YM + y * MiB + (i - N0 - y * 40960) * 16u : i < N2 ? OFF_KM + (i - N1) * 16u : i < N3 ? OFF_PT + 62 * 81920 + (i - N2) * 16u
                       : i < N4 ? OFF_DB + (i - N3) * 16u : (i < N5 && (i - N4) >> 4 != C_PARITY / 64) ? OFF_CTL + (i - N4) * 16u : OOB;
      st128(i < N3 ? z : z0, rws, o, 0);
    }
  } else {
    if (tid < 64) {
      const rsrc_t rs = mkrsrc(p.ws + OFF_PROBE, 65536);
      unsigned best[2] = {~0u, ~0u};
      st32(0u, rs, (lane >> 5) * 8192 + (b - 241) * 256 + (lane & 31) * 4, 0); WAIT_V0();
#pragma unroll 1
      for (int r = 0; r < PROBE_N; ++r)
#pragma unroll
        for (int q = 0; q < 2; ++q) {
          const unsigned long long t0 = __builtin_amdgcn_s_memtime(); WAIT_L0();
          st32(0u, rs, q * 8192 + (b - 241) * 256 + (lane & 31) * 4, 0); WAIT_V0();
          const unsigned long long t1 = __builtin_amdgcn_s_memtime(); WAIT_L0();
          const unsigned d = (unsigned)(t1 - t0); best[q] = d < best[q] ? d : best[q];
        }
      if (tid == 0) ((unsigned*)(p.ws + OFF_CTL))[C_PARITY + b - 241] = (best[1] < best[0] ? 1u : 0u) ^ ((xcc_id() >> 2) & 1u) ^ (unsigned)((OFF_PROBE >> 21) & 1u);
    }
  }
}

extern "C" void kernel_launch(void* const* d_in, const int* in_sizes, int n_in, void* d_out, int out_size, void* d_ws, size_t ws_size, hipStream_t stream) {
  Params p;
  p.x = (const uint16_t*)d_in[0]; p.mem0 = (const uint16_t*)d_in[1]; p.wq = (const uint16_t*)d_in[2]; p.wk = (const uint16_t*)d_in[3];
  p.wv = (const uint16_t*)d_in[4]; p.wo = (const uint16_t*)d_in[5]; p.w1 = (const uint16_t*)d_in[8]; p.b1 = (const uint16_t*)d_in[9];
  p.w2 = (const uint16_t*)d_in[10]; p.b2 = (const uint16_t*)d_in[11]; p.out = (uint16_t*)d_out; p.ws = (char*)d_ws;
  (void)hipFuncSetAttribute((const void*)k_main, hipFuncAttributeMaxDynamicSharedMemorySize, LDS_BYTES);
  hipLaunchKernelGGL(k_pre, dim3(256), dim3(256), 0, stream, p);
  hipLaunchKernelGGL(k_main, dim3(256), dim3(256), LDS_BYTES, stream, p);
}
```
